```python
import torch

def _softmax(x, axis=-1):
    return torch.softmax(x, dim=axis)
def _gelu(x):
    return torch.nn.functional.gelu(x, approximate="tanh")
def _silu(x):
    return torch.nn.functional.silu(x)
def _sigmoid(x):
    return torch.sigmoid(x)
def _layernorm(x, axis=-1, eps=1e-5):
    m = x.mean(dim=axis, keepdim=True)
    v = x.var(dim=axis, keepdim=True, unbiased=False)
    return (x - m) / torch.sqrt(v + eps)
def _scatter_add(tgt, idx, src, axis=0):
    return tgt.clone().index_add_(axis, idx, src)
def _segment_sum(src, seg, n):
    out = torch.zeros((n,) + src.shape[1:], dtype=src.dtype)
    return out.index_add_(0, seg, src)
def _pad(x, pw):
    flat = tuple(v for pair in reversed(pw) for v in pair)
    return torch.nn.functional.pad(x, flat)

kernel_name = 'standalone__weight_only_int_gptq__prompt'

def setup_inputs():

    _w = (lambda a: (lambda t: t.to(torch.bfloat16) if t.is_floating_point() else t)(torch.as_tensor(a)))
    B, S, D, F = 1, 2048, 1024, 3584
    bits = max(2, min(4, 4))
    pf = 32 // bits
    G = 64 if 64 > 0 else D
    G = max(pf, (G // pf) * pf)
    D = max(G, (D // G) * G)
    ng = D // G
    qmax = 1 << bits
    qw = torch.randint(0, qmax, (D, F)).to(torch.int64)
    packed = torch.zeros((D // pf, F), dtype=torch.int64)
    for i in range(pf):
        packed |= (qw[i::pf] << (i * bits))
    packed = packed.to(torch.int32)
    return {
        "x": _w((lambda *s: torch.randn(*s, dtype=torch.bfloat16) * 0.05)(B, S, D)),
        "qw_packed": _w(packed),
        "scales": _w(torch.abs((lambda *s: torch.randn(*s, dtype=torch.bfloat16) * 0.05)(ng, F)).to(float) * 0.1 + 0.01),
        "zeros": _w(torch.randint(0, qmax, (ng, F)).to(float)),
        "bits": _w((lambda _x: torch.as_tensor(_x, dtype=torch.int32))(bits)),
    }

def reference(x, qw_packed, scales, zeros, bits):
    b = int(bits)
    pf = 32 // b
    mask = (1 << b) - 1
    P, F = qw_packed.shape
    D = P * pf
    ng = scales.shape[0]
    G = D // ng
    shifts = (lambda x, dt: x.to(dt))(torch.arange(pf) * b, qw_packed.dtype)
    qw = ((qw_packed[:, None, :] >> shifts[None, :, None]) & mask).reshape(D, F)
    s = (lambda x, n, axis: torch.repeat_interleave(x, n, dim=axis))(scales, G, axis=0)
    z = (lambda x, n, axis: torch.repeat_interleave(x, n, dim=axis))(zeros, G, axis=0)
    w = ((lambda x, dt: x.to(dt))(qw, x.dtype) - z) * s
    return torch.einsum("bsd,df->bsf", x, w)
```

```cpp
#include <hip/hip_runtime.h>

__global__ __launch_bounds__(512) void gptq_w4a16_kernel(const void* x, const void* qw, const void* scales,
                                                         const void* zeros, void* out) {
  __shared__ __attribute__((aligned(1024))) char smem[163840];
  unsigned tid = threadIdx.x;
  asm volatile(
      "s_load_dwordx8 s[16:23], s[0:1], 0\n\t"
      "s_load_dwordx2 s[24:25], s[0:1], 32\n\t"
      "s_getpc_b64 s[56:57]\n\t"
      "LPC_%=:\n\t"
      "s_mov_b32 s58, LEND_%=-LPC_%=-4\n\t"
      "s_bfe_u32 s59, s2, 0x30003\n\t"
      "s_lshl_b32 s59, s59, 13\n\t"
      "v_lshrrev_b32 v2, 3, v0\n\t"
      "v_and_b32 v2, 56, v2\n\t"
      "v_and_or_b32 v2, v0, 7, v2\n\t"
      "v_lshl_add_u32 v2, v2, 7, s59\n\t"
      "v_min_u32 v2, s58, v2\n\t"
      "s_mov_b64 exec, 0xff\n\t"
      "global_load_dword v255, v2, s[56:57]\n\t"
      "s_mov_b64 exec, -1\n\t"
      "v_lshrrev_b32 v2, 6, v0\n\t"
      "v_and_b32 v1, 63, v0\n\t"
      "s_mov_b32 s53, %3\n\t"
      "v_readfirstlane_b32 s48, v2\n\t"
      "s_and_b32 s56, s2, 7\n\t"
      "s_lshr_b32 s57, s2, 3\n\t"
      "s_and_b32 s58, s56, 1\n\t"
      "s_add_u32 s58, s58, 31\n\t"
      "s_cmp_ge_u32 s57, s58\n\t"
      "s_cbranch_scc1 LEND_%=\n\t"
      "s_and_b32 s59, s56, 3\n\t"
      "s_lshl_b32 s58, s59, 1\n\t"
      "s_mul_i32 s59, s59, 7\n\t"
      "s_lshr_b32 s59, s59, 1\n\t"
      "s_sub_u32 s59, 28, s59\n\t"
      "s_cmp_ge_u32 s57, 14\n\t"
      "s_cselect_b32 s60, 14, 0\n\t"
      "s_addc_u32 s58, s58, 0\n\t"
      "s_cmp_ge_u32 s57, 28\n\t"
      "s_cselect_b32 s60, s59, s60\n\t"
      "s_cselect_b32 s58, 8, s58\n\t"
      "s_sub_u32 s57, s57, s60\n\t"
      "s_mul_i32 s59, s58, 14\n\t"
      "s_add_u32 s55, s57, s58\n\t"
      "s_and_b32 s55, s55, 1\n\t"
      "s_lshr_b32 s56, s56, 2\n\t"
      "s_mul_i32 s56, s56, 14\n\t"
      "s_add_u32 s56, s56, s57\n\t"
      "s_lshl_b32 s52, s56, 7\n\t"
      "s_bfe_u32 s57, s57, 0x10001\n\t"
      "s_cmp_gt_u32 s58, s57\n\t"
      "s_addc_u32 s59, s59, 0\n\t"
      "s_add_u32 s60, s57, 4\n\t"
      "s_cmp_gt_u32 s58, s60\n\t"
      "s_addc_u32 s59, s59, 0\n\t"
      "s_lshl_b32 s51, s59, 4\n\t"
      "s_mov_b32 s54, 14\n\t"
      "s_cmp_eq_u32 s58, s57\n\t"
      "s_cmov_b32 s54, 15\n\t"
      "s_cmp_eq_u32 s58, s60\n\t"
      "s_cmov_b32 s54, 15\n\t"
      "s_cmp_lt_u32 s48, 4\n\t"
      "s_cbranch_scc1 LCONS_%=\n\t"
      "s_and_b32 s48, s48, 3\n\t"
      "s_lshl_b32 s56, s48, 3\n\t"
      "s_add_u32 s56, s56, s51\n\t"
      "s_lshl_b32 s56, s56, 11\n\t"
      "s_lshl_b32 s57, s48, 2\n\t"
      "s_and_b32 s57, s57, 4\n\t"
      "v_lshrrev_b32 v2, 4, v1\n\t"
      "v_and_b32 v3, 7, v1\n\t"
      "v_xor_b32 v2, v2, v3\n\t"
      "v_xor_b32 v2, s57, v2\n\t"
      "v_lshlrev_b32 v2, 4, v2\n\t"
      "v_lshrrev_b32 v3, 3, v1\n\t"
      "v_lshl_add_u32 v2, v3, 11, v2\n\t"
      "s_lshl_b32 s57, s55, 7\n\t"
      "s_add_u32 s58, s56, s57\n\t"
      "v_add_u32 v100, s58, v2\n\t"
      "s_lshl_b32 s49, s48, 10\n\t"
      "s_add_u32 s49, s49, s53\n\t"
      "s_mov_b32 s30, 4194304\n\t"
      "s_mov_b32 s31, 0x20000\n\t"
      "s_waitcnt lgkmcnt(0)\n\t"
      "s_mov_b32 s28, s16\n\t"
      "s_and_b32 s29, s17, 0xffff\n\t"
      "s_add_u32 m0, s49, 0\n\t"
      "s_mov_b32 s50, 0\n\t"
      "buffer_load_dwordx4 v100, s[28:31], s50 offen  lds\n\t"
      "s_add_u32 m0, s49, 4096\n\t"
      "s_mov_b32 s50, 65536\n\t"
      "buffer_load_dwordx4 v100, s[28:31], s50 offen  lds\n\t"
      "s_add_u32 m0, s49, 8192\n\t"
      "s_mov_b32 s50, 131072\n\t"
      "buffer_load_dwordx4 v100, s[28:31], s50 offen  lds\n\t"
      "s_add_u32 m0, s49, 12288\n\t"
      "s_mov_b32 s50, 196608\n\t"
      "buffer_load_dwordx4 v100, s[28:31], s50 offen  lds\n\t"
      "s_add_u32 m0, s49, 16384\n\t"
      "s_mov_b32 s50, 262144\n\t"
      "buffer_load_dwordx4 v100, s[28:31], s50 offen  lds\n\t"
      "s_add_u32 m0, s49, 20480\n\t"
      "s_mov_b32 s50, 327680\n\t"
      "buffer_load_dwordx4 v100, s[28:31], s50 offen  lds\n\t"
      "s_cmp_eq_u32 s54, 15\n\t"
      "s_cbranch_scc1 LL15_%=\n\t"
      "s_branch LL14_%=\n\t"
      "LCONS_%=:\n\t"
      "s_lshl_b32 s56, s48, 5\n\t"
      "s_add_u32 s56, s56, s52\n\t"
      "s_lshl_b32 s57, s56, 1\n\t"
      "s_lshl_b32 s58, s56, 2\n\t"
      "v_lshrrev_b32 v13, 4, v1\n\t"
      "v_and_b32 v14, 15, v1\n\t"
      "v_lshl_add_u32 v3, v14, 2, s57\n\t"
      "v_mul_u32_u24 v10, 14336, v13\n\t"
      "v_lshlrev_b32 v11, 3, v14\n\t"
      "v_add3_u32 v10, v10, s58, v11\n\t"
      "s_mul_i32 s58, s55, 114688\n\t"
      "s_add_u32 s59, s58, 57344\n\t"
      "s_xor_b32 s62, s55, 1\n\t"
      "s_mul_i32 s60, s62, 114688\n\t"
      "s_add_u32 s61, s60, 57344\n\t"
      "s_mul_i32 s63, s55, 7168\n\t"
      "s_mul_i32 s62, s62, 7168\n\t"
      "s_mov_b32 s30, 114688\n\t"
      "s_mov_b32 s31, 0x20000\n\t"
      "s_mov_b32 s38, 114688\n\t"
      "s_mov_b32 s39, 0x20000\n\t"
      "s_mov_b32 s42, 1835008\n\t"
      "s_mov_b32 s43, 0x20000\n\t"
      "s_waitcnt lgkmcnt(0)\n\t"
      "s_mov_b32 s28, s20\n\t"
      "s_and_b32 s29, s21, 0xffff\n\t"
      "s_mov_b32 s36, s22\n\t"
      "s_and_b32 s37, s23, 0xffff\n\t"
      "s_mov_b32 s40, s18\n\t"
      "s_and_b32 s41, s19, 0xffff\n\t"
      "buffer_load_dword v26, v3, s[28:31], s63 offen\n\t"
      "buffer_load_dword v27, v3, s[36:39], s63 offen\n\t"
      "buffer_load_dwordx2 v[16:17], v10, s[40:43], s58 offen\n\t"
      "buffer_load_dwordx2 v[18:19], v10, s[40:43], s59 offen\n\t"
      "buffer_load_dword v76, v3, s[28:31], s62 offen\n\t"
      "buffer_load_dword v77, v3, s[36:39], s62 offen\n\t"
      "buffer_load_dwordx2 v[72:73], v10, s[40:43], s60 offen\n\t"
      "buffer_load_dwordx2 v[74:75], v10, s[40:43], s61 offen\n\t"
      "s_lshl_b32 s59, s48, 6\n\t"
      "s_add_u32 s59, s59, s53\n\t"
      "s_add_u32 s59, s59, 155648\n\t"
      "v_lshl_add_u32 v11, v14, 2, s59\n\t"
      "v_lshrrev_b32 v3, 1, v14\n\t"
      "v_xor_b32 v3, v3, v13\n\t"
      "v_lshlrev_b32 v3, 4, v3\n\t"
      "v_lshl_add_u32 v3, v14, 7, v3\n\t"
      "v_add_u32 v6, s53, v3\n\t"
      "v_xor_b32 v3, 64, v3\n\t"
      "v_add_u32 v7, s53, v3\n\t"
      "v_add_u32 v8, 0x10000, v6\n\t"
      "v_add_u32 v9, 0x10000, v7\n\t"
      "s_lshl_b32 s60, s48, 7\n\t"
      "v_lshl_add_u32 v3, v14, 3, s60\n\t"
      "v_and_b32 v2, 16, v1\n\t"
      "v_lshlrev_b32 v2, 3, v2\n\t"
      "v_xor_b32 v3, v3, v2\n\t"
      "v_lshl_add_u32 v3, v13, 9, v3\n\t"
      "s_add_u32 s60, s53, 131072\n\t"
      "v_add_u32 v10, s60, v3\n\t"
      "s_lshl_b32 s60, s48, 2\n\t"
      "v_add_u32 v3, s60, v13\n\t"
      "v_and_b32 v2, 7, v14\n\t"
      "v_xor_b32 v3, v3, v2\n\t"
      "v_lshlrev_b32 v3, 4, v3\n\t"
      "v_lshl_add_u32 v3, v14, 8, v3\n\t"
      "v_add_u32 v12, s53, v3\n\t"
      "v_add_u32 v2, 0x10000, v12\n\t"
      "v_add_u32 v3, 0x20000, v12\n\t"
      "s_mov_b32 s66, 0x4b800000\n\t"
      "s_mov_b32 s67, 0x49800000\n\t"
      "v_mov_b32 v13, 0x000f000f\n\t"
      "v_mov_b32 v14, 0x00f000f0\n\t"
      "s_getpc_b64 s[56:57]\n\t"
      "LHPC_%=:\n\t"
      "s_cmp_eq_u32 s54, 15\n\t"
      "s_cselect_b32 s58, LC15_%=-LC14_%=, 0\n\t"
      "s_lshl_b32 s59, s48, 12\n\t"
      "s_add_u32 s58, s58, s59\n\t"
      "s_add_u32 s58, s58, LC14_%=-LHPC_%=\n\t"
      "s_add_u32 s56, s56, s58\n\t"
      "s_addc_u32 s57, s57, 0\n\t"
      "s_mov_b64 exec, 0\n\t"
      "s_setpc_b64 s[56:57]\n\t"
      "LHRET_%=:\n\t"
      "s_mov_b64 exec, -1\n\t"
      "s_waitcnt vmcnt(6)\n\t"
      "v_lshlrev_b32 v28, 16, v26\n\t"
      "v_and_b32 v29, 0xffff0000, v26\n\t"
      "v_lshlrev_b32 v30, 16, v27\n\t"
      "v_and_b32 v31, 0xffff0000, v27\n\t"
      "v_mul_f32 v20, 0x4b800000, v28\n\t"
      "v_mul_f32 v24, 0x49800000, v28\n\t"
      "v_mul_f32 v22, v28, v30\n\t"
      "v_mul_f32 v21, 0x4b800000, v29\n\t"
      "v_mul_f32 v25, 0x49800000, v29\n\t"
      "v_mul_f32 v23, v29, v31\n\t"
      "s_waitcnt vmcnt(5)\n\t"
      "v_lshrrev_b32 v31, 8, v16\n\t"
      "v_and_b32 v28, v13, v16\n\t"
      "v_and_b32 v29, v14, v16\n\t"
      "v_and_b32 v30, v13, v31\n\t"
      "v_and_b32 v31, v14, v31\n\t"
      "v_fma_mix_f32 v32, v28, v20, -v22 op_sel:[0,0,0] op_sel_hi:[1,0,0]\n\t"
      "v_fma_mix_f32 v33, v29, v24, -v22 op_sel:[0,0,0] op_sel_hi:[1,0,0]\n\t"
      "v_fma_mix_f32 v34, v30, v20, -v22 op_sel:[0,0,0] op_sel_hi:[1,0,0]\n\t"
      "v_fma_mix_f32 v35, v31, v24, -v22 op_sel:[0,0,0] op_sel_hi:[1,0,0]\n\t"
      "v_fma_mix_f32 v36, v28, v20, -v22 op_sel:[1,0,0] op_sel_hi:[1,0,0]\n\t"
      "v_fma_mix_f32 v37, v29, v24, -v22 op_sel:[1,0,0] op_sel_hi:[1,0,0]\n\t"
      "v_cvt_pk_bf16_f32 v40, v32, v33\n\t"
      "v_cvt_pk_bf16_f32 v41, v34, v35\n\t"
      "v_fma_mix_f32 v38, v30, v20, -v22 op_sel:[1,0,0] op_sel_hi:[1,0,0]\n\t"
      "v_fma_mix_f32 v39, v31, v24, -v22 op_sel:[1,0,0] op_sel_hi:[1,0,0]\n\t"
      "v_cvt_pk_bf16_f32 v42, v36, v37\n\t"
      "v_cvt_pk_bf16_f32 v43, v38, v39\n\t"
      "v_lshrrev_b32 v31, 8, v17\n\t"
      "v_and_b32 v28, v13, v17\n\t"
      "v_and_b32 v29, v14, v17\n\t"
      "v_and_b32 v30, v13, v31\n\t"
      "v_and_b32 v31, v14, v31\n\t"
      "v_fma_mix_f32 v32, v28, v21, -v23 op_sel:[0,0,0] op_sel_hi:[1,0,0]\n\t"
      "v_fma_mix_f32 v33, v29, v25, -v23 op_sel:[0,0,0] op_sel_hi:[1,0,0]\n\t"
      "v_fma_mix_f32 v34, v30, v21, -v23 op_sel:[0,0,0] op_sel_hi:[1,0,0]\n\t"
      "v_fma_mix_f32 v35, v31, v25, -v23 op_sel:[0,0,0] op_sel_hi:[1,0,0]\n\t"
      "v_fma_mix_f32 v36, v28, v21, -v23 op_sel:[1,0,0] op_sel_hi:[1,0,0]\n\t"
      "v_fma_mix_f32 v37, v29, v25, -v23 op_sel:[1,0,0] op_sel_hi:[1,0,0]\n\t"
      "v_cvt_pk_bf16_f32 v44, v32, v33\n\t"
      "v_cvt_pk_bf16_f32 v45, v34, v35\n\t"
      "v_fma_mix_f32 v38, v30, v21, -v23 op_sel:[1,0,0] op_sel_hi:[1,0,0]\n\t"
      "v_fma_mix_f32 v39, v31, v25, -v23 op_sel:[1,0,0] op_sel_hi:[1,0,0]\n\t"
      "v_cvt_pk_bf16_f32 v46, v36, v37\n\t"
      "v_cvt_pk_bf16_f32 v47, v38, v39\n\t"
      "s_waitcnt vmcnt(4)\n\t"
      "v_lshrrev_b32 v31, 8, v18\n\t"
      "v_and_b32 v28, v13, v18\n\t"
      "v_and_b32 v29, v14, v18\n\t"
      "v_and_b32 v30, v13, v31\n\t"
      "v_and_b32 v31, v14, v31\n\t"
      "v_fma_mix_f32 v32, v28, v20, -v22 op_sel:[0,0,0] op_sel_hi:[1,0,0]\n\t"
      "v_fma_mix_f32 v33, v29, v24, -v22 op_sel:[0,0,0] op_sel_hi:[1,0,0]\n\t"
      "v_fma_mix_f32 v34, v30, v20, -v22 op_sel:[0,0,0] op_sel_hi:[1,0,0]\n\t"
      "v_fma_mix_f32 v35, v31, v24, -v22 op_sel:[0,0,0] op_sel_hi:[1,0,0]\n\t"
      "v_fma_mix_f32 v36, v28, v20, -v22 op_sel:[1,0,0] op_sel_hi:[1,0,0]\n\t"
      "v_fma_mix_f32 v37, v29, v24, -v22 op_sel:[1,0,0] op_sel_hi:[1,0,0]\n\t"
      "v_cvt_pk_bf16_f32 v48, v32, v33\n\t"
      "v_cvt_pk_bf16_f32 v49, v34, v35\n\t"
      "v_fma_mix_f32 v38, v30, v20, -v22 op_sel:[1,0,0] op_sel_hi:[1,0,0]\n\t"
      "v_fma_mix_f32 v39, v31, v24, -v22 op_sel:[1,0,0] op_sel_hi:[1,0,0]\n\t"
      "v_cvt_pk_bf16_f32 v50, v36, v37\n\t"
      "v_cvt_pk_bf16_f32 v51, v38, v39\n\t"
      "v_lshrrev_b32 v31, 8, v19\n\t"
      "v_and_b32 v28, v13, v19\n\t"
      "v_and_b32 v29, v14, v19\n\t"
      "v_and_b32 v30, v13, v31\n\t"
      "v_and_b32 v31, v14, v31\n\t"
      "v_fma_mix_f32 v32, v28, v21, -v23 op_sel:[0,0,0] op_sel_hi:[1,0,0]\n\t"
      "v_fma_mix_f32 v33, v29, v25, -v23 op_sel:[0,0,0] op_sel_hi:[1,0,0]\n\t"
      "v_fma_mix_f32 v34, v30, v21, -v23 op_sel:[0,0,0] op_sel_hi:[1,0,0]\n\t"
      "v_fma_mix_f32 v35, v31, v25, -v23 op_sel:[0,0,0] op_sel_hi:[1,0,0]\n\t"
      "v_fma_mix_f32 v36, v28, v21, -v23 op_sel:[1,0,0] op_sel_hi:[1,0,0]\n\t"
      "v_fma_mix_f32 v37, v29, v25, -v23 op_sel:[1,0,0] op_sel_hi:[1,0,0]\n\t"
      "v_cvt_pk_bf16_f32 v52, v32, v33\n\t"
      "v_cvt_pk_bf16_f32 v53, v34, v35\n\t"
      "v_fma_mix_f32 v38, v30, v21, -v23 op_sel:[1,0,0] op_sel_hi:[1,0,0]\n\t"
      "v_fma_mix_f32 v39, v31, v25, -v23 op_sel:[1,0,0] op_sel_hi:[1,0,0]\n\t"
      "v_cvt_pk_bf16_f32 v54, v36, v37\n\t"
      "v_cvt_pk_bf16_f32 v55, v38, v39\n\t"
      "s_waitcnt vmcnt(0)\n\t"
      "s_cmp_eq_u32 s54, 15\n\t"
      "s_cbranch_scc1 LC15_%=\n\t"
      "s_branch LC14_%=\n\t"
      "LL14_%=:\n\t"
      "s_mul_i32 s56, s48, 28672\n\t"
      "s_lshl_b32 s57, s52, 2\n\t"
      "s_add_u32 s56, s56, s57\n\t"
      "v_lshrrev_b32 v2, 5, v1\n\t"
      "v_mul_u32_u24 v3, 14336, v2\n\t"
      "v_lshlrev_b32 v2, 3, v2\n\t"
      "v_and_b32 v4, 31, v1\n\t"
      "v_xor_b32 v2, v2, v4\n\t"
      "v_lshlrev_b32 v2, 4, v2\n\t"
      "v_add3_u32 v2, v3, s56, v2\n\t"
      "s_xor_b32 s56, s55, 0\n\t"
      "s_mul_i32 s56, s56, 114688\n\t"
      "v_add_u32 v108, s56, v2\n\t"
      "s_xor_b32 s56, s55, 1\n\t"
      "s_mul_i32 s56, s56, 114688\n\t"
      "v_add_u32 v109, s56, v2\n\t"
      "s_xor_b32 s56, s55, 2\n\t"
      "s_mul_i32 s56, s56, 114688\n\t"
      "v_add_u32 v110, s56, v2\n\t"
      "s_xor_b32 s56, s55, 3\n\t"
      "s_mul_i32 s56, s56, 114688\n\t"
      "v_add_u32 v111, s56, v2\n\t"
      "s_mov_b32 s36, s18\n\t"
      "s_and_b32 s37, s19, 0xffff\n\t"
      "s_mov_b32 s38, 1835008\n\t"
      "s_mov_b32 s39, 0x20000\n\t"
      "s_add_u32 m0, s49, 139264\n\t"
      "s_mov_b32 s50, 0\n\t"
      "buffer_load_dwordx4 v110, s[36:39], s50 offen  lds\n\t"
      "s_add_u32 m0, s49, 24576\n\t"
      "s_mov_b32 s50, 393216\n\t"
      "buffer_load_dwordx4 v100, s[28:31], s50 offen  lds\n\t"
      "v_xor_b32 v101, 128, v100\n\t"
      "v_xor_b32 v102, 256, v100\n\t"
      "v_xor_b32 v103, 384, v100\n\t"
      "s_cmp_lt_u32 s48, 2\n\t"
      "s_cselect_b32 s56, 0, 0x40000000\n\t"
      "v_add_u32 v104, s56, v100\n\t"
      "v_add_u32 v105, s56, v101\n\t"
      "v_add_u32 v106, s56, v102\n\t"
      "v_add_u32 v107, s56, v103\n\t"
      "s_lshl_b32 s56, s48, 2\n\t"
      "v_lshrrev_b32 v5, 4, v1\n\t"
      "v_add_u32 v5, s56, v5\n\t"
      "v_xor_b32 v5, s55, v5\n\t"
      "v_mul_u32_u24 v5, 7168, v5\n\t"
      "v_and_b32 v3, 15, v1\n\t"
      "v_lshlrev_b32 v3, 4, v3\n\t"
      "s_lshl_b32 s56, s52, 1\n\t"
      "v_add3_u32 v5, v5, s56, v3\n\t"
      "s_add_u32 m0, s49, 155648\n\t"
      "s_mov_b32 s40, s20\n\t"
      "s_and_b32 s41, s21, 0xffff\n\t"
      "s_mov_b32 s42, 114688\n\t"
      "s_mov_b32 s43, 0x20000\n\t"
      "buffer_load_dwordx4 v5, s[40:43], 0 offen lds\n\t"
      "s_waitcnt vmcnt(7)\n\t"
      "s_barrier\n\t"
      "s_add_u32 m0, s49, 159744\n\t"
      "s_mov_b32 s44, s22\n\t"
      "s_and_b32 s45, s23, 0xffff\n\t"
      "s_mov_b32 s46, 114688\n\t"
      "s_mov_b32 s47, 0x20000\n\t"
      "buffer_load_dwordx4 v5, s[44:47], 0 offen lds\n\t"
      "s_add_u32 m0, s49, 28672\n\t"
      "s_mov_b32 s50, 0\n\t"
      "buffer_load_dwordx4 v101, s[28:31], s50 offen  lds\n\t"
      "s_waitcnt vmcnt(7)\n\t"
      "s_barrier\n\t"
      "s_add_u32 m0, s49, 32768\n\t"
      "s_mov_b32 s50, 65536\n\t"
      "buffer_load_dwordx4 v101, s[28:31], s50 offen  lds\n\t"
      "s_add_u32 m0, s49, 36864\n\t"
      "s_mov_b32 s50, 131072\n\t"
      "buffer_load_dwordx4 v101, s[28:31], s50 offen  lds\n\t"
      "s_add_u32 m0, s49, 40960\n\t"
      "s_mov_b32 s50, 196608\n\t"
      "buffer_load_dwordx4 v101, s[28:31], s50 offen  lds\n\t"
      "s_waitcnt vmcnt(7)\n\t"
      "s_barrier\n\t"
      "s_add_u32 m0, s49, 45056\n\t"
      "s_mov_b32 s50, 262144\n\t"
      "buffer_load_dwordx4 v101, s[28:31], s50 offen  lds\n\t"
      "s_add_u32 m0, s49, 49152\n\t"
      "s_mov_b32 s50, 327680\n\t"
      "buffer_load_dwordx4 v101, s[28:31], s50 offen  lds\n\t"
      "s_add_u32 m0, s49, 143360\n\t"
      "s_mov_b32 s50, 0\n\t"
      "buffer_load_dwordx4 v111, s[36:39], s50 offen  lds\n\t"
      "s_add_u32 m0, s49, 53248\n\t"
      "s_mov_b32 s50, 393216\n\t"
      "buffer_load_dwordx4 v101, s[28:31], s50 offen  lds\n\t"
      "s_waitcnt vmcnt(7)\n\t"
      "s_barrier\n\t"
      "s_add_u32 m0, s49, 57344\n\t"
      "s_mov_b32 s50, 0\n\t"
      "buffer_load_dwordx4 v102, s[28:31], s50 offen  lds\n\t"
      "s_add_u32 m0, s49, 61440\n\t"
      "s_mov_b32 s50, 65536\n\t"
      "buffer_load_dwordx4 v102, s[28:31], s50 offen  lds\n\t"
      "s_add_u32 m0, s49, 65536\n\t"
      "s_mov_b32 s50, 131072\n\t"
      "buffer_load_dwordx4 v102, s[28:31], s50 offen  lds\n\t"
      "s_waitcnt vmcnt(7)\n\t"
      "s_barrier\n\t"
      "s_add_u32 m0, s49, 69632\n\t"
      "s_mov_b32 s50, 196608\n\t"
      "buffer_load_dwordx4 v102, s[28:31], s50 offen  lds\n\t"
      "s_add_u32 m0, s49, 73728\n\t"
      "s_mov_b32 s50, 262144\n\t"
      "buffer_load_dwordx4 v102, s[28:31], s50 offen  lds\n\t"
      "s_add_u32 m0, s49, 77824\n\t"
      "s_mov_b32 s50, 327680\n\t"
      "buffer_load_dwordx4 v102, s[28:31], s50 offen  lds\n\t"
      "s_add_u32 m0, s49, 81920\n\t"
      "s_mov_b32 s50, 393216\n\t"
      "buffer_load_dwordx4 v102, s[28:31], s50 offen  lds\n\t"
      "s_add_u32 m0, s49, 147456\n\t"
      "s_mov_b32 s50, 458752\n\t"
      "buffer_load_dwordx4 v108, s[36:39], s50 offen  lds\n\t"
      "s_add_u32 m0, s49, 86016\n\t"
      "s_mov_b32 s50, 0\n\t"
      "buffer_load_dwordx4 v103, s[28:31], s50 offen  lds\n\t"
      "s_waitcnt vmcnt(9)\n\t"
      "s_barrier\n\t"
      "s_add_u32 m0, s49, 90112\n\t"
      "s_mov_b32 s50, 65536\n\t"
      "buffer_load_dwordx4 v103, s[28:31], s50 offen  lds\n\t"
      "s_add_u32 m0, s49, 94208\n\t"
      "s_mov_b32 s50, 131072\n\t"
      "buffer_load_dwordx4 v103, s[28:31], s50 offen  lds\n\t"
      "s_add_u32 m0, s49, 98304\n\t"
      "s_mov_b32 s50, 196608\n\t"
      "buffer_load_dwordx4 v103, s[28:31], s50 offen  lds\n\t"
      "s_add_u32 m0, s49, 102400\n\t"
      "s_mov_b32 s50, 262144\n\t"
      "buffer_load_dwordx4 v103, s[28:31], s50 offen  lds\n\t"
      "s_add_u32 m0, s49, 106496\n\t"
      "s_mov_b32 s50, 327680\n\t"
      "buffer_load_dwordx4 v103, s[28:31], s50 offen  lds\n\t"
      "s_waitcnt vmcnt(10)\n\t"
      "s_barrier\n\t"
      "s_add_u32 m0, s49, 110592\n\t"
      "s_mov_b32 s50, 393216\n\t"
      "buffer_load_dwordx4 v103, s[28:31], s50 offen  lds\n\t"
      "s_add_u32 m0, s49, 151552\n\t"
      "s_mov_b32 s50, 458752\n\t"
      "buffer_load_dwordx4 v109, s[36:39], s50 offen  lds\n\t"
      "s_add_u32 m0, s49, 114688\n\t"
      "s_mov_b32 s50, 512\n\t"
      "buffer_load_dwordx4 v100, s[28:31], s50 offen  lds\n\t"
      "s_add_u32 m0, s49, 118784\n\t"
      "s_mov_b32 s50, 66048\n\t"
      "buffer_load_dwordx4 v100, s[28:31], s50 offen  lds\n\t"
      "s_add_u32 m0, s49, 122880\n\t"
      "s_mov_b32 s50, 131584\n\t"
      "buffer_load_dwordx4 v100, s[28:31], s50 offen  lds\n\t"
      "s_add_u32 m0, s49, 126976\n\t"
      "s_mov_b32 s50, 197120\n\t"
      "buffer_load_dwordx4 v100, s[28:31], s50 offen  lds\n\t"
      "s_add_u32 m0, s49, 0\n\t"
      "s_mov_b32 s50, 262656\n\t"
      "buffer_load_dwordx4 v100, s[28:31], s50 offen  lds\n\t"
      "s_waitcnt vmcnt(13)\n\t"
      "s_barrier\n\t"
      "s_add_u32 m0, s49, 4096\n\t"
      "s_mov_b32 s50, 328192\n\t"
      "buffer_load_dwordx4 v100, s[28:31], s50 offen  lds\n\t"
      "s_add_u32 m0, s49, 8192\n\t"
      "s_mov_b32 s50, 393728\n\t"
      "buffer_load_dwordx4 v100, s[28:31], s50 offen  lds\n\t"
      "s_add_u32 m0, s49, 131072\n\t"
      "s_mov_b32 s50, 458752\n\t"
      "buffer_load_dwordx4 v110, s[36:39], s50 offen  lds\n\t"
      "s_add_u32 m0, s49, 12288\n\t"
      "s_mov_b32 s50, 512\n\t"
      "buffer_load_dwordx4 v101, s[28:31], s50 offen  lds\n\t"
      "s_add_u32 m0, s49, 16384\n\t"
      "s_mov_b32 s50, 66048\n\t"
      "buffer_load_dwordx4 v101, s[28:31], s50 offen  lds\n\t"
      "s_waitcnt vmcnt(10)\n\t"
      "s_barrier\n\t"
      "s_add_u32 m0, s49, 20480\n\t"
      "s_mov_b32 s50, 131584\n\t"
      "buffer_load_dwordx4 v101, s[28:31], s50 offen  lds\n\t"
      "s_add_u32 m0, s49, 24576\n\t"
      "s_mov_b32 s50, 197120\n\t"
      "buffer_load_dwordx4 v101, s[28:31], s50 offen  lds\n\t"
      "s_add_u32 m0, s49, 28672\n\t"
      "s_mov_b32 s50, 262656\n\t"
      "buffer_load_dwordx4 v101, s[28:31], s50 offen  lds\n\t"
      "s_add_u32 m0, s49, 32768\n\t"
      "s_mov_b32 s50, 328192\n\t"
      "buffer_load_dwordx4 v101, s[28:31], s50 offen  lds\n\t"
      "s_add_u32 m0, s49, 36864\n\t"
      "s_mov_b32 s50, 393728\n\t"
      "buffer_load_dwordx4 v101, s[28:31], s50 offen  lds\n\t"
      "s_add_u32 m0, s49, 135168\n\t"
      "s_mov_b32 s50, 458752\n\t"
      "buffer_load_dwordx4 v111, s[36:39], s50 offen  lds\n\t"
      "s_add_u32 m0, s49, 40960\n\t"
      "s_mov_b32 s50, 512\n\t"
      "buffer_load_dwordx4 v102, s[28:31], s50 offen  lds\n\t"
      "s_add_u32 m0, s49, 45056\n\t"
      "s_mov_b32 s50, 66048\n\t"
      "buffer_load_dwordx4 v102, s[28:31], s50 offen  lds\n\t"
      "s_add_u32 m0, s49, 49152\n\t"
      "s_mov_b32 s50, 131584\n\t"
      "buffer_load_dwordx4 v102, s[28:31], s50 offen  lds\n\t"
      "s_add_u32 m0, s49, 53248\n\t"
      "s_mov_b32 s50, 197120\n\t"
      "buffer_load_dwordx4 v102, s[28:31], s50 offen  lds\n\t"
      "s_add_u32 m0, s49, 57344\n\t"
      "s_mov_b32 s50, 262656\n\t"
      "buffer_load_dwordx4 v102, s[28:31], s50 offen  lds\n\t"
      "s_add_u32 m0, s49, 61440\n\t"
      "s_mov_b32 s50, 328192\n\t"
      "buffer_load_dwordx4 v102, s[28:31], s50 offen  lds\n\t"
      "s_add_u32 m0, s49, 65536\n\t"
      "s_mov_b32 s50, 393728\n\t"
      "buffer_load_dwordx4 v102, s[28:31], s50 offen  lds\n\t"
      "s_add_u32 m0, s49, 139264\n\t"
      "s_mov_b32 s50, 917504\n\t"
      "buffer_load_dwordx4 v108, s[36:39], s50 offen  lds\n\t"
      "s_mov_b32 s44, s24\n\t"
      "s_and_b32 s45, s25, 0xffff\n\t"
      "s_mov_b32 s46, 14680064\n\t"
      "s_mov_b32 s47, 0x20000\n\t"
      "s_lshl_b32 s56, s48, 2\n\t"
      "v_lshrrev_b32 v2, 4, v1\n\t"
      "v_add_u32 v2, s56, v2\n\t"
      "v_and_b32 v3, 15, v1\n\t"
      "v_and_b32 v4, 7, v2\n\t"
      "v_xor_b32 v4, v4, v3\n\t"
      "v_lshlrev_b32 v4, 4, v4\n\t"
      "v_lshl_add_u32 v4, v2, 8, v4\n\t"
      "v_add_u32 v9, s53, v4\n\t"
      "v_add_u32 v10, 0x10000, v9\n\t"
      "v_add_u32 v11, 0x20000, v9\n\t"
      "v_add_u32 v2, s51, v2\n\t"
      "v_mul_u32_u24 v2, 7168, v2\n\t"
      "s_lshl_b32 s56, s52, 1\n\t"
      "v_lshlrev_b32 v3, 4, v3\n\t"
      "v_add3_u32 v8, v2, s56, v3\n\t"
      "s_waitcnt vmcnt(16)\n\t"
      "s_barrier\n\t"
      "s_add_u32 m0, s49, 69632\n\t"
      "s_mov_b32 s50, 512\n\t"
      "buffer_load_dwordx4 v103, s[28:31], s50 offen  lds\n\t"
      "s_add_u32 m0, s49, 73728\n\t"
      "s_mov_b32 s50, 66048\n\t"
      "buffer_load_dwordx4 v103, s[28:31], s50 offen  lds\n\t"
      "s_add_u32 m0, s49, 77824\n\t"
      "s_mov_b32 s50, 131584\n\t"
      "buffer_load_dwordx4 v103, s[28:31], s50 offen  lds\n\t"
      "s_add_u32 m0, s49, 81920\n\t"
      "s_mov_b32 s50, 197120\n\t"
      "buffer_load_dwordx4 v103, s[28:31], s50 offen  lds\n\t"
      "s_add_u32 m0, s49, 86016\n\t"
      "s_mov_b32 s50, 262656\n\t"
      "buffer_load_dwordx4 v103, s[28:31], s50 offen  lds\n\t"
      "s_add_u32 m0, s49, 90112\n\t"
      "s_mov_b32 s50, 328192\n\t"
      "buffer_load_dwordx4 v103, s[28:31], s50 offen  lds\n\t"
      "s_add_u32 m0, s49, 94208\n\t"
      "s_mov_b32 s50, 393728\n\t"
      "buffer_load_dwordx4 v103, s[28:31], s50 offen  lds\n\t"
      "s_add_u32 m0, s49, 143360\n\t"
      "s_mov_b32 s50, 917504\n\t"
      "buffer_load_dwordx4 v109, s[36:39], s50 offen  lds\n\t"
      "s_add_u32 m0, s49, 98304\n\t"
      "s_mov_b32 s50, 1024\n\t"
      "buffer_load_dwordx4 v100, s[28:31], s50 offen  lds\n\t"
      "s_add_u32 m0, s49, 102400\n\t"
      "s_mov_b32 s50, 66560\n\t"
      "buffer_load_dwordx4 v100, s[28:31], s50 offen  lds\n\t"
      "s_add_u32 m0, s49, 106496\n\t"
      "s_mov_b32 s50, 132096\n\t"
      "buffer_load_dwordx4 v100, s[28:31], s50 offen  lds\n\t"
      "s_add_u32 m0, s49, 147456\n\t"
      "s_mov_b32 s50, 917504\n\t"
      "buffer_load_dwordx4 v110, s[36:39], s50 offen  lds\n\t"
      "s_waitcnt vmcnt(20)\n\t"
      "s_barrier\n\t"
      "s_add_u32 m0, s49, 110592\n\t"
      "s_mov_b32 s50, 197632\n\t"
      "buffer_load_dwordx4 v100, s[28:31], s50 offen  lds\n\t"
      "s_add_u32 m0, s49, 114688\n\t"
      "s_mov_b32 s50, 263168\n\t"
      "buffer_load_dwordx4 v100, s[28:31], s50 offen  lds\n\t"
      "s_add_u32 m0, s49, 118784\n\t"
      "s_mov_b32 s50, 328704\n\t"
      "buffer_load_dwordx4 v100, s[28:31], s50 offen  lds\n\t"
      "s_add_u32 m0, s49, 122880\n\t"
      "s_mov_b32 s50, 394240\n\t"
      "buffer_load_dwordx4 v100, s[28:31], s50 offen  lds\n\t"
      "s_add_u32 m0, s49, 126976\n\t"
      "s_mov_b32 s50, 1024\n\t"
      "buffer_load_dwordx4 v101, s[28:31], s50 offen  lds\n\t"
      "s_add_u32 m0, s49, 0\n\t"
      "s_mov_b32 s50, 66560\n\t"
      "buffer_load_dwordx4 v101, s[28:31], s50 offen  lds\n\t"
      "s_add_u32 m0, s49, 151552\n\t"
      "s_mov_b32 s50, 917504\n\t"
      "buffer_load_dwordx4 v111, s[36:39], s50 offen  lds\n\t"
      "s_waitcnt vmcnt(19)\n\t"
      "s_barrier\n\t"
      "s_add_u32 m0, s49, 4096\n\t"
      "s_mov_b32 s50, 132096\n\t"
      "buffer_load_dwordx4 v101, s[28:31], s50 offen  lds\n\t"
      "s_add_u32 m0, s49, 8192\n\t"
      "s_mov_b32 s50, 197632\n\t"
      "buffer_load_dwordx4 v101, s[28:31], s50 offen  lds\n\t"
      "s_add_u32 m0, s49, 12288\n\t"
      "s_mov_b32 s50, 263168\n\t"
      "buffer_load_dwordx4 v101, s[28:31], s50 offen  lds\n\t"
      "s_add_u32 m0, s49, 16384\n\t"
      "s_mov_b32 s50, 328704\n\t"
      "buffer_load_dwordx4 v101, s[28:31], s50 offen  lds\n\t"
      "s_add_u32 m0, s49, 20480\n\t"
      "s_mov_b32 s50, 394240\n\t"
      "buffer_load_dwordx4 v101, s[28:31], s50 offen  lds\n\t"
      "s_add_u32 m0, s49, 24576\n\t"
      "s_mov_b32 s50, 1024\n\t"
      "buffer_load_dwordx4 v102, s[28:31], s50 offen  lds\n\t"
      "s_add_u32 m0, s49, 131072\n\t"
      "s_mov_b32 s50, 1376256\n\t"
      "buffer_load_dwordx4 v108, s[36:39], s50 offen  lds\n\t"
      "s_waitcnt vmcnt(18)\n\t"
      "s_barrier\n\t"
      "s_add_u32 m0, s49, 28672\n\t"
      "s_mov_b32 s50, 66560\n\t"
      "buffer_load_dwordx4 v102, s[28:31], s50 offen  lds\n\t"
      "s_add_u32 m0, s49, 32768\n\t"
      "s_mov_b32 s50, 132096\n\t"
      "buffer_load_dwordx4 v102, s[28:31], s50 offen  lds\n\t"
      "s_add_u32 m0, s49, 36864\n\t"
      "s_mov_b32 s50, 197632\n\t"
      "buffer_load_dwordx4 v102, s[28:31], s50 offen  lds\n\t"
      "s_add_u32 m0, s49, 40960\n\t"
      "s_mov_b32 s50, 263168\n\t"
      "buffer_load_dwordx4 v102, s[28:31], s50 offen  lds\n\t"
      "s_add_u32 m0, s49, 45056\n\t"
      "s_mov_b32 s50, 328704\n\t"
      "buffer_load_dwordx4 v102, s[28:31], s50 offen  lds\n\t"
      "s_add_u32 m0, s49, 49152\n\t"
      "s_mov_b32 s50, 394240\n\t"
      "buffer_load_dwordx4 v102, s[28:31], s50 offen  lds\n\t"
      "s_add_u32 m0, s49, 53248\n\t"
      "s_mov_b32 s50, 1024\n\t"
      "buffer_load_dwordx4 v103, s[28:31], s50 offen  lds\n\t"
      "s_add_u32 m0, s49, 57344\n\t"
      "s_mov_b32 s50, 66560\n\t"
      "buffer_load_dwordx4 v103, s[28:31], s50 offen  lds\n\t"
      "s_add_u32 m0, s49, 61440\n\t"
      "s_mov_b32 s50, 132096\n\t"
      "buffer_load_dwordx4 v103, s[28:31], s50 offen  lds\n\t"
      "s_add_u32 m0, s49, 135168\n\t"
      "s_mov_b32 s50, 1376256\n\t"
      "buffer_load_dwordx4 v109, s[36:39], s50 offen  lds\n\t"
      "s_waitcnt vmcnt(20)\n\t"
      "s_barrier\n\t"
      "s_add_u32 m0, s49, 65536\n\t"
      "s_mov_b32 s50, 197632\n\t"
      "buffer_load_dwordx4 v103, s[28:31], s50 offen  lds\n\t"
      "s_add_u32 m0, s49, 69632\n\t"
      "s_mov_b32 s50, 263168\n\t"
      "buffer_load_dwordx4 v103, s[28:31], s50 offen  lds\n\t"
      "s_add_u32 m0, s49, 73728\n\t"
      "s_mov_b32 s50, 328704\n\t"
      "buffer_load_dwordx4 v103, s[28:31], s50 offen  lds\n\t"
      "s_add_u32 m0, s49, 77824\n\t"
      "s_mov_b32 s50, 394240\n\t"
      "buffer_load_dwordx4 v103, s[28:31], s50 offen  lds\n\t"
      "s_add_u32 m0, s49, 81920\n\t"
      "s_mov_b32 s50, 1536\n\t"
      "buffer_load_dwordx4 v100, s[28:31], s50 offen  lds\n\t"
      "s_add_u32 m0, s49, 86016\n\t"
      "s_mov_b32 s50, 1536\n\t"
      "buffer_load_dwordx4 v101, s[28:31], s50 offen  lds\n\t"
      "s_add_u32 m0, s49, 139264\n\t"
      "s_mov_b32 s50, 1376256\n\t"
      "buffer_load_dwordx4 v110, s[36:39], s50 offen  lds\n\t"
      "s_waitcnt vmcnt(19)\n\t"
      "s_barrier\n\t"
      "s_add_u32 m0, s49, 90112\n\t"
      "s_mov_b32 s50, 1536\n\t"
      "buffer_load_dwordx4 v102, s[28:31], s50 offen  lds\n\t"
      "s_add_u32 m0, s49, 94208\n\t"
      "s_mov_b32 s50, 1536\n\t"
      "buffer_load_dwordx4 v103, s[28:31], s50 offen  lds\n\t"
      "s_add_u32 m0, s49, 143360\n\t"
      "s_mov_b32 s50, 1376256\n\t"
      "buffer_load_dwordx4 v111, s[36:39], s50 offen  lds\n\t"
      "s_add_u32 m0, s49, 98304\n\t"
      "s_mov_b32 s50, 67072\n\t"
      "buffer_load_dwordx4 v100, s[28:31], s50 offen  lds\n\t"
      "s_add_u32 m0, s49, 102400\n\t"
      "s_mov_b32 s50, 67072\n\t"
      "buffer_load_dwordx4 v101, s[28:31], s50 offen  lds\n\t"
      "s_add_u32 m0, s49, 106496\n\t"
      "s_mov_b32 s50, 67072\n\t"
      "buffer_load_dwordx4 v102, s[28:31], s50 offen  lds\n\t"
      "s_add_u32 m0, s49, 110592\n\t"
      "s_mov_b32 s50, 67072\n\t"
      "buffer_load_dwordx4 v103, s[28:31], s50 offen  lds\n\t"
      "s_waitcnt vmcnt(18)\n\t"
      "s_barrier\n\t"
      "s_add_u32 m0, s49, 114688\n\t"
      "s_mov_b32 s50, 132608\n\t"
      "buffer_load_dwordx4 v100, s[28:31], s50 offen  lds\n\t"
      "s_add_u32 m0, s49, 118784\n\t"
      "s_mov_b32 s50, 132608\n\t"
      "buffer_load_dwordx4 v101, s[28:31], s50 offen  lds\n\t"
      "s_add_u32 m0, s49, 122880\n\t"
      "s_mov_b32 s50, 132608\n\t"
      "buffer_load_dwordx4 v102, s[28:31], s50 offen  lds\n\t"
      "s_add_u32 m0, s49, 126976\n\t"
      "s_mov_b32 s50, 132608\n\t"
      "buffer_load_dwordx4 v103, s[28:31], s50 offen  lds\n\t"
      "s_add_u32 m0, s49, 0\n\t"
      "s_mov_b32 s50, 198144\n\t"
      "buffer_load_dwordx4 v100, s[28:31], s50 offen  lds\n\t"
      "s_add_u32 m0, s49, 4096\n\t"
      "s_mov_b32 s50, 198144\n\t"
      "buffer_load_dwordx4 v101, s[28:31], s50 offen  lds\n\t"
      "s_add_u32 m0, s49, 8192\n\t"
      "s_mov_b32 s50, 198144\n\t"
      "buffer_load_dwordx4 v102, s[28:31], s50 offen  lds\n\t"
      "s_add_u32 m0, s49, 12288\n\t"
      "s_mov_b32 s50, 198144\n\t"
      "buffer_load_dwordx4 v103, s[28:31], s50 offen  lds\n\t"
      "s_add_u32 m0, s49, 16384\n\t"
      "s_mov_b32 s50, 263680\n\t"
      "buffer_load_dwordx4 v100, s[28:31], s50 offen  lds\n\t"
      "s_waitcnt vmcnt(19)\n\t"
      "s_barrier\n\t"
      "s_add_u32 m0, s49, 20480\n\t"
      "s_mov_b32 s50, 263680\n\t"
      "buffer_load_dwordx4 v101, s[28:31], s50 offen  lds\n\t"
      "s_add_u32 m0, s49, 24576\n\t"
      "s_mov_b32 s50, 263680\n\t"
      "buffer_load_dwordx4 v102, s[28:31], s50 offen  lds\n\t"
      "s_add_u32 m0, s49, 28672\n\t"
      "s_mov_b32 s50, 263680\n\t"
      "buffer_load_dwordx4 v103, s[28:31], s50 offen  lds\n\t"
      "s_add_u32 m0, s49, 32768\n\t"
      "s_mov_b32 s50, 329216\n\t"
      "buffer_load_dwordx4 v100, s[28:31], s50 offen  lds\n\t"
      "s_add_u32 m0, s49, 36864\n\t"
      "s_mov_b32 s50, 329216\n\t"
      "buffer_load_dwordx4 v101, s[28:31], s50 offen  lds\n\t"
      "s_add_u32 m0, s49, 40960\n\t"
      "s_mov_b32 s50, 329216\n\t"
      "buffer_load_dwordx4 v102, s[28:31], s50 offen  lds\n\t"
      "s_waitcnt vmcnt(19)\n\t"
      "s_barrier\n\t"
      "s_add_u32 m0, s49, 45056\n\t"
      "s_mov_b32 s50, 329216\n\t"
      "buffer_load_dwordx4 v103, s[28:31], s50 offen  lds\n\t"
      "s_add_u32 m0, s49, 49152\n\t"
      "s_mov_b32 s50, 394752\n\t"
      "buffer_load_dwordx4 v100, s[28:31], s50 offen  lds\n\t"
      "s_add_u32 m0, s49, 53248\n\t"
      "s_mov_b32 s50, 394752\n\t"
      "buffer_load_dwordx4 v101, s[28:31], s50 offen  lds\n\t"
      "s_add_u32 m0, s49, 57344\n\t"
      "s_mov_b32 s50, 394752\n\t"
      "buffer_load_dwordx4 v102, s[28:31], s50 offen  lds\n\t"
      "s_add_u32 m0, s49, 61440\n\t"
      "s_mov_b32 s50, 394752\n\t"
      "buffer_load_dwordx4 v103, s[28:31], s50 offen  lds\n\t"
      "s_waitcnt vmcnt(17)\n\t"
      "s_barrier\n\t"
      "s_waitcnt vmcnt(13)\n\t"
      "s_barrier\n\t"
      "s_waitcnt vmcnt(0)\n\t"
      "ds_read_b128 v[12:15], v10 offset:0\n\t"
      "ds_read_b128 v[16:19], v10 offset:4096\n\t"
      "s_mov_b32 s50, 0\n\t"
      "s_waitcnt lgkmcnt(1)\n\t"
      "buffer_store_dwordx4 v[12:15], v8, s[44:47], s50 offen sc1\n\t"
      "s_mov_b32 s50, 114688\n\t"
      "s_waitcnt lgkmcnt(0)\n\t"
      "buffer_store_dwordx4 v[16:19], v8, s[44:47], s50 offen sc1\n\t"
      "s_barrier\n\t"
      "ds_read_b128 v[20:23], v10 offset:8192\n\t"
      "ds_read_b128 v[24:27], v10 offset:12288\n\t"
      "s_mov_b32 s50, 229376\n\t"
      "s_waitcnt lgkmcnt(1)\n\t"
      "buffer_store_dwordx4 v[20:23], v8, s[44:47], s50 offen sc1\n\t"
      "s_mov_b32 s50, 344064\n\t"
      "s_waitcnt lgkmcnt(0)\n\t"
      "buffer_store_dwordx4 v[24:27], v8, s[44:47], s50 offen sc1\n\t"
      "s_barrier\n\t"
      "ds_read_b128 v[28:31], v9 offset:0\n\t"
      "ds_read_b128 v[32:35], v10 offset:16384\n\t"
      "s_mov_b32 s50, 458752\n\t"
      "s_waitcnt lgkmcnt(1)\n\t"
      "buffer_store_dwordx4 v[28:31], v8, s[44:47], s50 offen sc1\n\t"
      "s_mov_b32 s50, 573440\n\t"
      "s_waitcnt lgkmcnt(0)\n\t"
      "buffer_store_dwordx4 v[32:35], v8, s[44:47], s50 offen sc1\n\t"
      "s_barrier\n\t"
      "ds_read_b128 v[36:39], v9 offset:4096\n\t"
      "ds_read_b128 v[40:43], v9 offset:8192\n\t"
      "s_mov_b32 s50, 688128\n\t"
      "s_waitcnt lgkmcnt(1)\n\t"
      "buffer_store_dwordx4 v[36:39], v8, s[44:47], s50 offen sc1\n\t"
      "s_mov_b32 s50, 802816\n\t"
      "s_waitcnt lgkmcnt(0)\n\t"
      "buffer_store_dwordx4 v[40:43], v8, s[44:47], s50 offen sc1\n\t"
      "s_barrier\n\t"
      "ds_read_b128 v[44:47], v9 offset:12288\n\t"
      "ds_read_b128 v[48:51], v9 offset:16384\n\t"
      "s_mov_b32 s50, 917504\n\t"
      "s_waitcnt lgkmcnt(1)\n\t"
      "buffer_store_dwordx4 v[44:47], v8, s[44:47], s50 offen sc1\n\t"
      "s_mov_b32 s50, 1032192\n\t"
      "s_waitcnt lgkmcnt(0)\n\t"
      "buffer_store_dwordx4 v[48:51], v8, s[44:47], s50 offen sc1\n\t"
      "s_barrier\n\t"
      "ds_read_b128 v[52:55], v9 offset:20480\n\t"
      "ds_read_b128 v[56:59], v9 offset:24576\n\t"
      "s_mov_b32 s50, 1146880\n\t"
      "s_waitcnt lgkmcnt(1)\n\t"
      "buffer_store_dwordx4 v[52:55], v8, s[44:47], s50 offen sc1\n\t"
      "s_mov_b32 s50, 1261568\n\t"
      "s_waitcnt lgkmcnt(0)\n\t"
      "buffer_store_dwordx4 v[56:59], v8, s[44:47], s50 offen sc1\n\t"
      "s_barrier\n\t"
      "ds_read_b128 v[60:63], v9 offset:28672\n\t"
      "ds_read_b128 v[64:67], v9 offset:32768\n\t"
      "s_mov_b32 s50, 1376256\n\t"
      "s_waitcnt lgkmcnt(1)\n\t"
      "buffer_store_dwordx4 v[60:63], v8, s[44:47], s50 offen sc1\n\t"
      "s_mov_b32 s50, 1490944\n\t"
      "s_waitcnt lgkmcnt(0)\n\t"
      "buffer_store_dwordx4 v[64:67], v8, s[44:47], s50 offen sc1\n\t"
      "s_endpgm\n\t"
      "LL15_%=:\n\t"
      "s_add_u32 m0, s49, 24576\n\t"
      "s_mov_b32 s50, 393216\n\t"
      "buffer_load_dwordx4 v100, s[28:31], s50 offen  lds\n\t"
      "v_xor_b32 v101, 128, v100\n\t"
      "v_xor_b32 v102, 256, v100\n\t"
      "v_xor_b32 v103, 384, v100\n\t"
      "s_cmp_lt_u32 s48, 2\n\t"
      "s_cselect_b32 s56, 0, 0x40000000\n\t"
      "v_add_u32 v104, s56, v100\n\t"
      "v_add_u32 v105, s56, v101\n\t"
      "v_add_u32 v106, s56, v102\n\t"
      "v_add_u32 v107, s56, v103\n\t"
      "s_mul_i32 s56, s48, 28672\n\t"
      "s_lshl_b32 s57, s52, 2\n\t"
      "s_add_u32 s56, s56, s57\n\t"
      "v_lshrrev_b32 v2, 5, v1\n\t"
      "v_mul_u32_u24 v3, 14336, v2\n\t"
      "v_lshlrev_b32 v2, 3, v2\n\t"
      "v_and_b32 v4, 31, v1\n\t"
      "v_xor_b32 v2, v2, v4\n\t"
      "v_lshlrev_b32 v2, 4, v2\n\t"
      "v_add3_u32 v2, v3, s56, v2\n\t"
      "s_xor_b32 s56, s55, 0\n\t"
      "s_mul_i32 s56, s56, 114688\n\t"
      "v_add_u32 v108, s56, v2\n\t"
      "s_xor_b32 s56, s55, 1\n\t"
      "s_mul_i32 s56, s56, 114688\n\t"
      "v_add_u32 v109, s56, v2\n\t"
      "s_xor_b32 s56, s55, 2\n\t"
      "s_mul_i32 s56, s56, 114688\n\t"
      "v_add_u32 v110, s56, v2\n\t"
      "s_xor_b32 s56, s55, 3\n\t"
      "s_mul_i32 s56, s56, 114688\n\t"
      "v_add_u32 v111, s56, v2\n\t"
      "s_mov_b32 s36, s18\n\t"
      "s_and_b32 s37, s19, 0xffff\n\t"
      "s_mov_b32 s38, 1835008\n\t"
      "s_mov_b32 s39, 0x20000\n\t"
      "s_add_u32 m0, s49, 139264\n\t"
      "s_mov_b32 s50, 0\n\t"
      "buffer_load_dwordx4 v110, s[36:39], s50 offen  lds\n\t"
      "s_add_u32 m0, s49, 28672\n\t"
      "s_mov_b32 s50, 458752\n\t"
      "buffer_load_dwordx4 v104, s[28:31], s50 offen  lds\n\t"
      "s_waitcnt vmcnt(7)\n\t"
      "s_barrier\n\t"
      "s_lshl_b32 s56, s48, 2\n\t"
      "v_lshrrev_b32 v5, 4, v1\n\t"
      "v_add_u32 v5, s56, v5\n\t"
      "v_xor_b32 v5, s55, v5\n\t"
      "v_mul_u32_u24 v5, 7168, v5\n\t"
      "v_and_b32 v3, 15, v1\n\t"
      "v_lshlrev_b32 v3, 4, v3\n\t"
      "s_lshl_b32 s56, s52, 1\n\t"
      "v_add3_u32 v5, v5, s56, v3\n\t"
      "s_add_u32 m0, s49, 155648\n\t"
      "s_mov_b32 s40, s20\n\t"
      "s_and_b32 s41, s21, 0xffff\n\t"
      "s_mov_b32 s42, 114688\n\t"
      "s_mov_b32 s43, 0x20000\n\t"
      "buffer_load_dwordx4 v5, s[40:43], 0 offen lds\n\t"
      "s_add_u32 m0, s49, 159744\n\t"
      "s_mov_b32 s44, s22\n\t"
      "s_and_b32 s45, s23, 0xffff\n\t"
      "s_mov_b32 s46, 114688\n\t"
      "s_mov_b32 s47, 0x20000\n\t"
      "buffer_load_dwordx4 v5, s[44:47], 0 offen lds\n\t"
      "s_waitcnt vmcnt(7)\n\t"
      "s_barrier\n\t"
      "s_add_u32 m0, s49, 32768\n\t"
      "s_mov_b32 s50, 0\n\t"
      "buffer_load_dwordx4 v101, s[28:31], s50 offen  lds\n\t"
      "s_add_u32 m0, s49, 36864\n\t"
      "s_mov_b32 s50, 65536\n\t"
      "buffer_load_dwordx4 v101, s[28:31], s50 offen  lds\n\t"
      "s_add_u32 m0, s49, 40960\n\t"
      "s_mov_b32 s50, 131072\n\t"
      "buffer_load_dwordx4 v101, s[28:31], s50 offen  lds\n\t"
      "s_waitcnt vmcnt(8)\n\t"
      "s_barrier\n\t"
      "s_add_u32 m0, s49, 45056\n\t"
      "s_mov_b32 s50, 196608\n\t"
      "buffer_load_dwordx4 v101, s[28:31], s50 offen  lds\n\t"
      "s_add_u32 m0, s49, 49152\n\t"
      "s_mov_b32 s50, 262144\n\t"
      "buffer_load_dwordx4 v101, s[28:31], s50 offen  lds\n\t"
      "s_add_u32 m0, s49, 53248\n\t"
      "s_mov_b32 s50, 327680\n\t"
      "buffer_load_dwordx4 v101, s[28:31], s50 offen  lds\n\t"
      "s_add_u32 m0, s49, 57344\n\t"
      "s_mov_b32 s50, 393216\n\t"
      "buffer_load_dwordx4 v101, s[28:31], s50 offen  lds\n\t"
      "s_add_u32 m0, s49, 143360\n\t"
      "s_mov_b32 s50, 0\n\t"
      "buffer_load_dwordx4 v111, s[36:39], s50 offen  lds\n\t"
      "s_waitcnt vmcnt(7)\n\t"
      "s_barrier\n\t"
      "s_add_u32 m0, s49, 61440\n\t"
      "s_mov_b32 s50, 458752\n\t"
      "buffer_load_dwordx4 v105, s[28:31], s50 offen  lds\n\t"
      "s_add_u32 m0, s49, 65536\n\t"
      "s_mov_b32 s50, 0\n\t"
      "buffer_load_dwordx4 v102, s[28:31], s50 offen  lds\n\t"
      "s_add_u32 m0, s49, 69632\n\t"
      "s_mov_b32 s50, 65536\n\t"
      "buffer_load_dwordx4 v102, s[28:31], s50 offen  lds\n\t"
      "s_waitcnt vmcnt(7)\n\t"
      "s_barrier\n\t"
      "s_add_u32 m0, s49, 73728\n\t"
      "s_mov_b32 s50, 131072\n\t"
      "buffer_load_dwordx4 v102, s[28:31], s50 offen  lds\n\t"
      "s_add_u32 m0, s49, 77824\n\t"
      "s_mov_b32 s50, 196608\n\t"
      "buffer_load_dwordx4 v102, s[28:31], s50 offen  lds\n\t"
      "s_add_u32 m0, s49, 81920\n\t"
      "s_mov_b32 s50, 262144\n\t"
      "buffer_load_dwordx4 v102, s[28:31], s50 offen  lds\n\t"
      "s_add_u32 m0, s49, 86016\n\t"
      "s_mov_b32 s50, 327680\n\t"
      "buffer_load_dwordx4 v102, s[28:31], s50 offen  lds\n\t"
      "s_add_u32 m0, s49, 90112\n\t"
      "s_mov_b32 s50, 393216\n\t"
      "buffer_load_dwordx4 v102, s[28:31], s50 offen  lds\n\t"
      "s_add_u32 m0, s49, 147456\n\t"
      "s_mov_b32 s50, 458752\n\t"
      "buffer_load_dwordx4 v108, s[36:39], s50 offen  lds\n\t"
      "s_waitcnt vmcnt(9)\n\t"
      "s_barrier\n\t"
      "s_add_u32 m0, s49, 94208\n\t"
      "s_mov_b32 s50, 458752\n\t"
      "buffer_load_dwordx4 v106, s[28:31], s50 offen  lds\n\t"
      "s_add_u32 m0, s49, 98304\n\t"
      "s_mov_b32 s50, 0\n\t"
      "buffer_load_dwordx4 v103, s[28:31], s50 offen  lds\n\t"
      "s_add_u32 m0, s49, 102400\n\t"
      "s_mov_b32 s50, 65536\n\t"
      "buffer_load_dwordx4 v103, s[28:31], s50 offen  lds\n\t"
      "s_add_u32 m0, s49, 106496\n\t"
      "s_mov_b32 s50, 131072\n\t"
      "buffer_load_dwordx4 v103, s[28:31], s50 offen  lds\n\t"
      "s_add_u32 m0, s49, 110592\n\t"
      "s_mov_b32 s50, 196608\n\t"
      "buffer_load_dwordx4 v103, s[28:31], s50 offen  lds\n\t"
      "s_waitcnt vmcnt(10)\n\t"
      "s_barrier\n\t"
      "s_add_u32 m0, s49, 114688\n\t"
      "s_mov_b32 s50, 262144\n\t"
      "buffer_load_dwordx4 v103, s[28:31], s50 offen  lds\n\t"
      "s_add_u32 m0, s49, 118784\n\t"
      "s_mov_b32 s50, 327680\n\t"
      "buffer_load_dwordx4 v103, s[28:31], s50 offen  lds\n\t"
      "s_add_u32 m0, s49, 122880\n\t"
      "s_mov_b32 s50, 393216\n\t"
      "buffer_load_dwordx4 v103, s[28:31], s50 offen  lds\n\t"
      "s_add_u32 m0, s49, 151552\n\t"
      "s_mov_b32 s50, 458752\n\t"
      "buffer_load_dwordx4 v109, s[36:39], s50 offen  lds\n\t"
      "s_add_u32 m0, s49, 126976\n\t"
      "s_mov_b32 s50, 458752\n\t"
      "buffer_load_dwordx4 v107, s[28:31], s50 offen  lds\n\t"
      "s_add_u32 m0, s49, 0\n\t"
      "s_mov_b32 s50, 512\n\t"
      "buffer_load_dwordx4 v100, s[28:31], s50 offen  lds\n\t"
      "s_add_u32 m0, s49, 4096\n\t"
      "s_mov_b32 s50, 66048\n\t"
      "buffer_load_dwordx4 v100, s[28:31], s50 offen  lds\n\t"
      "s_waitcnt vmcnt(11)\n\t"
      "s_barrier\n\t"
      "s_add_u32 m0, s49, 8192\n\t"
      "s_mov_b32 s50, 131584\n\t"
      "buffer_load_dwordx4 v100, s[28:31], s50 offen  lds\n\t"
      "s_add_u32 m0, s49, 12288\n\t"
      "s_mov_b32 s50, 197120\n\t"
      "buffer_load_dwordx4 v100, s[28:31], s50 offen  lds\n\t"
      "s_add_u32 m0, s49, 16384\n\t"
      "s_mov_b32 s50, 262656\n\t"
      "buffer_load_dwordx4 v100, s[28:31], s50 offen  lds\n\t"
      "s_add_u32 m0, s49, 20480\n\t"
      "s_mov_b32 s50, 328192\n\t"
      "buffer_load_dwordx4 v100, s[28:31], s50 offen  lds\n\t"
      "s_add_u32 m0, s49, 24576\n\t"
      "s_mov_b32 s50, 393728\n\t"
      "buffer_load_dwordx4 v100, s[28:31], s50 offen  lds\n\t"
      "s_add_u32 m0, s49, 131072\n\t"
      "s_mov_b32 s50, 458752\n\t"
      "buffer_load_dwordx4 v110, s[36:39], s50 offen  lds\n\t"
      "s_add_u32 m0, s49, 28672\n\t"
      "s_mov_b32 s50, 459264\n\t"
      "buffer_load_dwordx4 v104, s[28:31], s50 offen  lds\n\t"
      "s_add_u32 m0, s49, 32768\n\t"
      "s_mov_b32 s50, 512\n\t"
      "buffer_load_dwordx4 v101, s[28:31], s50 offen  lds\n\t"
      "s_waitcnt vmcnt(10)\n\t"
      "s_barrier\n\t"
      "s_add_u32 m0, s49, 36864\n\t"
      "s_mov_b32 s50, 66048\n\t"
      "buffer_load_dwordx4 v101, s[28:31], s50 offen  lds\n\t"
      "s_add_u32 m0, s49, 40960\n\t"
      "s_mov_b32 s50, 131584\n\t"
      "buffer_load_dwordx4 v101, s[28:31], s50 offen  lds\n\t"
      "s_add_u32 m0, s49, 45056\n\t"
      "s_mov_b32 s50, 197120\n\t"
      "buffer_load_dwordx4 v101, s[28:31], s50 offen  lds\n\t"
      "s_add_u32 m0, s49, 49152\n\t"
      "s_mov_b32 s50, 262656\n\t"
      "buffer_load_dwordx4 v101, s[28:31], s50 offen  lds\n\t"
      "s_add_u32 m0, s49, 53248\n\t"
      "s_mov_b32 s50, 328192\n\t"
      "buffer_load_dwordx4 v101, s[28:31], s50 offen  lds\n\t"
      "s_add_u32 m0, s49, 57344\n\t"
      "s_mov_b32 s50, 393728\n\t"
      "buffer_load_dwordx4 v101, s[28:31], s50 offen  lds\n\t"
      "s_add_u32 m0, s49, 135168\n\t"
      "s_mov_b32 s50, 458752\n\t"
      "buffer_load_dwordx4 v111, s[36:39], s50 offen  lds\n\t"
      "s_add_u32 m0, s49, 61440\n\t"
      "s_mov_b32 s50, 459264\n\t"
      "buffer_load_dwordx4 v105, s[28:31], s50 offen  lds\n\t"
      "s_add_u32 m0, s49, 65536\n\t"
      "s_mov_b32 s50, 512\n\t"
      "buffer_load_dwordx4 v102, s[28:31], s50 offen  lds\n\t"
      "s_add_u32 m0, s49, 69632\n\t"
      "s_mov_b32 s50, 66048\n\t"
      "buffer_load_dwordx4 v102, s[28:31], s50 offen  lds\n\t"
      "s_add_u32 m0, s49, 73728\n\t"
      "s_mov_b32 s50, 131584\n\t"
      "buffer_load_dwordx4 v102, s[28:31], s50 offen  lds\n\t"
      "s_add_u32 m0, s49, 77824\n\t"
      "s_mov_b32 s50, 197120\n\t"
      "buffer_load_dwordx4 v102, s[28:31], s50 offen  lds\n\t"
      "s_add_u32 m0, s49, 81920\n\t"
      "s_mov_b32 s50, 262656\n\t"
      "buffer_load_dwordx4 v102, s[28:31], s50 offen  lds\n\t"
      "s_add_u32 m0, s49, 86016\n\t"
      "s_mov_b32 s50, 328192\n\t"
      "buffer_load_dwordx4 v102, s[28:31], s50 offen  lds\n\t"
      "s_add_u32 m0, s49, 139264\n\t"
      "s_mov_b32 s50, 917504\n\t"
      "buffer_load_dwordx4 v108, s[36:39], s50 offen  lds\n\t"
      "s_mov_b32 s44, s24\n\t"
      "s_and_b32 s45, s25, 0xffff\n\t"
      "s_mov_b32 s46, 14680064\n\t"
      "s_mov_b32 s47, 0x20000\n\t"
      "s_lshl_b32 s56, s48, 2\n\t"
      "v_lshrrev_b32 v2, 4, v1\n\t"
      "v_add_u32 v2, s56, v2\n\t"
      "v_and_b32 v3, 15, v1\n\t"
      "v_and_b32 v4, 7, v2\n\t"
      "v_xor_b32 v4, v4, v3\n\t"
      "v_lshlrev_b32 v4, 4, v4\n\t"
      "v_lshl_add_u32 v4, v2, 8, v4\n\t"
      "v_add_u32 v9, s53, v4\n\t"
      "v_add_u32 v10, 0x10000, v9\n\t"
      "v_add_u32 v11, 0x20000, v9\n\t"
      "v_add_u32 v2, s51, v2\n\t"
      "v_mul_u32_u24 v2, 7168, v2\n\t"
      "s_lshl_b32 s56, s52, 1\n\t"
      "v_lshlrev_b32 v3, 4, v3\n\t"
      "v_add3_u32 v8, v2, s56, v3\n\t"
      "s_waitcnt vmcnt(16)\n\t"
      "s_barrier\n\t"
      "s_add_u32 m0, s49, 90112\n\t"
      "s_mov_b32 s50, 393728\n\t"
      "buffer_load_dwordx4 v102, s[28:31], s50 offen  lds\n\t"
      "s_add_u32 m0, s49, 94208\n\t"
      "s_mov_b32 s50, 459264\n\t"
      "buffer_load_dwordx4 v106, s[28:31], s50 offen  lds\n\t"
      "s_add_u32 m0, s49, 98304\n\t"
      "s_mov_b32 s50, 512\n\t"
      "buffer_load_dwordx4 v103, s[28:31], s50 offen  lds\n\t"
      "s_add_u32 m0, s49, 102400\n\t"
      "s_mov_b32 s50, 66048\n\t"
      "buffer_load_dwordx4 v103, s[28:31], s50 offen  lds\n\t"
      "s_add_u32 m0, s49, 106496\n\t"
      "s_mov_b32 s50, 131584\n\t"
      "buffer_load_dwordx4 v103, s[28:31], s50 offen  lds\n\t"
      "s_add_u32 m0, s49, 110592\n\t"
      "s_mov_b32 s50, 197120\n\t"
      "buffer_load_dwordx4 v103, s[28:31], s50 offen  lds\n\t"
      "s_add_u32 m0, s49, 143360\n\t"
      "s_mov_b32 s50, 917504\n\t"
      "buffer_load_dwordx4 v109, s[36:39], s50 offen  lds\n\t"
      "s_add_u32 m0, s49, 147456\n\t"
      "s_mov_b32 s50, 917504\n\t"
      "buffer_load_dwordx4 v110, s[36:39], s50 offen  lds\n\t"
      "s_waitcnt vmcnt(15)\n\t"
      "s_barrier\n\t"
      "s_add_u32 m0, s49, 114688\n\t"
      "s_mov_b32 s50, 262656\n\t"
      "buffer_load_dwordx4 v103, s[28:31], s50 offen  lds\n\t"
      "s_add_u32 m0, s49, 118784\n\t"
      "s_mov_b32 s50, 328192\n\t"
      "buffer_load_dwordx4 v103, s[28:31], s50 offen  lds\n\t"
      "s_add_u32 m0, s49, 122880\n\t"
      "s_mov_b32 s50, 393728\n\t"
      "buffer_load_dwordx4 v103, s[28:31], s50 offen  lds\n\t"
      "s_add_u32 m0, s49, 126976\n\t"
      "s_mov_b32 s50, 459264\n\t"
      "buffer_load_dwordx4 v107, s[28:31], s50 offen  lds\n\t"
      "s_add_u32 m0, s49, 0\n\t"
      "s_mov_b32 s50, 1024\n\t"
      "buffer_load_dwordx4 v100, s[28:31], s50 offen  lds\n\t"
      "s_add_u32 m0, s49, 4096\n\t"
      "s_mov_b32 s50, 66560\n\t"
      "buffer_load_dwordx4 v100, s[28:31], s50 offen  lds\n\t"
      "s_add_u32 m0, s49, 8192\n\t"
      "s_mov_b32 s50, 132096\n\t"
      "buffer_load_dwordx4 v100, s[28:31], s50 offen  lds\n\t"
      "s_add_u32 m0, s49, 12288\n\t"
      "s_mov_b32 s50, 197632\n\t"
      "buffer_load_dwordx4 v100, s[28:31], s50 offen  lds\n\t"
      "s_add_u32 m0, s49, 16384\n\t"
      "s_mov_b32 s50, 263168\n\t"
      "buffer_load_dwordx4 v100, s[28:31], s50 offen  lds\n\t"
      "s_add_u32 m0, s49, 20480\n\t"
      "s_mov_b32 s50, 328704\n\t"
      "buffer_load_dwordx4 v100, s[28:31], s50 offen  lds\n\t"
      "s_add_u32 m0, s49, 151552\n\t"
      "s_mov_b32 s50, 917504\n\t"
      "buffer_load_dwordx4 v111, s[36:39], s50 offen  lds\n\t"
      "s_waitcnt vmcnt(17)\n\t"
      "s_barrier\n\t"
      "s_add_u32 m0, s49, 24576\n\t"
      "s_mov_b32 s50, 394240\n\t"
      "buffer_load_dwordx4 v100, s[28:31], s50 offen  lds\n\t"
      "s_add_u32 m0, s49, 28672\n\t"
      "s_mov_b32 s50, 459776\n\t"
      "buffer_load_dwordx4 v104, s[28:31], s50 offen  lds\n\t"
      "s_add_u32 m0, s49, 32768\n\t"
      "s_mov_b32 s50, 1024\n\t"
      "buffer_load_dwordx4 v101, s[28:31], s50 offen  lds\n\t"
      "s_add_u32 m0, s49, 36864\n\t"
      "s_mov_b32 s50, 66560\n\t"
      "buffer_load_dwordx4 v101, s[28:31], s50 offen  lds\n\t"
      "s_add_u32 m0, s49, 40960\n\t"
      "s_mov_b32 s50, 132096\n\t"
      "buffer_load_dwordx4 v101, s[28:31], s50 offen  lds\n\t"
      "s_add_u32 m0, s49, 45056\n\t"
      "s_mov_b32 s50, 197632\n\t"
      "buffer_load_dwordx4 v101, s[28:31], s50 offen  lds\n\t"
      "s_add_u32 m0, s49, 131072\n\t"
      "s_mov_b32 s50, 1376256\n\t"
      "buffer_load_dwordx4 v108, s[36:39], s50 offen  lds\n\t"
      "s_waitcnt vmcnt(14)\n\t"
      "s_barrier\n\t"
      "s_add_u32 m0, s49, 49152\n\t"
      "s_mov_b32 s50, 263168\n\t"
      "buffer_load_dwordx4 v101, s[28:31], s50 offen  lds\n\t"
      "s_add_u32 m0, s49, 53248\n\t"
      "s_mov_b32 s50, 328704\n\t"
      "buffer_load_dwordx4 v101, s[28:31], s50 offen  lds\n\t"
      "s_add_u32 m0, s49, 57344\n\t"
      "s_mov_b32 s50, 394240\n\t"
      "buffer_load_dwordx4 v101, s[28:31], s50 offen  lds\n\t"
      "s_add_u32 m0, s49, 61440\n\t"
      "s_mov_b32 s50, 459776\n\t"
      "buffer_load_dwordx4 v105, s[28:31], s50 offen  lds\n\t"
      "s_add_u32 m0, s49, 65536\n\t"
      "s_mov_b32 s50, 1024\n\t"
      "buffer_load_dwordx4 v102, s[28:31], s50 offen  lds\n\t"
      "s_add_u32 m0, s49, 69632\n\t"
      "s_mov_b32 s50, 66560\n\t"
      "buffer_load_dwordx4 v102, s[28:31], s50 offen  lds\n\t"
      "s_add_u32 m0, s49, 73728\n\t"
      "s_mov_b32 s50, 132096\n\t"
      "buffer_load_dwordx4 v102, s[28:31], s50 offen  lds\n\t"
      "s_add_u32 m0, s49, 77824\n\t"
      "s_mov_b32 s50, 197632\n\t"
      "buffer_load_dwordx4 v102, s[28:31], s50 offen  lds\n\t"
      "s_add_u32 m0, s49, 81920\n\t"
      "s_mov_b32 s50, 263168\n\t"
      "buffer_load_dwordx4 v102, s[28:31], s50 offen  lds\n\t"
      "s_add_u32 m0, s49, 86016\n\t"
      "s_mov_b32 s50, 328704\n\t"
      "buffer_load_dwordx4 v102, s[28:31], s50 offen  lds\n\t"
      "s_add_u32 m0, s49, 135168\n\t"
      "s_mov_b32 s50, 1376256\n\t"
      "buffer_load_dwordx4 v109, s[36:39], s50 offen  lds\n\t"
      "s_waitcnt vmcnt(16)\n\t"
      "s_barrier\n\t"
      "s_add_u32 m0, s49, 90112\n\t"
      "s_mov_b32 s50, 394240\n\t"
      "buffer_load_dwordx4 v102, s[28:31], s50 offen  lds\n\t"
      "s_add_u32 m0, s49, 94208\n\t"
      "s_mov_b32 s50, 459776\n\t"
      "buffer_load_dwordx4 v106, s[28:31], s50 offen  lds\n\t"
      "s_add_u32 m0, s49, 98304\n\t"
      "s_mov_b32 s50, 1024\n\t"
      "buffer_load_dwordx4 v103, s[28:31], s50 offen  lds\n\t"
      "s_add_u32 m0, s49, 102400\n\t"
      "s_mov_b32 s50, 66560\n\t"
      "buffer_load_dwordx4 v103, s[28:31], s50 offen  lds\n\t"
      "s_add_u32 m0, s49, 106496\n\t"
      "s_mov_b32 s50, 132096\n\t"
      "buffer_load_dwordx4 v103, s[28:31], s50 offen  lds\n\t"
      "s_add_u32 m0, s49, 110592\n\t"
      "s_mov_b32 s50, 197632\n\t"
      "buffer_load_dwordx4 v103, s[28:31], s50 offen  lds\n\t"
      "s_add_u32 m0, s49, 139264\n\t"
      "s_mov_b32 s50, 1376256\n\t"
      "buffer_load_dwordx4 v110, s[36:39], s50 offen  lds\n\t"
      "s_waitcnt vmcnt(14)\n\t"
      "s_barrier\n\t"
      "s_add_u32 m0, s49, 114688\n\t"
      "s_mov_b32 s50, 263168\n\t"
      "buffer_load_dwordx4 v103, s[28:31], s50 offen  lds\n\t"
      "s_add_u32 m0, s49, 118784\n\t"
      "s_mov_b32 s50, 328704\n\t"
      "buffer_load_dwordx4 v103, s[28:31], s50 offen  lds\n\t"
      "s_add_u32 m0, s49, 122880\n\t"
      "s_mov_b32 s50, 394240\n\t"
      "buffer_load_dwordx4 v103, s[28:31], s50 offen  lds\n\t"
      "s_add_u32 m0, s49, 126976\n\t"
      "s_mov_b32 s50, 459776\n\t"
      "buffer_load_dwordx4 v107, s[28:31], s50 offen  lds\n\t"
      "s_add_u32 m0, s49, 0\n\t"
      "s_mov_b32 s50, 1536\n\t"
      "buffer_load_dwordx4 v100, s[28:31], s50 offen  lds\n\t"
      "s_add_u32 m0, s49, 4096\n\t"
      "s_mov_b32 s50, 67072\n\t"
      "buffer_load_dwordx4 v100, s[28:31], s50 offen  lds\n\t"
      "s_add_u32 m0, s49, 8192\n\t"
      "s_mov_b32 s50, 132608\n\t"
      "buffer_load_dwordx4 v100, s[28:31], s50 offen  lds\n\t"
      "s_add_u32 m0, s49, 12288\n\t"
      "s_mov_b32 s50, 198144\n\t"
      "buffer_load_dwordx4 v100, s[28:31], s50 offen  lds\n\t"
      "s_add_u32 m0, s49, 16384\n\t"
      "s_mov_b32 s50, 263680\n\t"
      "buffer_load_dwordx4 v100, s[28:31], s50 offen  lds\n\t"
      "s_add_u32 m0, s49, 20480\n\t"
      "s_mov_b32 s50, 329216\n\t"
      "buffer_load_dwordx4 v100, s[28:31], s50 offen  lds\n\t"
      "s_add_u32 m0, s49, 143360\n\t"
      "s_mov_b32 s50, 1376256\n\t"
      "buffer_load_dwordx4 v111, s[36:39], s50 offen  lds\n\t"
      "s_waitcnt vmcnt(16)\n\t"
      "s_barrier\n\t"
      "s_add_u32 m0, s49, 24576\n\t"
      "s_mov_b32 s50, 394752\n\t"
      "buffer_load_dwordx4 v100, s[28:31], s50 offen  lds\n\t"
      "s_add_u32 m0, s49, 28672\n\t"
      "s_mov_b32 s50, 460288\n\t"
      "buffer_load_dwordx4 v104, s[28:31], s50 offen  lds\n\t"
      "s_add_u32 m0, s49, 32768\n\t"
      "s_mov_b32 s50, 1536\n\t"
      "buffer_load_dwordx4 v101, s[28:31], s50 offen  lds\n\t"
      "s_add_u32 m0, s49, 36864\n\t"
      "s_mov_b32 s50, 1536\n\t"
      "buffer_load_dwordx4 v102, s[28:31], s50 offen  lds\n\t"
      "s_add_u32 m0, s49, 40960\n\t"
      "s_mov_b32 s50, 1536\n\t"
      "buffer_load_dwordx4 v103, s[28:31], s50 offen  lds\n\t"
      "s_add_u32 m0, s49, 45056\n\t"
      "s_mov_b32 s50, 67072\n\t"
      "buffer_load_dwordx4 v101, s[28:31], s50 offen  lds\n\t"
      "s_waitcnt vmcnt(13)\n\t"
      "s_barrier\n\t"
      "s_add_u32 m0, s49, 49152\n\t"
      "s_mov_b32 s50, 67072\n\t"
      "buffer_load_dwordx4 v102, s[28:31], s50 offen  lds\n\t"
      "s_add_u32 m0, s49, 53248\n\t"
      "s_mov_b32 s50, 67072\n\t"
      "buffer_load_dwordx4 v103, s[28:31], s50 offen  lds\n\t"
      "s_add_u32 m0, s49, 57344\n\t"
      "s_mov_b32 s50, 132608\n\t"
      "buffer_load_dwordx4 v101, s[28:31], s50 offen  lds\n\t"
      "s_add_u32 m0, s49, 61440\n\t"
      "s_mov_b32 s50, 132608\n\t"
      "buffer_load_dwordx4 v102, s[28:31], s50 offen  lds\n\t"
      "s_add_u32 m0, s49, 65536\n\t"
      "s_mov_b32 s50, 132608\n\t"
      "buffer_load_dwordx4 v103, s[28:31], s50 offen  lds\n\t"
      "s_add_u32 m0, s49, 69632\n\t"
      "s_mov_b32 s50, 198144\n\t"
      "buffer_load_dwordx4 v101, s[28:31], s50 offen  lds\n\t"
      "s_add_u32 m0, s49, 73728\n\t"
      "s_mov_b32 s50, 198144\n\t"
      "buffer_load_dwordx4 v102, s[28:31], s50 offen  lds\n\t"
      "s_add_u32 m0, s49, 77824\n\t"
      "s_mov_b32 s50, 198144\n\t"
      "buffer_load_dwordx4 v103, s[28:31], s50 offen  lds\n\t"
      "s_add_u32 m0, s49, 81920\n\t"
      "s_mov_b32 s50, 263680\n\t"
      "buffer_load_dwordx4 v101, s[28:31], s50 offen  lds\n\t"
      "s_add_u32 m0, s49, 86016\n\t"
      "s_mov_b32 s50, 263680\n\t"
      "buffer_load_dwordx4 v102, s[28:31], s50 offen  lds\n\t"
      "s_waitcnt vmcnt(14)\n\t"
      "s_barrier\n\t"
      "s_add_u32 m0, s49, 90112\n\t"
      "s_mov_b32 s50, 263680\n\t"
      "buffer_load_dwordx4 v103, s[28:31], s50 offen  lds\n\t"
      "s_add_u32 m0, s49, 94208\n\t"
      "s_mov_b32 s50, 329216\n\t"
      "buffer_load_dwordx4 v101, s[28:31], s50 offen  lds\n\t"
      "s_add_u32 m0, s49, 98304\n\t"
      "s_mov_b32 s50, 329216\n\t"
      "buffer_load_dwordx4 v102, s[28:31], s50 offen  lds\n\t"
      "s_add_u32 m0, s49, 102400\n\t"
      "s_mov_b32 s50, 329216\n\t"
      "buffer_load_dwordx4 v103, s[28:31], s50 offen  lds\n\t"
      "s_add_u32 m0, s49, 106496\n\t"
      "s_mov_b32 s50, 394752\n\t"
      "buffer_load_dwordx4 v101, s[28:31], s50 offen  lds\n\t"
      "s_add_u32 m0, s49, 110592\n\t"
      "s_mov_b32 s50, 460288\n\t"
      "buffer_load_dwordx4 v105, s[28:31], s50 offen  lds\n\t"
      "s_waitcnt vmcnt(17)\n\t"
      "s_barrier\n\t"
      "s_add_u32 m0, s49, 114688\n\t"
      "s_mov_b32 s50, 394752\n\t"
      "buffer_load_dwordx4 v102, s[28:31], s50 offen  lds\n\t"
      "s_add_u32 m0, s49, 118784\n\t"
      "s_mov_b32 s50, 460288\n\t"
      "buffer_load_dwordx4 v106, s[28:31], s50 offen  lds\n\t"
      "s_add_u32 m0, s49, 122880\n\t"
      "s_mov_b32 s50, 394752\n\t"
      "buffer_load_dwordx4 v103, s[28:31], s50 offen  lds\n\t"
      "s_add_u32 m0, s49, 126976\n\t"
      "s_mov_b32 s50, 460288\n\t"
      "buffer_load_dwordx4 v107, s[28:31], s50 offen  lds\n\t"
      "s_waitcnt vmcnt(14)\n\t"
      "s_barrier\n\t"
      "s_waitcnt vmcnt(11)\n\t"
      "s_barrier\n\t"
      "s_waitcnt vmcnt(0)\n\t"
      "ds_read_b128 v[12:15], v9 offset:0\n\t"
      "ds_read_b128 v[16:19], v9 offset:4096\n\t"
      "s_mov_b32 s50, 0\n\t"
      "s_waitcnt lgkmcnt(1)\n\t"
      "buffer_store_dwordx4 v[12:15], v8, s[44:47], s50 offen sc1\n\t"
      "s_mov_b32 s50, 114688\n\t"
      "s_waitcnt lgkmcnt(0)\n\t"
      "buffer_store_dwordx4 v[16:19], v8, s[44:47], s50 offen sc1\n\t"
      "s_barrier\n\t"
      "ds_read_b128 v[20:23], v9 offset:8192\n\t"
      "ds_read_b128 v[24:27], v9 offset:12288\n\t"
      "s_mov_b32 s50, 229376\n\t"
      "s_waitcnt lgkmcnt(1)\n\t"
      "buffer_store_dwordx4 v[20:23], v8, s[44:47], s50 offen sc1\n\t"
      "s_mov_b32 s50, 344064\n\t"
      "s_waitcnt lgkmcnt(0)\n\t"
      "buffer_store_dwordx4 v[24:27], v8, s[44:47], s50 offen sc1\n\t"
      "s_barrier\n\t"
      "ds_read_b128 v[28:31], v9 offset:16384\n\t"
      "ds_read_b128 v[32:35], v9 offset:20480\n\t"
      "s_mov_b32 s50, 458752\n\t"
      "s_waitcnt lgkmcnt(1)\n\t"
      "buffer_store_dwordx4 v[28:31], v8, s[44:47], s50 offen sc1\n\t"
      "s_mov_b32 s50, 573440\n\t"
      "s_waitcnt lgkmcnt(0)\n\t"
      "buffer_store_dwordx4 v[32:35], v8, s[44:47], s50 offen sc1\n\t"
      "s_barrier\n\t"
      "ds_read_b128 v[36:39], v9 offset:24576\n\t"
      "ds_read_b128 v[40:43], v9 offset:28672\n\t"
      "s_mov_b32 s50, 688128\n\t"
      "s_waitcnt lgkmcnt(1)\n\t"
      "buffer_store_dwordx4 v[36:39], v8, s[44:47], s50 offen sc1\n\t"
      "s_mov_b32 s50, 802816\n\t"
      "s_waitcnt lgkmcnt(0)\n\t"
      "buffer_store_dwordx4 v[40:43], v8, s[44:47], s50 offen sc1\n\t"
      "s_barrier\n\t"
      "ds_read_b128 v[44:47], v9 offset:32768\n\t"
      "ds_read_b128 v[48:51], v9 offset:36864\n\t"
      "s_mov_b32 s50, 917504\n\t"
      "s_waitcnt lgkmcnt(1)\n\t"
      "buffer_store_dwordx4 v[44:47], v8, s[44:47], s50 offen sc1\n\t"
      "s_mov_b32 s50, 1032192\n\t"
      "s_waitcnt lgkmcnt(0)\n\t"
      "buffer_store_dwordx4 v[48:51], v8, s[44:47], s50 offen sc1\n\t"
      "s_barrier\n\t"
      "ds_read_b128 v[52:55], v9 offset:40960\n\t"
      "ds_read_b128 v[56:59], v9 offset:45056\n\t"
      "s_mov_b32 s50, 1146880\n\t"
      "s_waitcnt lgkmcnt(1)\n\t"
      "buffer_store_dwordx4 v[52:55], v8, s[44:47], s50 offen sc1\n\t"
      "s_mov_b32 s50, 1261568\n\t"
      "s_waitcnt lgkmcnt(0)\n\t"
      "buffer_store_dwordx4 v[56:59], v8, s[44:47], s50 offen sc1\n\t"
      "s_barrier\n\t"
      "ds_read_b128 v[60:63], v9 offset:49152\n\t"
      "ds_read_b128 v[64:67], v9 offset:53248\n\t"
      "ds_read_b128 v[68:71], v9 offset:57344\n\t"
      "s_mov_b32 s50, 1376256\n\t"
      "s_waitcnt lgkmcnt(2)\n\t"
      "buffer_store_dwordx4 v[60:63], v8, s[44:47], s50 offen sc1\n\t"
      "s_mov_b32 s50, 1490944\n\t"
      "s_waitcnt lgkmcnt(1)\n\t"
      "buffer_store_dwordx4 v[64:67], v8, s[44:47], s50 offen sc1\n\t"
      "s_mov_b32 s50, 1605632\n\t"
      "s_waitcnt lgkmcnt(0)\n\t"
      "buffer_store_dwordx4 v[68:71], v8, s[44:47], s50 offen sc1\n\t"
      "s_endpgm\n\t"
      ".p2align 12\n\t"
      "LC14_%=:\n\t"
      "LH14x0_%=:\n\t"
      "s_cbranch_execz LH14x4_%=\n\t"
      "s_barrier\n\t"
      "ds_read_b128 v[140:143], v6 offset:0\n\t"
      "ds_read_b128 v[136:139], v7 offset:0\n\t"
      "ds_read_b128 v[132:135], v6 offset:2048\n\t"
      "ds_read_b128 v[128:131], v7 offset:2048\n\t"
      "ds_read_b128 v[124:127], v6 offset:4096\n\t"
      "ds_read_b128 v[120:123], v7 offset:4096\n\t"
      "s_waitcnt lgkmcnt(5)\n\t"
      "v_mfma_f32_16x16x32_bf16 v[144:147], v[40:43], v[140:143], 0\n\t"
      "ds_read_b128 v[116:119], v6 offset:6144\n\t"
      "s_waitcnt lgkmcnt(5)\n\t"
      "v_mfma_f32_16x16x32_bf16 v[144:147], v[48:51], v[136:139], v[144:147]\n\t"
      "ds_read_b128 v[112:115], v7 offset:6144\n\t"
      "v_lshlrev_b32 v28, 16, v76\n\t"
      "v_and_b32 v29, 0xffff0000, v76\n\t"
      "v_mfma_f32_16x16x32_bf16 v[148:151], v[52:55], v[136:139], 0\n\t"
      "v_lshlrev_b32 v30, 16, v77\n\t"
      "v_and_b32 v31, 0xffff0000, v77\n\t"
      "v_mfma_f32_16x16x32_bf16 v[148:151], v[44:47], v[140:143], v[148:151]\n\t"
      "v_mul_f32 v20, 0x4b800000, v28\n\t"
      "v_mul_f32 v24, 0x49800000, v28\n\t"
      "s_waitcnt lgkmcnt(5)\n\t"
      "v_mfma_f32_16x16x32_bf16 v[156:159], v[44:47], v[132:135], 0\n\t"
      "v_mul_f32 v22, v28, v30\n\t"
      "s_waitcnt lgkmcnt(4)\n\t"
      "v_mfma_f32_16x16x32_bf16 v[156:159], v[52:55], v[128:131], v[156:159]\n\t"
      "s_barrier\n\t"
      "v_mfma_f32_16x16x32_bf16 v[152:155], v[48:51], v[128:131], 0\n\t"
      "ds_read_b128 v[108:111], v6 offset:8192\n\t"
      "v_mul_f32 v21, 0x4b800000, v29\n\t"
      "v_mfma_f32_16x16x32_bf16 v[152:155], v[40:43], v[132:135], v[152:155]\n\t"
      "ds_read_b128 v[104:107], v7 offset:8192\n\t"
      "v_mul_f32 v25, 0x49800000, v29\n\t"
      "s_waitcnt lgkmcnt(2)\n\t"
      "v_mfma_f32_16x16x32_bf16 v[160:163], v[40:43], v[124:127], 0\n\t"
      "ds_read_b128 v[100:103], v6 offset:10240\n\t"
      "v_mul_f32 v23, v29, v31\n\t"
      "v_mfma_f32_16x16x32_bf16 v[160:163], v[48:51], v[120:123], v[160:163]\n\t"
      "ds_read_b128 v[96:99], v7 offset:10240\n\t"
      "v_lshrrev_b32 v31, 8, v72\n\t"
      "v_and_b32 v28, v13, v72\n\t"
      "v_mfma_f32_16x16x32_bf16 v[164:167], v[52:55], v[120:123], 0\n\t"
      "ds_read_b128 v[92:95], v6 offset:12288\n\t"
      "v_and_b32 v29, v14, v72\n\t"
      "v_mfma_f32_16x16x32_bf16 v[164:167], v[44:47], v[124:127], v[164:167]\n\t"
      "v_and_b32 v30, v13, v31\n\t"
      "v_and_b32 v31, v14, v31\n\t"
      "v_fma_mix_f32 v32, v28, v20, -v22 op_sel:[0,0,0] op_sel_hi:[1,0,0]\n\t"
      "v_mfma_f32_16x16x32_bf16 v[172:175], v[44:47], v[116:119], 0\n\t"
      "ds_read_b128 v[88:91], v7 offset:12288\n\t"
      "v_fma_mix_f32 v33, v29, v24, -v22 op_sel:[0,0,0] op_sel_hi:[1,0,0]\n\t"
      "v_mfma_f32_16x16x32_bf16 v[172:175], v[52:55], v[112:115], v[172:175]\n\t"
      "v_fma_mix_f32 v34, v30, v20, -v22 op_sel:[0,0,0] op_sel_hi:[1,0,0]\n\t"
      "v_fma_mix_f32 v35, v31, v24, -v22 op_sel:[0,0,0] op_sel_hi:[1,0,0]\n\t"
      "v_fma_mix_f32 v36, v28, v20, -v22 op_sel:[1,0,0] op_sel_hi:[1,0,0]\n\t"
      "v_mfma_f32_16x16x32_bf16 v[168:171], v[48:51], v[112:115], 0\n\t"
      "ds_read_b128 v[84:87], v6 offset:14336\n\t"
      "v_fma_mix_f32 v37, v29, v24, -v22 op_sel:[1,0,0] op_sel_hi:[1,0,0]\n\t"
      "v_mfma_f32_16x16x32_bf16 v[168:171], v[40:43], v[116:119], v[168:171]\n\t"
      "v_cvt_pk_bf16_f32 v56, v32, v33\n\t"
      "v_cvt_pk_bf16_f32 v57, v34, v35\n\t"
      "s_waitcnt lgkmcnt(3)\n\t"
      "v_mfma_f32_16x16x32_bf16 v[176:179], v[40:43], v[108:111], 0\n\t"
      "ds_read_b128 v[80:83], v7 offset:14336\n\t"
      "v_fma_mix_f32 v38, v30, v20, -v22 op_sel:[1,0,0] op_sel_hi:[1,0,0]\n\t"
      "v_mfma_f32_16x16x32_bf16 v[176:179], v[48:51], v[104:107], v[176:179]\n\t"
      "s_barrier\n\t"
      "v_mfma_f32_16x16x32_bf16 v[180:183], v[52:55], v[104:107], 0\n\t"
      "ds_read_b128 v[136:139], v6 offset:16384\n\t"
      "v_fma_mix_f32 v39, v31, v24, -v22 op_sel:[1,0,0] op_sel_hi:[1,0,0]\n\t"
      "v_mfma_f32_16x16x32_bf16 v[180:183], v[44:47], v[108:111], v[180:183]\n\t"
      "ds_read_b128 v[140:143], v7 offset:16384\n\t"
      "v_cvt_pk_bf16_f32 v58, v36, v37\n\t"
      "v_cvt_pk_bf16_f32 v59, v38, v39\n\t"
      "v_mfma_f32_16x16x32_bf16 v[188:191], v[44:47], v[100:103], 0\n\t"
      "ds_read_b128 v[128:131], v6 offset:18432\n\t"
      "v_lshrrev_b32 v31, 8, v73\n\t"
      "v_mfma_f32_16x16x32_bf16 v[188:191], v[52:55], v[96:99], v[188:191]\n\t"
      "v_and_b32 v28, v13, v73\n\t"
      "v_and_b32 v29, v14, v73\n\t"
      "v_and_b32 v30, v13, v31\n\t"
      "v_mfma_f32_16x16x32_bf16 v[184:187], v[48:51], v[96:99], 0\n\t"
      "ds_read_b128 v[132:135], v7 offset:18432\n\t"
      "v_and_b32 v31, v14, v31\n\t"
      "v_mfma_f32_16x16x32_bf16 v[184:187], v[40:43], v[100:103], v[184:187]\n\t"
      "v_fma_mix_f32 v32, v28, v21, -v23 op_sel:[0,0,0] op_sel_hi:[1,0,0]\n\t"
      "v_fma_mix_f32 v33, v29, v25, -v23 op_sel:[0,0,0] op_sel_hi:[1,0,0]\n\t"
      "s_waitcnt lgkmcnt(4)\n\t"
      "v_mfma_f32_16x16x32_bf16 v[192:195], v[40:43], v[92:95], 0\n\t"
      "ds_read_b128 v[120:123], v6 offset:20480\n\t"
      "v_fma_mix_f32 v34, v30, v21, -v23 op_sel:[0,0,0] op_sel_hi:[1,0,0]\n\t"
      "v_mfma_f32_16x16x32_bf16 v[192:195], v[48:51], v[88:91], v[192:195]\n\t"
      "v_fma_mix_f32 v35, v31, v25, -v23 op_sel:[0,0,0] op_sel_hi:[1,0,0]\n\t"
      "v_fma_mix_f32 v36, v28, v21, -v23 op_sel:[1,0,0] op_sel_hi:[1,0,0]\n\t"
      "v_fma_mix_f32 v37, v29, v25, -v23 op_sel:[1,0,0] op_sel_hi:[1,0,0]\n\t"
      "v_mfma_f32_16x16x32_bf16 v[196:199], v[52:55], v[88:91], 0\n\t"
      "ds_read_b128 v[124:127], v7 offset:20480\n\t"
      "v_cvt_pk_bf16_f32 v60, v32, v33\n\t"
      "v_mfma_f32_16x16x32_bf16 v[196:199], v[44:47], v[92:95], v[196:199]\n\t"
      "v_cvt_pk_bf16_f32 v61, v34, v35\n\t"
      "v_fma_mix_f32 v38, v30, v21, -v23 op_sel:[1,0,0] op_sel_hi:[1,0,0]\n\t"
      "v_fma_mix_f32 v39, v31, v25, -v23 op_sel:[1,0,0] op_sel_hi:[1,0,0]\n\t"
      "v_mfma_f32_16x16x32_bf16 v[204:207], v[44:47], v[84:87], 0\n\t"
      "ds_read_b128 v[112:115], v6 offset:22528\n\t"
      "v_cvt_pk_bf16_f32 v62, v36, v37\n\t"
      "v_mfma_f32_16x16x32_bf16 v[204:207], v[52:55], v[80:83], v[204:207]\n\t"
      "v_cvt_pk_bf16_f32 v63, v38, v39\n\t"
      "v_lshrrev_b32 v31, 8, v74\n\t"
      "v_and_b32 v28, v13, v74\n\t"
      "v_mfma_f32_16x16x32_bf16 v[200:203], v[48:51], v[80:83], 0\n\t"
      "ds_read_b128 v[116:119], v7 offset:22528\n\t"
      "v_and_b32 v29, v14, v74\n\t"
      "v_mfma_f32_16x16x32_bf16 v[200:203], v[40:43], v[84:87], v[200:203]\n\t"
      "v_and_b32 v30, v13, v31\n\t"
      "v_and_b32 v31, v14, v31\n\t"
      "s_waitcnt lgkmcnt(3)\n\t"
      "v_mfma_f32_16x16x32_bf16 v[208:211], v[40:43], v[136:139], 0\n\t"
      "v_fma_mix_f32 v32, v28, v20, -v22 op_sel:[0,0,0] op_sel_hi:[1,0,0]\n\t"
      "ds_read_b64 v[16:17], v10 offset:8192\n\t"
      "v_mfma_f32_16x16x32_bf16 v[208:211], v[48:51], v[140:143], v[208:211]\n\t"
      "s_barrier\n\t"
      "v_mfma_f32_16x16x32_bf16 v[212:215], v[52:55], v[140:143], 0\n\t"
      "ds_read_b128 v[104:107], v6 offset:24576\n\t"
      "ds_read_b64 v[18:19], v10 offset:10240\n\t"
      "v_mfma_f32_16x16x32_bf16 v[212:215], v[44:47], v[136:139], v[212:215]\n\t"
      "ds_read_b128 v[108:111], v7 offset:24576\n\t"
      "ds_read2st64_b32 v[26:27], v11 offset0:2 offset1:18\n\t"
      "v_fma_mix_f32 v33, v29, v24, -v22 op_sel:[0,0,0] op_sel_hi:[1,0,0]\n\t"
      "v_mfma_f32_16x16x32_bf16 v[220:223], v[44:47], v[128:131], 0\n\t"
      "ds_read_b128 v[96:99], v6 offset:26624\n\t"
      "v_fma_mix_f32 v34, v30, v20, -v22 op_sel:[0,0,0] op_sel_hi:[1,0,0]\n\t"
      "v_mfma_f32_16x16x32_bf16 v[220:223], v[52:55], v[132:135], v[220:223]\n\t"
      "ds_read_b128 v[100:103], v7 offset:26624\n\t"
      "v_fma_mix_f32 v35, v31, v24, -v22 op_sel:[0,0,0] op_sel_hi:[1,0,0]\n\t"
      "v_mfma_f32_16x16x32_bf16 v[216:219], v[48:51], v[132:135], 0\n\t"
      "ds_read_b128 v[88:91], v6 offset:28672\n\t"
      "v_fma_mix_f32 v36, v28, v20, -v22 op_sel:[1,0,0] op_sel_hi:[1,0,0]\n\t"
      "v_mfma_f32_16x16x32_bf16 v[216:219], v[40:43], v[128:131], v[216:219]\n\t"
      "v_fma_mix_f32 v37, v29, v24, -v22 op_sel:[1,0,0] op_sel_hi:[1,0,0]\n\t"
      "v_cvt_pk_bf16_f32 v64, v32, v33\n\t"
      "v_cvt_pk_bf16_f32 v65, v34, v35\n\t"
      "v_mfma_f32_16x16x32_bf16 v[224:227], v[40:43], v[120:123], 0\n\t"
      "ds_read_b128 v[92:95], v7 offset:28672\n\t"
      "s_waitcnt lgkmcnt(6)\n\t"
      "v_mfma_f32_16x16x32_bf16 v[224:227], v[48:51], v[124:127], v[224:227]\n\t"
      "v_fma_mix_f32 v38, v30, v20, -v22 op_sel:[1,0,0] op_sel_hi:[1,0,0]\n\t"
      "v_fma_mix_f32 v39, v31, v24, -v22 op_sel:[1,0,0] op_sel_hi:[1,0,0]\n\t"
      "v_cvt_pk_bf16_f32 v66, v36, v37\n\t"
      "v_mfma_f32_16x16x32_bf16 v[228:231], v[52:55], v[124:127], 0\n\t"
      "ds_read_b128 v[80:83], v6 offset:30720\n\t"
      "v_cvt_pk_bf16_f32 v67, v38, v39\n\t"
      "v_mfma_f32_16x16x32_bf16 v[228:231], v[44:47], v[120:123], v[228:231]\n\t"
      "v_lshrrev_b32 v31, 8, v75\n\t"
      "v_and_b32 v28, v13, v75\n\t"
      "v_mfma_f32_16x16x32_bf16 v[236:239], v[44:47], v[112:115], 0\n\t"
      "ds_read_b128 v[84:87], v7 offset:30720\n\t"
      "v_and_b32 v29, v14, v75\n\t"
      "v_mfma_f32_16x16x32_bf16 v[236:239], v[52:55], v[116:119], v[236:239]\n\t"
      "s_barrier\n\t"
      "v_mfma_f32_16x16x32_bf16 v[232:235], v[48:51], v[116:119], 0\n\t"
      "ds_read_b128 v[140:143], v6 offset:32768\n\t"
      "v_and_b32 v30, v13, v31\n\t"
      "v_mfma_f32_16x16x32_bf16 v[232:235], v[40:43], v[112:115], v[232:235]\n\t"
      "v_and_b32 v31, v14, v31\n\t"
      "v_fma_mix_f32 v32, v28, v21, -v23 op_sel:[0,0,0] op_sel_hi:[1,0,0]\n\t"
      "v_mfma_f32_16x16x32_bf16 v[240:243], v[40:43], v[104:107], 0\n\t"
      "ds_read_b128 v[136:139], v7 offset:32768\n\t"
      "s_waitcnt lgkmcnt(3)\n\t"
      "v_mfma_f32_16x16x32_bf16 v[240:243], v[48:51], v[108:111], v[240:243]\n\t"
      "ds_read_b128 v[132:135], v6 offset:34816\n\t"
      "v_fma_mix_f32 v33, v29, v25, -v23 op_sel:[0,0,0] op_sel_hi:[1,0,0]\n\t"
      "v_mfma_f32_16x16x32_bf16 v[244:247], v[52:55], v[108:111], 0\n\t"
      "ds_read_b128 v[128:131], v7 offset:34816\n\t"
      "v_fma_mix_f32 v34, v30, v21, -v23 op_sel:[0,0,0] op_sel_hi:[1,0,0]\n\t"
      "v_mfma_f32_16x16x32_bf16 v[244:247], v[44:47], v[104:107], v[244:247]\n\t"
      "v_fma_mix_f32 v35, v31, v25, -v23 op_sel:[0,0,0] op_sel_hi:[1,0,0]\n\t"
      "v_fma_mix_f32 v36, v28, v21, -v23 op_sel:[1,0,0] op_sel_hi:[1,0,0]\n\t"
      "v_fma_mix_f32 v37, v29, v25, -v23 op_sel:[1,0,0] op_sel_hi:[1,0,0]\n\t"
      "v_mfma_f32_16x16x32_bf16 v[252:255], v[44:47], v[96:99], 0\n\t"
      "ds_read_b128 v[124:127], v6 offset:36864\n\t"
      "v_cvt_pk_bf16_f32 v68, v32, v33\n\t"
      "v_mfma_f32_16x16x32_bf16 v[252:255], v[52:55], v[100:103], v[252:255]\n\t"
      "v_cvt_pk_bf16_f32 v69, v34, v35\n\t"
      "v_fma_mix_f32 v38, v30, v21, -v23 op_sel:[1,0,0] op_sel_hi:[1,0,0]\n\t"
      "v_mfma_f32_16x16x32_bf16 v[248:251], v[48:51], v[100:103], 0\n\t"
      "ds_read_b128 v[120:123], v7 offset:36864\n\t"
      "v_fma_mix_f32 v39, v31, v25, -v23 op_sel:[1,0,0] op_sel_hi:[1,0,0]\n\t"
      "v_mfma_f32_16x16x32_bf16 v[248:251], v[40:43], v[96:99], v[248:251]\n\t"
      "v_cvt_pk_bf16_f32 v70, v36, v37\n\t"
      "v_cvt_pk_bf16_f32 v71, v38, v39\n\t"
      "v_mfma_f32_16x16x32_bf16 v[144:147], v[56:59], v[88:91], v[144:147]\n\t"
      "ds_read_b128 v[116:119], v6 offset:38912\n\t"
      "v_lshlrev_b32 v28, 16, v26\n\t"
      "v_mfma_f32_16x16x32_bf16 v[144:147], v[64:67], v[92:95], v[144:147]\n\t"
      "v_and_b32 v29, 0xffff0000, v26\n\t"
      "v_lshlrev_b32 v30, 16, v27\n\t"
      "v_and_b32 v31, 0xffff0000, v27\n\t"
      "v_mfma_f32_16x16x32_bf16 v[148:151], v[68:71], v[92:95], v[148:151]\n\t"
      "ds_read_b128 v[112:115], v7 offset:38912\n\t"
      "v_mul_f32 v20, 0x4b800000, v28\n\t"
      "v_mfma_f32_16x16x32_bf16 v[148:151], v[60:63], v[88:91], v[148:151]\n\t"
      "v_mul_f32 v24, 0x49800000, v28\n\t"
      "v_mul_f32 v22, v28, v30\n\t"
      "v_mul_f32 v21, 0x4b800000, v29\n\t"
      "v_mfma_f32_16x16x32_bf16 v[156:159], v[60:63], v[80:83], v[156:159]\n\t"
      "ds_read_b128 v[108:111], v6 offset:40960\n\t"
      "s_waitcnt lgkmcnt(3)\n\t"
      "v_mfma_f32_16x16x32_bf16 v[156:159], v[68:71], v[84:87], v[156:159]\n\t"
      "v_mul_f32 v25, 0x49800000, v29\n\t"
      "v_mul_f32 v23, v29, v31\n\t"
      "ds_read2st64_b32 v[26:27], v11 offset0:3 offset1:19\n\t"
      "v_mfma_f32_16x16x32_bf16 v[152:155], v[64:67], v[84:87], v[152:155]\n\t"
      "ds_read_b128 v[104:107], v7 offset:40960\n\t"
      "v_lshrrev_b32 v31, 8, v16\n\t"
      "v_mfma_f32_16x16x32_bf16 v[152:155], v[56:59], v[80:83], v[152:155]\n\t"
      "v_and_b32 v28, v13, v16\n\t"
      "v_and_b32 v29, v14, v16\n\t"
      "v_and_b32 v30, v13, v31\n\t"
      "v_mfma_f32_16x16x32_bf16 v[160:163], v[56:59], v[140:143], v[160:163]\n\t"
      "ds_read_b128 v[100:103], v6 offset:43008\n\t"
      "v_and_b32 v31, v14, v31\n\t"
      "v_mfma_f32_16x16x32_bf16 v[160:163], v[64:67], v[136:139], v[160:163]\n\t"
      "ds_read_b128 v[96:99], v7 offset:43008\n\t"
      "v_fma_mix_f32 v32, v28, v20, -v22 op_sel:[0,0,0] op_sel_hi:[1,0,0]\n\t"
      "v_fma_mix_f32 v33, v29, v24, -v22 op_sel:[0,0,0] op_sel_hi:[1,0,0]\n\t"
      "v_mfma_f32_16x16x32_bf16 v[164:167], v[68:71], v[136:139], v[164:167]\n\t"
      "v_fma_mix_f32 v34, v30, v20, -v22 op_sel:[0,0,0] op_sel_hi:[1,0,0]\n\t"
      "v_fma_mix_f32 v35, v31, v24, -v22 op_sel:[0,0,0] op_sel_hi:[1,0,0]\n\t"
      "v_mfma_f32_16x16x32_bf16 v[164:167], v[60:63], v[140:143], v[164:167]\n\t"
      "v_fma_mix_f32 v36, v28, v20, -v22 op_sel:[1,0,0] op_sel_hi:[1,0,0]\n\t"
      "v_fma_mix_f32 v37, v29, v24, -v22 op_sel:[1,0,0] op_sel_hi:[1,0,0]\n\t"
      "v_mfma_f32_16x16x32_bf16 v[172:175], v[60:63], v[132:135], v[172:175]\n\t"
      "v_cvt_pk_bf16_f32 v40, v32, v33\n\t"
      "v_cvt_pk_bf16_f32 v41, v34, v35\n\t"
      "v_mfma_f32_16x16x32_bf16 v[172:175], v[68:71], v[128:131], v[172:175]\n\t"
      "s_barrier\n\t"
      "v_mfma_f32_16x16x32_bf16 v[168:171], v[64:67], v[128:131], v[168:171]\n\t"
      "ds_read_b128 v[92:95], v6 offset:45056\n\t"
      "v_fma_mix_f32 v38, v30, v20, -v22 op_sel:[1,0,0] op_sel_hi:[1,0,0]\n\t"
      "v_mfma_f32_16x16x32_bf16 v[168:171], v[56:59], v[132:135], v[168:171]\n\t"
      "ds_read_b128 v[88:91], v7 offset:45056\n\t"
      "v_fma_mix_f32 v39, v31, v24, -v22 op_sel:[1,0,0] op_sel_hi:[1,0,0]\n\t"
      "v_mfma_f32_16x16x32_bf16 v[176:179], v[56:59], v[124:127], v[176:179]\n\t"
      "ds_read_b128 v[84:87], v6 offset:47104\n\t"
      "v_cvt_pk_bf16_f32 v42, v36, v37\n\t"
      "v_mfma_f32_16x16x32_bf16 v[176:179], v[64:67], v[120:123], v[176:179]\n\t"
      "ds_read_b128 v[80:83], v7 offset:47104\n\t"
      "v_cvt_pk_bf16_f32 v43, v38, v39\n\t"
      "v_mfma_f32_16x16x32_bf16 v[180:183], v[68:71], v[120:123], v[180:183]\n\t"
      "ds_read_b128 v[136:139], v6 offset:49152\n\t"
      "v_lshrrev_b32 v31, 8, v17\n\t"
      "v_mfma_f32_16x16x32_bf16 v[180:183], v[60:63], v[124:127], v[180:183]\n\t"
      "v_and_b32 v28, v13, v17\n\t"
      "s_waitcnt lgkmcnt(5)\n\t"
      "v_mfma_f32_16x16x32_bf16 v[188:191], v[60:63], v[116:119], v[188:191]\n\t"
      "ds_read_b128 v[140:143], v7 offset:49152\n\t"
      "v_and_b32 v29, v14, v17\n\t"
      "v_mfma_f32_16x16x32_bf16 v[188:191], v[68:71], v[112:115], v[188:191]\n\t"
      "v_and_b32 v30, v13, v31\n\t"
      "v_and_b32 v31, v14, v31\n\t"
      "ds_read_b64 v[16:17], v10 offset:12288\n\t"
      "v_mfma_f32_16x16x32_bf16 v[184:187], v[64:67], v[112:115], v[184:187]\n\t"
      "ds_read_b128 v[128:131], v6 offset:51200\n\t"
      "v_fma_mix_f32 v32, v28, v21, -v23 op_sel:[0,0,0] op_sel_hi:[1,0,0]\n\t"
      "v_mfma_f32_16x16x32_bf16 v[184:187], v[56:59], v[116:119], v[184:187]\n\t"
      "v_fma_mix_f32 v33, v29, v25, -v23 op_sel:[0,0,0] op_sel_hi:[1,0,0]\n\t"
      "v_fma_mix_f32 v34, v30, v21, -v23 op_sel:[0,0,0] op_sel_hi:[1,0,0]\n\t"
      "v_mfma_f32_16x16x32_bf16 v[192:195], v[56:59], v[108:111], v[192:195]\n\t"
      "ds_read_b128 v[132:135], v7 offset:51200\n\t"
      "v_fma_mix_f32 v35, v31, v25, -v23 op_sel:[0,0,0] op_sel_hi:[1,0,0]\n\t"
      "v_mfma_f32_16x16x32_bf16 v[192:195], v[64:67], v[104:107], v[192:195]\n\t"
      "ds_read_b128 v[120:123], v6 offset:53248\n\t"
      "v_fma_mix_f32 v36, v28, v21, -v23 op_sel:[1,0,0] op_sel_hi:[1,0,0]\n\t"
      "v_mfma_f32_16x16x32_bf16 v[196:199], v[68:71], v[104:107], v[196:199]\n\t"
      "ds_read_b128 v[124:127], v7 offset:53248\n\t"
      "v_fma_mix_f32 v37, v29, v25, -v23 op_sel:[1,0,0] op_sel_hi:[1,0,0]\n\t"
      "v_mfma_f32_16x16x32_bf16 v[196:199], v[60:63], v[108:111], v[196:199]\n\t"
      "v_cvt_pk_bf16_f32 v44, v32, v33\n\t"
      "v_cvt_pk_bf16_f32 v45, v34, v35\n\t"
      "v_mfma_f32_16x16x32_bf16 v[204:207], v[60:63], v[100:103], v[204:207]\n\t"
      "ds_read_b128 v[112:115], v6 offset:55296\n\t"
      "v_fma_mix_f32 v38, v30, v21, -v23 op_sel:[1,0,0] op_sel_hi:[1,0,0]\n\t"
      "v_mfma_f32_16x16x32_bf16 v[204:207], v[68:71], v[96:99], v[204:207]\n\t"
      "v_fma_mix_f32 v39, v31, v25, -v23 op_sel:[1,0,0] op_sel_hi:[1,0,0]\n\t"
      "v_cvt_pk_bf16_f32 v46, v36, v37\n\t"
      "v_mfma_f32_16x16x32_bf16 v[200:203], v[64:67], v[96:99], v[200:203]\n\t"
      "ds_read_b128 v[116:119], v7 offset:55296\n\t"
      "v_cvt_pk_bf16_f32 v47, v38, v39\n\t"
      "v_mfma_f32_16x16x32_bf16 v[200:203], v[56:59], v[100:103], v[200:203]\n\t"
      "v_lshrrev_b32 v31, 8, v18\n\t"
      "s_waitcnt lgkmcnt(3)\n\t"
      "v_mfma_f32_16x16x32_bf16 v[208:211], v[56:59], v[92:95], v[208:211]\n\t"
      "v_and_b32 v28, v13, v18\n\t"
      "v_and_b32 v29, v14, v18\n\t"
      "v_mfma_f32_16x16x32_bf16 v[208:211], v[64:67], v[88:91], v[208:211]\n\t"
      "s_barrier\n\t"
      "v_mfma_f32_16x16x32_bf16 v[212:215], v[68:71], v[88:91], v[212:215]\n\t"
      "ds_read_b128 v[104:107], v6 offset:57344\n\t"
      "v_and_b32 v30, v13, v31\n\t"
      "v_mfma_f32_16x16x32_bf16 v[212:215], v[60:63], v[92:95], v[212:215]\n\t"
      "ds_read_b128 v[108:111], v7 offset:57344\n\t"
      "v_and_b32 v31, v14, v31\n\t"
      "v_mfma_f32_16x16x32_bf16 v[220:223], v[60:63], v[84:87], v[220:223]\n\t"
      "ds_read_b128 v[96:99], v6 offset:59392\n\t"
      "v_fma_mix_f32 v32, v28, v20, -v22 op_sel:[0,0,0] op_sel_hi:[1,0,0]\n\t"
      "v_mfma_f32_16x16x32_bf16 v[220:223], v[68:71], v[80:83], v[220:223]\n\t"
      "v_fma_mix_f32 v33, v29, v24, -v22 op_sel:[0,0,0] op_sel_hi:[1,0,0]\n\t"
      "v_fma_mix_f32 v34, v30, v20, -v22 op_sel:[0,0,0] op_sel_hi:[1,0,0]\n\t"
      "v_fma_mix_f32 v35, v31, v24, -v22 op_sel:[0,0,0] op_sel_hi:[1,0,0]\n\t"
      "v_mfma_f32_16x16x32_bf16 v[216:219], v[64:67], v[80:83], v[216:219]\n\t"
      "ds_read_b128 v[100:103], v7 offset:59392\n\t"
      "v_fma_mix_f32 v36, v28, v20, -v22 op_sel:[1,0,0] op_sel_hi:[1,0,0]\n\t"
      "v_mfma_f32_16x16x32_bf16 v[216:219], v[56:59], v[84:87], v[216:219]\n\t"
      "v_fma_mix_f32 v37, v29, v24, -v22 op_sel:[1,0,0] op_sel_hi:[1,0,0]\n\t"
      "v_cvt_pk_bf16_f32 v48, v32, v33\n\t"
      "v_mfma_f32_16x16x32_bf16 v[224:227], v[56:59], v[136:139], v[224:227]\n\t"
      "ds_read_b128 v[88:91], v6 offset:61440\n\t"
      "v_cvt_pk_bf16_f32 v49, v34, v35\n\t"
      "v_mfma_f32_16x16x32_bf16 v[224:227], v[64:67], v[140:143], v[224:227]\n\t"
      "ds_read_b128 v[92:95], v7 offset:61440\n\t"
      "v_fma_mix_f32 v38, v30, v20, -v22 op_sel:[1,0,0] op_sel_hi:[1,0,0]\n\t"
      "v_mfma_f32_16x16x32_bf16 v[228:231], v[68:71], v[140:143], v[228:231]\n\t"
      "ds_read_b128 v[80:83], v6 offset:63488\n\t"
      "v_fma_mix_f32 v39, v31, v24, -v22 op_sel:[1,0,0] op_sel_hi:[1,0,0]\n\t"
      "v_mfma_f32_16x16x32_bf16 v[228:231], v[60:63], v[136:139], v[228:231]\n\t"
      "v_cvt_pk_bf16_f32 v50, v36, v37\n\t"
      "v_cvt_pk_bf16_f32 v51, v38, v39\n\t"
      "v_mfma_f32_16x16x32_bf16 v[236:239], v[60:63], v[128:131], v[236:239]\n\t"
      "ds_read_b128 v[84:87], v7 offset:63488\n\t"
      "v_lshrrev_b32 v31, 8, v19\n\t"
      "v_mfma_f32_16x16x32_bf16 v[236:239], v[68:71], v[132:135], v[236:239]\n\t"
      "v_and_b32 v28, v13, v19\n\t"
      "v_and_b32 v29, v14, v19\n\t"
      "v_mfma_f32_16x16x32_bf16 v[232:235], v[64:67], v[132:135], v[232:235]\n\t"
      "ds_read_b128 v[140:143], v8 offset:0\n\t"
      "v_and_b32 v30, v13, v31\n\t"
      "v_mfma_f32_16x16x32_bf16 v[232:235], v[56:59], v[128:131], v[232:235]\n\t"
      "v_and_b32 v31, v14, v31\n\t"
      "ds_read_b64 v[18:19], v10 offset:14336\n\t"
      "v_mfma_f32_16x16x32_bf16 v[240:243], v[56:59], v[120:123], v[240:243]\n\t"
      "ds_read_b128 v[136:139], v9 offset:0\n\t"
      "s_waitcnt lgkmcnt(4)\n\t"
      "v_mfma_f32_16x16x32_bf16 v[240:243], v[64:67], v[124:127], v[240:243]\n\t"
      "v_fma_mix_f32 v32, v28, v21, -v23 op_sel:[0,0,0] op_sel_hi:[1,0,0]\n\t"
      "v_fma_mix_f32 v33, v29, v25, -v23 op_sel:[0,0,0] op_sel_hi:[1,0,0]\n\t"
      "v_fma_mix_f32 v34, v30, v21, -v23 op_sel:[0,0,0] op_sel_hi:[1,0,0]\n\t"
      "v_mfma_f32_16x16x32_bf16 v[244:247], v[68:71], v[124:127], v[244:247]\n\t"
      "ds_read_b128 v[132:135], v8 offset:2048\n\t"
      "v_fma_mix_f32 v35, v31, v25, -v23 op_sel:[0,0,0] op_sel_hi:[1,0,0]\n\t"
      "v_mfma_f32_16x16x32_bf16 v[244:247], v[60:63], v[120:123], v[244:247]\n\t"
      "v_fma_mix_f32 v36, v28, v21, -v23 op_sel:[1,0,0] op_sel_hi:[1,0,0]\n\t"
      "v_fma_mix_f32 v37, v29, v25, -v23 op_sel:[1,0,0] op_sel_hi:[1,0,0]\n\t"
      "v_mfma_f32_16x16x32_bf16 v[252:255], v[60:63], v[112:115], v[252:255]\n\t"
      "ds_read_b128 v[128:131], v9 offset:2048\n\t"
      "v_cvt_pk_bf16_f32 v52, v32, v33\n\t"
      "v_mfma_f32_16x16x32_bf16 v[252:255], v[68:71], v[116:119], v[252:255]\n\t"
      "v_cvt_pk_bf16_f32 v53, v34, v35\n\t"
      "v_fma_mix_f32 v38, v30, v21, -v23 op_sel:[1,0,0] op_sel_hi:[1,0,0]\n\t"
      "v_mfma_f32_16x16x32_bf16 v[248:251], v[64:67], v[116:119], v[248:251]\n\t"
      "ds_read_b128 v[124:127], v8 offset:4096\n\t"
      "v_fma_mix_f32 v39, v31, v25, -v23 op_sel:[1,0,0] op_sel_hi:[1,0,0]\n\t"
      "v_mfma_f32_16x16x32_bf16 v[248:251], v[56:59], v[112:115], v[248:251]\n\t"
      "v_cvt_pk_bf16_f32 v54, v36, v37\n\t"
      "v_cvt_pk_bf16_f32 v55, v38, v39\n\t"
      "v_mfma_f32_16x16x32_bf16 v[144:147], v[40:43], v[104:107], v[144:147]\n\t"
      "ds_read_b128 v[120:123], v9 offset:4096\n\t"
      "v_lshlrev_b32 v28, 16, v26\n\t"
      "v_mfma_f32_16x16x32_bf16 v[144:147], v[48:51], v[108:111], v[144:147]\n\t"
      "ds_read_b128 v[116:119], v8 offset:6144\n\t"
      "v_and_b32 v29, 0xffff0000, v26\n\t"
      "v_lshlrev_b32 v30, 16, v27\n\t"
      "v_mfma_f32_16x16x32_bf16 v[148:151], v[52:55], v[108:111], v[148:151]\n\t"
      "ds_read_b128 v[112:115], v9 offset:6144\n\t"
      "v_and_b32 v31, 0xffff0000, v27\n\t"
      "v_mfma_f32_16x16x32_bf16 v[148:151], v[44:47], v[104:107], v[148:151]\n\t"
      "v_mul_f32 v20, 0x4b800000, v28\n\t"
      "v_mul_f32 v24, 0x49800000, v28\n\t"
      "v_mul_f32 v22, v28, v30\n\t"
      "v_mfma_f32_16x16x32_bf16 v[156:159], v[44:47], v[96:99], v[156:159]\n\t"
      "v_mul_f32 v21, 0x4b800000, v29\n\t"
      "v_mul_f32 v25, 0x49800000, v29\n\t"
      "v_mfma_f32_16x16x32_bf16 v[156:159], v[52:55], v[100:103], v[156:159]\n\t"
      "s_barrier\n\t"
      "v_mfma_f32_16x16x32_bf16 v[152:155], v[48:51], v[100:103], v[152:155]\n\t"
      "ds_read_b128 v[108:111], v8 offset:8192\n\t"
      "v_mul_f32 v23, v29, v31\n\t"
      "v_mfma_f32_16x16x32_bf16 v[152:155], v[40:43], v[96:99], v[152:155]\n\t"
      "ds_read_b128 v[104:107], v9 offset:8192\n\t"
      "ds_read2st64_b32 v[26:27], v11 offset0:4 offset1:20\n\t"
      "v_lshrrev_b32 v31, 8, v16\n\t"
      "v_mfma_f32_16x16x32_bf16 v[160:163], v[40:43], v[88:91], v[160:163]\n\t"
      "ds_read_b128 v[100:103], v8 offset:10240\n\t"
      "v_and_b32 v28, v13, v16\n\t"
      "v_mfma_f32_16x16x32_bf16 v[160:163], v[48:51], v[92:95], v[160:163]\n\t"
      "v_and_b32 v29, v14, v16\n\t"
      "v_and_b32 v30, v13, v31\n\t"
      "v_and_b32 v31, v14, v31\n\t"
      "v_mfma_f32_16x16x32_bf16 v[164:167], v[52:55], v[92:95], v[164:167]\n\t"
      "ds_read_b128 v[96:99], v9 offset:10240\n\t"
      "v_fma_mix_f32 v32, v28, v20, -v22 op_sel:[0,0,0] op_sel_hi:[1,0,0]\n\t"
      "v_mfma_f32_16x16x32_bf16 v[164:167], v[44:47], v[88:91], v[164:167]\n\t"
      "v_fma_mix_f32 v33, v29, v24, -v22 op_sel:[0,0,0] op_sel_hi:[1,0,0]\n\t"
      "v_fma_mix_f32 v34, v30, v20, -v22 op_sel:[0,0,0] op_sel_hi:[1,0,0]\n\t"
      "v_mfma_f32_16x16x32_bf16 v[172:175], v[44:47], v[80:83], v[172:175]\n\t"
      "ds_read_b128 v[92:95], v8 offset:12288\n\t"
      "s_waitcnt lgkmcnt(5)\n\t"
      "v_mfma_f32_16x16x32_bf16 v[172:175], v[52:55], v[84:87], v[172:175]\n\t"
      "v_fma_mix_f32 v35, v31, v24, -v22 op_sel:[0,0,0] op_sel_hi:[1,0,0]\n\t"
      "v_fma_mix_f32 v36, v28, v20, -v22 op_sel:[1,0,0] op_sel_hi:[1,0,0]\n\t"
      "v_fma_mix_f32 v37, v29, v24, -v22 op_sel:[1,0,0] op_sel_hi:[1,0,0]\n\t"
      "v_mfma_f32_16x16x32_bf16 v[168:171], v[48:51], v[84:87], v[168:171]\n\t"
      "ds_read_b128 v[88:91], v9 offset:12288\n\t"
      "v_cvt_pk_bf16_f32 v56, v32, v33\n\t"
      "v_mfma_f32_16x16x32_bf16 v[168:171], v[40:43], v[80:83], v[168:171]\n\t"
      "v_cvt_pk_bf16_f32 v57, v34, v35\n\t"
      "v_fma_mix_f32 v38, v30, v20, -v22 op_sel:[1,0,0] op_sel_hi:[1,0,0]\n\t"
      "v_mfma_f32_16x16x32_bf16 v[176:179], v[40:43], v[140:143], v[176:179]\n\t"
      "ds_read_b128 v[84:87], v8 offset:14336\n\t"
      "v_fma_mix_f32 v39, v31, v24, -v22 op_sel:[1,0,0] op_sel_hi:[1,0,0]\n\t"
      "v_mfma_f32_16x16x32_bf16 v[176:179], v[48:51], v[136:139], v[176:179]\n\t"
      "ds_read_b128 v[80:83], v9 offset:14336\n\t"
      "v_cvt_pk_bf16_f32 v58, v36, v37\n\t"
      "v_mfma_f32_16x16x32_bf16 v[180:183], v[52:55], v[136:139], v[180:183]\n\t"
      "ds_read_b128 v[136:139], v8 offset:16384\n\t"
      "v_cvt_pk_bf16_f32 v59, v38, v39\n\t"
      "v_mfma_f32_16x16x32_bf16 v[180:183], v[44:47], v[140:143], v[180:183]\n\t"
      "v_lshrrev_b32 v31, 8, v17\n\t"
      "v_and_b32 v28, v13, v17\n\t"
      "v_mfma_f32_16x16x32_bf16 v[188:191], v[44:47], v[132:135], v[188:191]\n\t"
      "ds_read_b128 v[140:143], v9 offset:16384\n\t"
      "v_and_b32 v29, v14, v17\n\t"
      "v_mfma_f32_16x16x32_bf16 v[188:191], v[52:55], v[128:131], v[188:191]\n\t"
      "v_and_b32 v30, v13, v31\n\t"
      "v_and_b32 v31, v14, v31\n\t"
      "v_mfma_f32_16x16x32_bf16 v[184:187], v[48:51], v[128:131], v[184:187]\n\t"
      "ds_read_b128 v[128:131], v8 offset:18432\n\t"
      "ds_read_b64 v[16:17], v10 offset:16384\n\t"
      "v_mfma_f32_16x16x32_bf16 v[184:187], v[40:43], v[132:135], v[184:187]\n\t"
      "v_fma_mix_f32 v32, v28, v21, -v23 op_sel:[0,0,0] op_sel_hi:[1,0,0]\n\t"
      "v_fma_mix_f32 v33, v29, v25, -v23 op_sel:[0,0,0] op_sel_hi:[1,0,0]\n\t"
      "v_mfma_f32_16x16x32_bf16 v[192:195], v[40:43], v[124:127], v[192:195]\n\t"
      "ds_read_b128 v[132:135], v9 offset:18432\n\t"
      "v_fma_mix_f32 v34, v30, v21, -v23 op_sel:[0,0,0] op_sel_hi:[1,0,0]\n\t"
      "v_mfma_f32_16x16x32_bf16 v[192:195], v[48:51], v[120:123], v[192:195]\n\t"
      "s_barrier\n\t"
      "v_mfma_f32_16x16x32_bf16 v[196:199], v[52:55], v[120:123], v[196:199]\n\t"
      "ds_read_b128 v[120:123], v8 offset:20480\n\t"
      "v_fma_mix_f32 v35, v31, v25, -v23 op_sel:[0,0,0] op_sel_hi:[1,0,0]\n\t"
      "v_mfma_f32_16x16x32_bf16 v[196:199], v[44:47], v[124:127], v[196:199]\n\t"
      "v_fma_mix_f32 v36, v28, v21, -v23 op_sel:[1,0,0] op_sel_hi:[1,0,0]\n\t"
      "v_fma_mix_f32 v37, v29, v25, -v23 op_sel:[1,0,0] op_sel_hi:[1,0,0]\n\t"
      "v_mfma_f32_16x16x32_bf16 v[204:207], v[44:47], v[116:119], v[204:207]\n\t"
      "ds_read_b128 v[124:127], v9 offset:20480\n\t"
      "v_cvt_pk_bf16_f32 v60, v32, v33\n\t"
      "v_mfma_f32_16x16x32_bf16 v[204:207], v[52:55], v[112:115], v[204:207]\n\t"
      "v_cvt_pk_bf16_f32 v61, v34, v35\n\t"
      "v_fma_mix_f32 v38, v30, v21, -v23 op_sel:[1,0,0] op_sel_hi:[1,0,0]\n\t"
      "v_mfma_f32_16x16x32_bf16 v[200:203], v[48:51], v[112:115], v[200:203]\n\t"
      "ds_read_b128 v[112:115], v8 offset:22528\n\t"
      "v_fma_mix_f32 v39, v31, v25, -v23 op_sel:[1,0,0] op_sel_hi:[1,0,0]\n\t"
      "v_mfma_f32_16x16x32_bf16 v[200:203], v[40:43], v[116:119], v[200:203]\n\t"
      "v_cvt_pk_bf16_f32 v62, v36, v37\n\t"
      "v_cvt_pk_bf16_f32 v63, v38, v39\n\t"
      "v_mfma_f32_16x16x32_bf16 v[208:211], v[40:43], v[108:111], v[208:211]\n\t"
      "ds_read_b128 v[116:119], v9 offset:22528\n\t"
      "s_waitcnt lgkmcnt(3)\n\t"
      "v_mfma_f32_16x16x32_bf16 v[208:211], v[48:51], v[104:107], v[208:211]\n\t"
      "v_lshrrev_b32 v31, 8, v18\n\t"
      "v_and_b32 v28, v13, v18\n\t"
      "v_and_b32 v29, v14, v18\n\t"
      "v_mfma_f32_16x16x32_bf16 v[212:215], v[52:55], v[104:107], v[212:215]\n\t"
      "ds_read_b128 v[104:107], v8 offset:24576\n\t"
      "v_and_b32 v30, v13, v31\n\t"
      "v_mfma_f32_16x16x32_bf16 v[212:215], v[44:47], v[108:111], v[212:215]\n\t"
      "ds_read_b128 v[108:111], v9 offset:24576\n\t"
      "v_and_b32 v31, v14, v31\n\t"
      "v_mfma_f32_16x16x32_bf16 v[220:223], v[44:47], v[100:103], v[220:223]\n\t"
      "v_fma_mix_f32 v32, v28, v20, -v22 op_sel:[0,0,0] op_sel_hi:[1,0,0]\n\t"
      "v_fma_mix_f32 v33, v29, v24, -v22 op_sel:[0,0,0] op_sel_hi:[1,0,0]\n\t"
      "v_mfma_f32_16x16x32_bf16 v[220:223], v[52:55], v[96:99], v[220:223]\n\t"
      "v_fma_mix_f32 v34, v30, v20, -v22 op_sel:[0,0,0] op_sel_hi:[1,0,0]\n\t"
      "v_fma_mix_f32 v35, v31, v24, -v22 op_sel:[0,0,0] op_sel_hi:[1,0,0]\n\t"
      "v_mfma_f32_16x16x32_bf16 v[216:219], v[48:51], v[96:99], v[216:219]\n\t"
      "ds_read_b128 v[96:99], v8 offset:26624\n\t"
      "v_fma_mix_f32 v36, v28, v20, -v22 op_sel:[1,0,0] op_sel_hi:[1,0,0]\n\t"
      "v_mfma_f32_16x16x32_bf16 v[216:219], v[40:43], v[100:103], v[216:219]\n\t"
      "ds_read_b128 v[100:103], v9 offset:26624\n\t"
      "v_fma_mix_f32 v37, v29, v24, -v22 op_sel:[1,0,0] op_sel_hi:[1,0,0]\n\t"
      "v_mfma_f32_16x16x32_bf16 v[224:227], v[40:43], v[92:95], v[224:227]\n\t"
      "v_cvt_pk_bf16_f32 v64, v32, v33\n\t"
      "v_cvt_pk_bf16_f32 v65, v34, v35\n\t"
      "v_mfma_f32_16x16x32_bf16 v[224:227], v[48:51], v[88:91], v[224:227]\n\t"
      "v_fma_mix_f32 v38, v30, v20, -v22 op_sel:[1,0,0] op_sel_hi:[1,0,0]\n\t"
      "v_fma_mix_f32 v39, v31, v24, -v22 op_sel:[1,0,0] op_sel_hi:[1,0,0]\n\t"
      "v_mfma_f32_16x16x32_bf16 v[228:231], v[52:55], v[88:91], v[228:231]\n\t"
      "ds_read_b128 v[88:91], v8 offset:28672\n\t"
      "v_cvt_pk_bf16_f32 v66, v36, v37\n\t"
      "v_mfma_f32_16x16x32_bf16 v[228:231], v[44:47], v[92:95], v[228:231]\n\t"
      "ds_read_b128 v[92:95], v9 offset:28672\n\t"
      "v_cvt_pk_bf16_f32 v67, v38, v39\n\t"
      "v_mfma_f32_16x16x32_bf16 v[236:239], v[44:47], v[84:87], v[236:239]\n\t"
      "v_lshrrev_b32 v31, 8, v19\n\t"
      "v_and_b32 v28, v13, v19\n\t"
      "v_mfma_f32_16x16x32_bf16 v[236:239], v[52:55], v[80:83], v[236:239]\n\t"
      "v_and_b32 v29, v14, v19\n\t"
      "v_and_b32 v30, v13, v31\n\t"
      "v_mfma_f32_16x16x32_bf16 v[232:235], v[48:51], v[80:83], v[232:235]\n\t"
      "ds_read_b128 v[80:83], v8 offset:30720\n\t"
      "v_and_b32 v31, v14, v31\n\t"
      "v_mfma_f32_16x16x32_bf16 v[232:235], v[40:43], v[84:87], v[232:235]\n\t"
      "ds_read_b128 v[84:87], v9 offset:30720\n\t"
      "ds_read_b64 v[18:19], v10 offset:18432\n\t"
      "v_mfma_f32_16x16x32_bf16 v[240:243], v[40:43], v[136:139], v[240:243]\n\t"
      "v_fma_mix_f32 v32, v28, v21, -v23 op_sel:[0,0,0] op_sel_hi:[1,0,0]\n\t"
      "v_fma_mix_f32 v33, v29, v25, -v23 op_sel:[0,0,0] op_sel_hi:[1,0,0]\n\t"
      "v_mfma_f32_16x16x32_bf16 v[240:243], v[48:51], v[140:143], v[240:243]\n\t"
      "v_fma_mix_f32 v34, v30, v21, -v23 op_sel:[0,0,0] op_sel_hi:[1,0,0]\n\t"
      "v_fma_mix_f32 v35, v31, v25, -v23 op_sel:[0,0,0] op_sel_hi:[1,0,0]\n\t"
      "v_mfma_f32_16x16x32_bf16 v[244:247], v[52:55], v[140:143], v[244:247]\n\t"
      "ds_read_b128 v[140:143], v8 offset:32768\n\t"
      "v_fma_mix_f32 v36, v28, v21, -v23 op_sel:[1,0,0] op_sel_hi:[1,0,0]\n\t"
      "v_mfma_f32_16x16x32_bf16 v[244:247], v[44:47], v[136:139], v[244:247]\n\t"
      "ds_read_b128 v[136:139], v9 offset:32768\n\t"
      "v_fma_mix_f32 v37, v29, v25, -v23 op_sel:[1,0,0] op_sel_hi:[1,0,0]\n\t"
      "v_mfma_f32_16x16x32_bf16 v[252:255], v[44:47], v[128:131], v[252:255]\n\t"
      "v_cvt_pk_bf16_f32 v68, v32, v33\n\t"
      "v_cvt_pk_bf16_f32 v69, v34, v35\n\t"
      "v_mfma_f32_16x16x32_bf16 v[252:255], v[52:55], v[132:135], v[252:255]\n\t"
      "v_fma_mix_f32 v38, v30, v21, -v23 op_sel:[1,0,0] op_sel_hi:[1,0,0]\n\t"
      "v_fma_mix_f32 v39, v31, v25, -v23 op_sel:[1,0,0] op_sel_hi:[1,0,0]\n\t"
      "v_mfma_f32_16x16x32_bf16 v[248:251], v[48:51], v[132:135], v[248:251]\n\t"
      "ds_read_b128 v[132:135], v8 offset:34816\n\t"
      "v_cvt_pk_bf16_f32 v70, v36, v37\n\t"
      "v_mfma_f32_16x16x32_bf16 v[248:251], v[40:43], v[128:131], v[248:251]\n\t"
      "ds_read_b128 v[128:131], v9 offset:34816\n\t"
      "v_cvt_pk_bf16_f32 v71, v38, v39\n\t"
      "v_mfma_f32_16x16x32_bf16 v[144:147], v[56:59], v[120:123], v[144:147]\n\t"
      "v_lshlrev_b32 v28, 16, v26\n\t"
      "s_waitcnt lgkmcnt(3)\n\t"
      "v_mfma_f32_16x16x32_bf16 v[144:147], v[64:67], v[124:127], v[144:147]\n\t"
      "v_and_b32 v29, 0xffff0000, v26\n\t"
      "v_lshlrev_b32 v30, 16, v27\n\t"
      "v_and_b32 v31, 0xffff0000, v27\n\t"
      "v_mfma_f32_16x16x32_bf16 v[148:151], v[68:71], v[124:127], v[148:151]\n\t"
      "ds_read_b128 v[124:127], v8 offset:36864\n\t"
      "v_mul_f32 v20, 0x4b800000, v28\n\t"
      "v_mfma_f32_16x16x32_bf16 v[148:151], v[60:63], v[120:123], v[148:151]\n\t"
      "ds_read_b128 v[120:123], v9 offset:36864\n\t"
      "v_mul_f32 v24, 0x49800000, v28\n\t"
      "v_mfma_f32_16x16x32_bf16 v[156:159], v[60:63], v[112:115], v[156:159]\n\t"
      "v_mul_f32 v22, v28, v30\n\t"
      "v_mul_f32 v21, 0x4b800000, v29\n\t"
      "v_mfma_f32_16x16x32_bf16 v[156:159], v[68:71], v[116:119], v[156:159]\n\t"
      "v_mul_f32 v25, 0x49800000, v29\n\t"
      "v_mul_f32 v23, v29, v31\n\t"
      "v_mfma_f32_16x16x32_bf16 v[152:155], v[64:67], v[116:119], v[152:155]\n\t"
      "ds_read_b128 v[116:119], v8 offset:38912\n\t"
      "ds_read2st64_b32 v[26:27], v11 offset0:5 offset1:21\n\t"
      "v_mfma_f32_16x16x32_bf16 v[152:155], v[56:59], v[112:115], v[152:155]\n\t"
      "ds_read_b128 v[112:115], v9 offset:38912\n\t"
      "v_lshrrev_b32 v31, 8, v16\n\t"
      "v_mfma_f32_16x16x32_bf16 v[160:163], v[56:59], v[104:107], v[160:163]\n\t"
      "v_and_b32 v28, v13, v16\n\t"
      "v_and_b32 v29, v14, v16\n\t"
      "LH14x1_%=:\n\t"
      "s_cbranch_execz LHRET_%=\n\t"
      "v_mfma_f32_16x16x32_bf16 v[160:163], v[64:67], v[108:111], v[160:163]\n\t"
      "v_and_b32 v30, v13, v31\n\t"
      "v_and_b32 v31, v14, v31\n\t"
      "v_mfma_f32_16x16x32_bf16 v[164:167], v[68:71], v[108:111], v[164:167]\n\t"
      "ds_read_b128 v[108:111], v8 offset:40960\n\t"
      "v_fma_mix_f32 v32, v28, v20, -v22 op_sel:[0,0,0] op_sel_hi:[1,0,0]\n\t"
      "v_mfma_f32_16x16x32_bf16 v[164:167], v[60:63], v[104:107], v[164:167]\n\t"
      "ds_read_b128 v[104:107], v9 offset:40960\n\t"
      "v_fma_mix_f32 v33, v29, v24, -v22 op_sel:[0,0,0] op_sel_hi:[1,0,0]\n\t"
      "v_mfma_f32_16x16x32_bf16 v[172:175], v[60:63], v[96:99], v[172:175]\n\t"
      "v_fma_mix_f32 v34, v30, v20, -v22 op_sel:[0,0,0] op_sel_hi:[1,0,0]\n\t"
      "v_fma_mix_f32 v35, v31, v24, -v22 op_sel:[0,0,0] op_sel_hi:[1,0,0]\n\t"
      "v_mfma_f32_16x16x32_bf16 v[172:175], v[68:71], v[100:103], v[172:175]\n\t"
      "v_fma_mix_f32 v36, v28, v20, -v22 op_sel:[1,0,0] op_sel_hi:[1,0,0]\n\t"
      "v_fma_mix_f32 v37, v29, v24, -v22 op_sel:[1,0,0] op_sel_hi:[1,0,0]\n\t"
      "v_mfma_f32_16x16x32_bf16 v[168:171], v[64:67], v[100:103], v[168:171]\n\t"
      "ds_read_b128 v[100:103], v8 offset:43008\n\t"
      "v_cvt_pk_bf16_f32 v40, v32, v33\n\t"
      "v_mfma_f32_16x16x32_bf16 v[168:171], v[56:59], v[96:99], v[168:171]\n\t"
      "ds_read_b128 v[96:99], v9 offset:43008\n\t"
      "v_cvt_pk_bf16_f32 v41, v34, v35\n\t"
      "v_mfma_f32_16x16x32_bf16 v[176:179], v[56:59], v[88:91], v[176:179]\n\t"
      "v_fma_mix_f32 v38, v30, v20, -v22 op_sel:[1,0,0] op_sel_hi:[1,0,0]\n\t"
      "v_fma_mix_f32 v39, v31, v24, -v22 op_sel:[1,0,0] op_sel_hi:[1,0,0]\n\t"
      "v_mfma_f32_16x16x32_bf16 v[176:179], v[64:67], v[92:95], v[176:179]\n\t"
      "v_cvt_pk_bf16_f32 v42, v36, v37\n\t"
      "v_cvt_pk_bf16_f32 v43, v38, v39\n\t"
      "v_mfma_f32_16x16x32_bf16 v[180:183], v[68:71], v[92:95], v[180:183]\n\t"
      "ds_read_b128 v[92:95], v8 offset:45056\n\t"
      "v_lshrrev_b32 v31, 8, v17\n\t"
      "v_mfma_f32_16x16x32_bf16 v[180:183], v[60:63], v[88:91], v[180:183]\n\t"
      "ds_read_b128 v[88:91], v9 offset:45056\n\t"
      "v_and_b32 v28, v13, v17\n\t"
      "v_mfma_f32_16x16x32_bf16 v[188:191], v[60:63], v[80:83], v[188:191]\n\t"
      "v_and_b32 v29, v14, v17\n\t"
      "v_and_b32 v30, v13, v31\n\t"
      "v_mfma_f32_16x16x32_bf16 v[188:191], v[68:71], v[84:87], v[188:191]\n\t"
      "v_and_b32 v31, v14, v31\n\t"
      "ds_read_b64 v[16:17], v10 offset:20480\n\t"
      "v_mfma_f32_16x16x32_bf16 v[184:187], v[64:67], v[84:87], v[184:187]\n\t"
      "ds_read_b128 v[84:87], v8 offset:47104\n\t"
      "v_fma_mix_f32 v32, v28, v21, -v23 op_sel:[0,0,0] op_sel_hi:[1,0,0]\n\t"
      "v_mfma_f32_16x16x32_bf16 v[184:187], v[56:59], v[80:83], v[184:187]\n\t"
      "ds_read_b128 v[80:83], v9 offset:47104\n\t"
      "v_fma_mix_f32 v33, v29, v25, -v23 op_sel:[0,0,0] op_sel_hi:[1,0,0]\n\t"
      "v_mfma_f32_16x16x32_bf16 v[192:195], v[56:59], v[140:143], v[192:195]\n\t"
      "v_fma_mix_f32 v34, v30, v21, -v23 op_sel:[0,0,0] op_sel_hi:[1,0,0]\n\t"
      "s_waitcnt lgkmcnt(4)\n\t"
      "v_mfma_f32_16x16x32_bf16 v[192:195], v[64:67], v[136:139], v[192:195]\n\t"
      "s_barrier\n\t"
      "v_mfma_f32_16x16x32_bf16 v[196:199], v[68:71], v[136:139], v[196:199]\n\t"
      "ds_read_b128 v[136:139], v8 offset:49152\n\t"
      "v_fma_mix_f32 v35, v31, v25, -v23 op_sel:[0,0,0] op_sel_hi:[1,0,0]\n\t"
      "v_mfma_f32_16x16x32_bf16 v[196:199], v[60:63], v[140:143], v[196:199]\n\t"
      "ds_read_b128 v[140:143], v9 offset:49152\n\t"
      "v_fma_mix_f32 v36, v28, v21, -v23 op_sel:[1,0,0] op_sel_hi:[1,0,0]\n\t"
      "v_mfma_f32_16x16x32_bf16 v[204:207], v[60:63], v[132:135], v[204:207]\n\t"
      "v_fma_mix_f32 v37, v29, v25, -v23 op_sel:[1,0,0] op_sel_hi:[1,0,0]\n\t"
      "v_cvt_pk_bf16_f32 v44, v32, v33\n\t"
      "v_mfma_f32_16x16x32_bf16 v[204:207], v[68:71], v[128:131], v[204:207]\n\t"
      "v_cvt_pk_bf16_f32 v45, v34, v35\n\t"
      "v_fma_mix_f32 v38, v30, v21, -v23 op_sel:[1,0,0] op_sel_hi:[1,0,0]\n\t"
      "v_mfma_f32_16x16x32_bf16 v[200:203], v[64:67], v[128:131], v[200:203]\n\t"
      "ds_read_b128 v[128:131], v8 offset:51200\n\t"
      "v_fma_mix_f32 v39, v31, v25, -v23 op_sel:[1,0,0] op_sel_hi:[1,0,0]\n\t"
      "v_mfma_f32_16x16x32_bf16 v[200:203], v[56:59], v[132:135], v[200:203]\n\t"
      "ds_read_b128 v[132:135], v9 offset:51200\n\t"
      "v_cvt_pk_bf16_f32 v46, v36, v37\n\t"
      "v_mfma_f32_16x16x32_bf16 v[208:211], v[56:59], v[124:127], v[208:211]\n\t"
      "v_cvt_pk_bf16_f32 v47, v38, v39\n\t"
      "v_lshrrev_b32 v31, 8, v18\n\t"
      "v_mfma_f32_16x16x32_bf16 v[208:211], v[64:67], v[120:123], v[208:211]\n\t"
      "v_and_b32 v28, v13, v18\n\t"
      "v_and_b32 v29, v14, v18\n\t"
      "v_mfma_f32_16x16x32_bf16 v[212:215], v[68:71], v[120:123], v[212:215]\n\t"
      "ds_read_b128 v[120:123], v8 offset:53248\n\t"
      "v_and_b32 v30, v13, v31\n\t"
      "v_mfma_f32_16x16x32_bf16 v[212:215], v[60:63], v[124:127], v[212:215]\n\t"
      "ds_read_b128 v[124:127], v9 offset:53248\n\t"
      "v_and_b32 v31, v14, v31\n\t"
      "v_mfma_f32_16x16x32_bf16 v[220:223], v[60:63], v[116:119], v[220:223]\n\t"
      "v_fma_mix_f32 v32, v28, v20, -v22 op_sel:[0,0,0] op_sel_hi:[1,0,0]\n\t"
      "v_fma_mix_f32 v33, v29, v24, -v22 op_sel:[0,0,0] op_sel_hi:[1,0,0]\n\t"
      "v_mfma_f32_16x16x32_bf16 v[220:223], v[68:71], v[112:115], v[220:223]\n\t"
      "v_fma_mix_f32 v34, v30, v20, -v22 op_sel:[0,0,0] op_sel_hi:[1,0,0]\n\t"
      "v_fma_mix_f32 v35, v31, v24, -v22 op_sel:[0,0,0] op_sel_hi:[1,0,0]\n\t"
      "v_mfma_f32_16x16x32_bf16 v[216:219], v[64:67], v[112:115], v[216:219]\n\t"
      "ds_read_b128 v[112:115], v8 offset:55296\n\t"
      "v_fma_mix_f32 v36, v28, v20, -v22 op_sel:[1,0,0] op_sel_hi:[1,0,0]\n\t"
      "v_mfma_f32_16x16x32_bf16 v[216:219], v[56:59], v[116:119], v[216:219]\n\t"
      "ds_read_b128 v[116:119], v9 offset:55296\n\t"
      "v_fma_mix_f32 v37, v29, v24, -v22 op_sel:[1,0,0] op_sel_hi:[1,0,0]\n\t"
      "v_mfma_f32_16x16x32_bf16 v[224:227], v[56:59], v[108:111], v[224:227]\n\t"
      "v_cvt_pk_bf16_f32 v48, v32, v33\n\t"
      "v_cvt_pk_bf16_f32 v49, v34, v35\n\t"
      "v_mfma_f32_16x16x32_bf16 v[224:227], v[64:67], v[104:107], v[224:227]\n\t"
      "v_fma_mix_f32 v38, v30, v20, -v22 op_sel:[1,0,0] op_sel_hi:[1,0,0]\n\t"
      "v_fma_mix_f32 v39, v31, v24, -v22 op_sel:[1,0,0] op_sel_hi:[1,0,0]\n\t"
      "v_mfma_f32_16x16x32_bf16 v[228:231], v[68:71], v[104:107], v[228:231]\n\t"
      "ds_read_b128 v[104:107], v8 offset:57344\n\t"
      "v_cvt_pk_bf16_f32 v50, v36, v37\n\t"
      "v_mfma_f32_16x16x32_bf16 v[228:231], v[60:63], v[108:111], v[228:231]\n\t"
      "ds_read_b128 v[108:111], v9 offset:57344\n\t"
      "v_cvt_pk_bf16_f32 v51, v38, v39\n\t"
      "v_mfma_f32_16x16x32_bf16 v[236:239], v[60:63], v[100:103], v[236:239]\n\t"
      "v_lshrrev_b32 v31, 8, v19\n\t"
      "v_and_b32 v28, v13, v19\n\t"
      "v_mfma_f32_16x16x32_bf16 v[236:239], v[68:71], v[96:99], v[236:239]\n\t"
      "v_and_b32 v29, v14, v19\n\t"
      "v_and_b32 v30, v13, v31\n\t"
      "v_mfma_f32_16x16x32_bf16 v[232:235], v[64:67], v[96:99], v[232:235]\n\t"
      "ds_read_b128 v[96:99], v8 offset:59392\n\t"
      "v_and_b32 v31, v14, v31\n\t"
      "v_mfma_f32_16x16x32_bf16 v[232:235], v[56:59], v[100:103], v[232:235]\n\t"
      "ds_read_b128 v[100:103], v9 offset:59392\n\t"
      "ds_read_b64 v[18:19], v10 offset:22528\n\t"
      "v_mfma_f32_16x16x32_bf16 v[240:243], v[56:59], v[92:95], v[240:243]\n\t"
      "v_fma_mix_f32 v32, v28, v21, -v23 op_sel:[0,0,0] op_sel_hi:[1,0,0]\n\t"
      "s_waitcnt lgkmcnt(4)\n\t"
      "v_mfma_f32_16x16x32_bf16 v[240:243], v[64:67], v[88:91], v[240:243]\n\t"
      "v_fma_mix_f32 v33, v29, v25, -v23 op_sel:[0,0,0] op_sel_hi:[1,0,0]\n\t"
      "v_fma_mix_f32 v34, v30, v21, -v23 op_sel:[0,0,0] op_sel_hi:[1,0,0]\n\t"
      "v_mfma_f32_16x16x32_bf16 v[244:247], v[68:71], v[88:91], v[244:247]\n\t"
      "ds_read_b128 v[88:91], v8 offset:61440\n\t"
      "v_fma_mix_f32 v35, v31, v25, -v23 op_sel:[0,0,0] op_sel_hi:[1,0,0]\n\t"
      "v_mfma_f32_16x16x32_bf16 v[244:247], v[60:63], v[92:95], v[244:247]\n\t"
      "ds_read_b128 v[92:95], v9 offset:61440\n\t"
      "v_fma_mix_f32 v36, v28, v21, -v23 op_sel:[1,0,0] op_sel_hi:[1,0,0]\n\t"
      "v_mfma_f32_16x16x32_bf16 v[252:255], v[60:63], v[84:87], v[252:255]\n\t"
      "v_fma_mix_f32 v37, v29, v25, -v23 op_sel:[1,0,0] op_sel_hi:[1,0,0]\n\t"
      "v_cvt_pk_bf16_f32 v52, v32, v33\n\t"
      "v_mfma_f32_16x16x32_bf16 v[252:255], v[68:71], v[80:83], v[252:255]\n\t"
      "v_cvt_pk_bf16_f32 v53, v34, v35\n\t"
      "v_fma_mix_f32 v38, v30, v21, -v23 op_sel:[1,0,0] op_sel_hi:[1,0,0]\n\t"
      "v_mfma_f32_16x16x32_bf16 v[248:251], v[64:67], v[80:83], v[248:251]\n\t"
      "ds_read_b128 v[80:83], v8 offset:63488\n\t"
      "v_fma_mix_f32 v39, v31, v25, -v23 op_sel:[1,0,0] op_sel_hi:[1,0,0]\n\t"
      "v_mfma_f32_16x16x32_bf16 v[248:251], v[56:59], v[84:87], v[248:251]\n\t"
      "ds_read_b128 v[84:87], v9 offset:63488\n\t"
      "v_cvt_pk_bf16_f32 v54, v36, v37\n\t"
      "v_cvt_pk_bf16_f32 v55, v38, v39\n\t"
      "v_mfma_f32_16x16x32_bf16 v[144:147], v[40:43], v[136:139], v[144:147]\n\t"
      "v_lshlrev_b32 v28, 16, v26\n\t"
      "v_and_b32 v29, 0xffff0000, v26\n\t"
      "v_mfma_f32_16x16x32_bf16 v[144:147], v[48:51], v[140:143], v[144:147]\n\t"
      "v_lshlrev_b32 v30, 16, v27\n\t"
      "v_and_b32 v31, 0xffff0000, v27\n\t"
      "v_mfma_f32_16x16x32_bf16 v[148:151], v[52:55], v[140:143], v[148:151]\n\t"
      "ds_read_b128 v[140:143], v6 offset:0\n\t"
      "v_mul_f32 v20, 0x4b800000, v28\n\t"
      "v_mfma_f32_16x16x32_bf16 v[148:151], v[44:47], v[136:139], v[148:151]\n\t"
      "ds_read_b128 v[136:139], v7 offset:0\n\t"
      "v_mul_f32 v24, 0x49800000, v28\n\t"
      "v_mfma_f32_16x16x32_bf16 v[156:159], v[44:47], v[128:131], v[156:159]\n\t"
      "v_mul_f32 v22, v28, v30\n\t"
      "v_mul_f32 v21, 0x4b800000, v29\n\t"
      "v_mfma_f32_16x16x32_bf16 v[156:159], v[52:55], v[132:135], v[156:159]\n\t"
      "v_mul_f32 v25, 0x49800000, v29\n\t"
      "v_mul_f32 v23, v29, v31\n\t"
      "v_mfma_f32_16x16x32_bf16 v[152:155], v[48:51], v[132:135], v[152:155]\n\t"
      "ds_read_b128 v[132:135], v6 offset:2048\n\t"
      "ds_read2st64_b32 v[26:27], v11 offset0:6 offset1:22\n\t"
      "v_mfma_f32_16x16x32_bf16 v[152:155], v[40:43], v[128:131], v[152:155]\n\t"
      "ds_read_b128 v[128:131], v7 offset:2048\n\t"
      "v_lshrrev_b32 v31, 8, v16\n\t"
      "v_mfma_f32_16x16x32_bf16 v[160:163], v[40:43], v[120:123], v[160:163]\n\t"
      "v_and_b32 v28, v13, v16\n\t"
      "v_and_b32 v29, v14, v16\n\t"
      "v_mfma_f32_16x16x32_bf16 v[160:163], v[48:51], v[124:127], v[160:163]\n\t"
      "v_and_b32 v30, v13, v31\n\t"
      "v_and_b32 v31, v14, v31\n\t"
      "v_mfma_f32_16x16x32_bf16 v[164:167], v[52:55], v[124:127], v[164:167]\n\t"
      "ds_read_b128 v[124:127], v6 offset:4096\n\t"
      "v_fma_mix_f32 v32, v28, v20, -v22 op_sel:[0,0,0] op_sel_hi:[1,0,0]\n\t"
      "v_mfma_f32_16x16x32_bf16 v[164:167], v[44:47], v[120:123], v[164:167]\n\t"
      "ds_read_b128 v[120:123], v7 offset:4096\n\t"
      "v_fma_mix_f32 v33, v29, v24, -v22 op_sel:[0,0,0] op_sel_hi:[1,0,0]\n\t"
      "v_mfma_f32_16x16x32_bf16 v[172:175], v[44:47], v[112:115], v[172:175]\n\t"
      "v_fma_mix_f32 v34, v30, v20, -v22 op_sel:[0,0,0] op_sel_hi:[1,0,0]\n\t"
      "v_fma_mix_f32 v35, v31, v24, -v22 op_sel:[0,0,0] op_sel_hi:[1,0,0]\n\t"
      "v_mfma_f32_16x16x32_bf16 v[172:175], v[52:55], v[116:119], v[172:175]\n\t"
      "v_fma_mix_f32 v36, v28, v20, -v22 op_sel:[1,0,0] op_sel_hi:[1,0,0]\n\t"
      "v_fma_mix_f32 v37, v29, v24, -v22 op_sel:[1,0,0] op_sel_hi:[1,0,0]\n\t"
      "v_mfma_f32_16x16x32_bf16 v[168:171], v[48:51], v[116:119], v[168:171]\n\t"
      "ds_read_b128 v[116:119], v6 offset:6144\n\t"
      "v_cvt_pk_bf16_f32 v56, v32, v33\n\t"
      "v_mfma_f32_16x16x32_bf16 v[168:171], v[40:43], v[112:115], v[168:171]\n\t"
      "ds_read_b128 v[112:115], v7 offset:6144\n\t"
      "v_cvt_pk_bf16_f32 v57, v34, v35\n\t"
      "v_mfma_f32_16x16x32_bf16 v[176:179], v[40:43], v[104:107], v[176:179]\n\t"
      "v_fma_mix_f32 v38, v30, v20, -v22 op_sel:[1,0,0] op_sel_hi:[1,0,0]\n\t"
      "s_waitcnt lgkmcnt(3)\n\t"
      "v_mfma_f32_16x16x32_bf16 v[176:179], v[48:51], v[108:111], v[176:179]\n\t"
      "v_fma_mix_f32 v39, v31, v24, -v22 op_sel:[1,0,0] op_sel_hi:[1,0,0]\n\t"
      "v_cvt_pk_bf16_f32 v58, v36, v37\n\t"
      "v_mfma_f32_16x16x32_bf16 v[180:183], v[52:55], v[108:111], v[180:183]\n\t"
      "ds_read_b128 v[108:111], v6 offset:8192\n\t"
      "v_cvt_pk_bf16_f32 v59, v38, v39\n\t"
      "v_mfma_f32_16x16x32_bf16 v[180:183], v[44:47], v[104:107], v[180:183]\n\t"
      "ds_read_b128 v[104:107], v7 offset:8192\n\t"
      "v_lshrrev_b32 v31, 8, v17\n\t"
      "v_mfma_f32_16x16x32_bf16 v[188:191], v[44:47], v[96:99], v[188:191]\n\t"
      "v_and_b32 v28, v13, v17\n\t"
      "v_and_b32 v29, v14, v17\n\t"
      "v_mfma_f32_16x16x32_bf16 v[188:191], v[52:55], v[100:103], v[188:191]\n\t"
      "v_and_b32 v30, v13, v31\n\t"
      "v_and_b32 v31, v14, v31\n\t"
      "v_mfma_f32_16x16x32_bf16 v[184:187], v[48:51], v[100:103], v[184:187]\n\t"
      "ds_read_b128 v[100:103], v6 offset:10240\n\t"
      "ds_read_b64 v[16:17], v10 offset:0\n\t"
      "v_mfma_f32_16x16x32_bf16 v[184:187], v[40:43], v[96:99], v[184:187]\n\t"
      "ds_read_b128 v[96:99], v7 offset:10240\n\t"
      "v_fma_mix_f32 v32, v28, v21, -v23 op_sel:[0,0,0] op_sel_hi:[1,0,0]\n\t"
      "v_mfma_f32_16x16x32_bf16 v[192:195], v[40:43], v[88:91], v[192:195]\n\t"
      "v_fma_mix_f32 v33, v29, v25, -v23 op_sel:[0,0,0] op_sel_hi:[1,0,0]\n\t"
      "v_fma_mix_f32 v34, v30, v21, -v23 op_sel:[0,0,0] op_sel_hi:[1,0,0]\n\t"
      "v_mfma_f32_16x16x32_bf16 v[192:195], v[48:51], v[92:95], v[192:195]\n\t"
      "s_barrier\n\t"
      "v_mfma_f32_16x16x32_bf16 v[196:199], v[52:55], v[92:95], v[196:199]\n\t"
      "ds_read_b128 v[92:95], v6 offset:12288\n\t"
      "v_fma_mix_f32 v35, v31, v25, -v23 op_sel:[0,0,0] op_sel_hi:[1,0,0]\n\t"
      "v_mfma_f32_16x16x32_bf16 v[196:199], v[44:47], v[88:91], v[196:199]\n\t"
      "ds_read_b128 v[88:91], v7 offset:12288\n\t"
      "v_fma_mix_f32 v36, v28, v21, -v23 op_sel:[1,0,0] op_sel_hi:[1,0,0]\n\t"
      "v_mfma_f32_16x16x32_bf16 v[204:207], v[44:47], v[80:83], v[204:207]\n\t"
      "v_fma_mix_f32 v37, v29, v25, -v23 op_sel:[1,0,0] op_sel_hi:[1,0,0]\n\t"
      "v_cvt_pk_bf16_f32 v60, v32, v33\n\t"
      "v_mfma_f32_16x16x32_bf16 v[204:207], v[52:55], v[84:87], v[204:207]\n\t"
      "v_cvt_pk_bf16_f32 v61, v34, v35\n\t"
      "v_fma_mix_f32 v38, v30, v21, -v23 op_sel:[1,0,0] op_sel_hi:[1,0,0]\n\t"
      "v_mfma_f32_16x16x32_bf16 v[200:203], v[48:51], v[84:87], v[200:203]\n\t"
      "ds_read_b128 v[84:87], v6 offset:14336\n\t"
      "v_fma_mix_f32 v39, v31, v25, -v23 op_sel:[1,0,0] op_sel_hi:[1,0,0]\n\t"
      "v_mfma_f32_16x16x32_bf16 v[200:203], v[40:43], v[80:83], v[200:203]\n\t"
      "ds_read_b128 v[80:83], v7 offset:14336\n\t"
      "v_cvt_pk_bf16_f32 v62, v36, v37\n\t"
      "v_mfma_f32_16x16x32_bf16 v[208:211], v[40:43], v[140:143], v[208:211]\n\t"
      "v_cvt_pk_bf16_f32 v63, v38, v39\n\t"
      "v_lshrrev_b32 v31, 8, v18\n\t"
      "v_mfma_f32_16x16x32_bf16 v[208:211], v[48:51], v[136:139], v[208:211]\n\t"
      "v_and_b32 v28, v13, v18\n\t"
      "v_and_b32 v29, v14, v18\n\t"
      "v_mfma_f32_16x16x32_bf16 v[212:215], v[52:55], v[136:139], v[212:215]\n\t"
      "ds_read_b128 v[136:139], v6 offset:16384\n\t"
      "v_and_b32 v30, v13, v31\n\t"
      "v_mfma_f32_16x16x32_bf16 v[212:215], v[44:47], v[140:143], v[212:215]\n\t"
      "ds_read_b128 v[140:143], v7 offset:16384\n\t"
      "v_and_b32 v31, v14, v31\n\t"
      "v_mfma_f32_16x16x32_bf16 v[220:223], v[44:47], v[132:135], v[220:223]\n\t"
      "v_fma_mix_f32 v32, v28, v20, -v22 op_sel:[0,0,0] op_sel_hi:[1,0,0]\n\t"
      "v_fma_mix_f32 v33, v29, v24, -v22 op_sel:[0,0,0] op_sel_hi:[1,0,0]\n\t"
      "v_mfma_f32_16x16x32_bf16 v[220:223], v[52:55], v[128:131], v[220:223]\n\t"
      "v_fma_mix_f32 v34, v30, v20, -v22 op_sel:[0,0,0] op_sel_hi:[1,0,0]\n\t"
      "v_fma_mix_f32 v35, v31, v24, -v22 op_sel:[0,0,0] op_sel_hi:[1,0,0]\n\t"
      "v_mfma_f32_16x16x32_bf16 v[216:219], v[48:51], v[128:131], v[216:219]\n\t"
      "ds_read_b128 v[128:131], v6 offset:18432\n\t"
      "v_fma_mix_f32 v36, v28, v20, -v22 op_sel:[1,0,0] op_sel_hi:[1,0,0]\n\t"
      "v_mfma_f32_16x16x32_bf16 v[216:219], v[40:43], v[132:135], v[216:219]\n\t"
      "ds_read_b128 v[132:135], v7 offset:18432\n\t"
      "v_fma_mix_f32 v37, v29, v24, -v22 op_sel:[1,0,0] op_sel_hi:[1,0,0]\n\t"
      "v_mfma_f32_16x16x32_bf16 v[224:227], v[40:43], v[124:127], v[224:227]\n\t"
      "v_cvt_pk_bf16_f32 v64, v32, v33\n\t"
      "s_waitcnt lgkmcnt(3)\n\t"
      "v_mfma_f32_16x16x32_bf16 v[224:227], v[48:51], v[120:123], v[224:227]\n\t"
      "v_cvt_pk_bf16_f32 v65, v34, v35\n\t"
      "v_fma_mix_f32 v38, v30, v20, -v22 op_sel:[1,0,0] op_sel_hi:[1,0,0]\n\t"
      "v_mfma_f32_16x16x32_bf16 v[228:231], v[52:55], v[120:123], v[228:231]\n\t"
      "ds_read_b128 v[120:123], v6 offset:20480\n\t"
      "v_fma_mix_f32 v39, v31, v24, -v22 op_sel:[1,0,0] op_sel_hi:[1,0,0]\n\t"
      "v_mfma_f32_16x16x32_bf16 v[228:231], v[44:47], v[124:127], v[228:231]\n\t"
      "ds_read_b128 v[124:127], v7 offset:20480\n\t"
      "v_cvt_pk_bf16_f32 v66, v36, v37\n\t"
      "v_mfma_f32_16x16x32_bf16 v[236:239], v[44:47], v[116:119], v[236:239]\n\t"
      "v_cvt_pk_bf16_f32 v67, v38, v39\n\t"
      "v_lshrrev_b32 v31, 8, v19\n\t"
      "v_mfma_f32_16x16x32_bf16 v[236:239], v[52:55], v[112:115], v[236:239]\n\t"
      "v_and_b32 v28, v13, v19\n\t"
      "v_and_b32 v29, v14, v19\n\t"
      "v_mfma_f32_16x16x32_bf16 v[232:235], v[48:51], v[112:115], v[232:235]\n\t"
      "ds_read_b128 v[112:115], v6 offset:22528\n\t"
      "v_and_b32 v30, v13, v31\n\t"
      "v_mfma_f32_16x16x32_bf16 v[232:235], v[40:43], v[116:119], v[232:235]\n\t"
      "ds_read_b128 v[116:119], v7 offset:22528\n\t"
      "v_and_b32 v31, v14, v31\n\t"
      "v_mfma_f32_16x16x32_bf16 v[240:243], v[40:43], v[108:111], v[240:243]\n\t"
      "ds_read_b64 v[18:19], v10 offset:2048\n\t"
      "v_fma_mix_f32 v32, v28, v21, -v23 op_sel:[0,0,0] op_sel_hi:[1,0,0]\n\t"
      "v_mfma_f32_16x16x32_bf16 v[240:243], v[48:51], v[104:107], v[240:243]\n\t"
      "v_fma_mix_f32 v33, v29, v25, -v23 op_sel:[0,0,0] op_sel_hi:[1,0,0]\n\t"
      "v_fma_mix_f32 v34, v30, v21, -v23 op_sel:[0,0,0] op_sel_hi:[1,0,0]\n\t"
      "v_mfma_f32_16x16x32_bf16 v[244:247], v[52:55], v[104:107], v[244:247]\n\t"
      "ds_read_b128 v[104:107], v6 offset:24576\n\t"
      "v_fma_mix_f32 v35, v31, v25, -v23 op_sel:[0,0,0] op_sel_hi:[1,0,0]\n\t"
      "v_mfma_f32_16x16x32_bf16 v[244:247], v[44:47], v[108:111], v[244:247]\n\t"
      "ds_read_b128 v[108:111], v7 offset:24576\n\t"
      "v_fma_mix_f32 v36, v28, v21, -v23 op_sel:[1,0,0] op_sel_hi:[1,0,0]\n\t"
      "v_mfma_f32_16x16x32_bf16 v[252:255], v[44:47], v[100:103], v[252:255]\n\t"
      "v_fma_mix_f32 v37, v29, v25, -v23 op_sel:[1,0,0] op_sel_hi:[1,0,0]\n\t"
      "v_cvt_pk_bf16_f32 v68, v32, v33\n\t"
      "v_mfma_f32_16x16x32_bf16 v[252:255], v[52:55], v[96:99], v[252:255]\n\t"
      "v_cvt_pk_bf16_f32 v69, v34, v35\n\t"
      "v_fma_mix_f32 v38, v30, v21, -v23 op_sel:[1,0,0] op_sel_hi:[1,0,0]\n\t"
      "v_mfma_f32_16x16x32_bf16 v[248:251], v[48:51], v[96:99], v[248:251]\n\t"
      "ds_read_b128 v[96:99], v6 offset:26624\n\t"
      "v_fma_mix_f32 v39, v31, v25, -v23 op_sel:[1,0,0] op_sel_hi:[1,0,0]\n\t"
      "v_mfma_f32_16x16x32_bf16 v[248:251], v[40:43], v[100:103], v[248:251]\n\t"
      "ds_read_b128 v[100:103], v7 offset:26624\n\t"
      "v_cvt_pk_bf16_f32 v70, v36, v37\n\t"
      "v_cvt_pk_bf16_f32 v71, v38, v39\n\t"
      "v_mfma_f32_16x16x32_bf16 v[144:147], v[56:59], v[92:95], v[144:147]\n\t"
      "v_lshlrev_b32 v28, 16, v26\n\t"
      "v_and_b32 v29, 0xffff0000, v26\n\t"
      "v_mfma_f32_16x16x32_bf16 v[144:147], v[64:67], v[88:91], v[144:147]\n\t"
      "v_lshlrev_b32 v30, 16, v27\n\t"
      "v_and_b32 v31, 0xffff0000, v27\n\t"
      "v_mfma_f32_16x16x32_bf16 v[148:151], v[68:71], v[88:91], v[148:151]\n\t"
      "ds_read_b128 v[88:91], v6 offset:28672\n\t"
      "v_mul_f32 v20, 0x4b800000, v28\n\t"
      "v_mfma_f32_16x16x32_bf16 v[148:151], v[60:63], v[92:95], v[148:151]\n\t"
      "ds_read_b128 v[92:95], v7 offset:28672\n\t"
      "v_mul_f32 v24, 0x49800000, v28\n\t"
      "v_mfma_f32_16x16x32_bf16 v[156:159], v[60:63], v[84:87], v[156:159]\n\t"
      "v_mul_f32 v22, v28, v30\n\t"
      "v_mul_f32 v21, 0x4b800000, v29\n\t"
      "v_mfma_f32_16x16x32_bf16 v[156:159], v[68:71], v[80:83], v[156:159]\n\t"
      "v_mul_f32 v25, 0x49800000, v29\n\t"
      "v_mul_f32 v23, v29, v31\n\t"
      "v_mfma_f32_16x16x32_bf16 v[152:155], v[64:67], v[80:83], v[152:155]\n\t"
      "ds_read_b128 v[80:83], v6 offset:30720\n\t"
      "ds_read2st64_b32 v[26:27], v11 offset0:7 offset1:23\n\t"
      "v_mfma_f32_16x16x32_bf16 v[152:155], v[56:59], v[84:87], v[152:155]\n\t"
      "ds_read_b128 v[84:87], v7 offset:30720\n\t"
      "v_lshrrev_b32 v31, 8, v16\n\t"
      "v_mfma_f32_16x16x32_bf16 v[160:163], v[56:59], v[136:139], v[160:163]\n\t"
      "v_and_b32 v28, v13, v16\n\t"
      "s_waitcnt lgkmcnt(4)\n\t"
      "v_mfma_f32_16x16x32_bf16 v[160:163], v[64:67], v[140:143], v[160:163]\n\t"
      "v_and_b32 v29, v14, v16\n\t"
      "v_and_b32 v30, v13, v31\n\t"
      "v_and_b32 v31, v14, v31\n\t"
      "v_mfma_f32_16x16x32_bf16 v[164:167], v[68:71], v[140:143], v[164:167]\n\t"
      "ds_read_b128 v[140:143], v6 offset:32768\n\t"
      "v_fma_mix_f32 v32, v28, v20, -v22 op_sel:[0,0,0] op_sel_hi:[1,0,0]\n\t"
      "v_mfma_f32_16x16x32_bf16 v[164:167], v[60:63], v[136:139], v[164:167]\n\t"
      "ds_read_b128 v[136:139], v7 offset:32768\n\t"
      "v_fma_mix_f32 v33, v29, v24, -v22 op_sel:[0,0,0] op_sel_hi:[1,0,0]\n\t"
      "v_mfma_f32_16x16x32_bf16 v[172:175], v[60:63], v[128:131], v[172:175]\n\t"
      "v_fma_mix_f32 v34, v30, v20, -v22 op_sel:[0,0,0] op_sel_hi:[1,0,0]\n\t"
      "v_fma_mix_f32 v35, v31, v24, -v22 op_sel:[0,0,0] op_sel_hi:[1,0,0]\n\t"
      "v_mfma_f32_16x16x32_bf16 v[172:175], v[68:71], v[132:135], v[172:175]\n\t"
      "v_fma_mix_f32 v36, v28, v20, -v22 op_sel:[1,0,0] op_sel_hi:[1,0,0]\n\t"
      "v_fma_mix_f32 v37, v29, v24, -v22 op_sel:[1,0,0] op_sel_hi:[1,0,0]\n\t"
      "v_mfma_f32_16x16x32_bf16 v[168:171], v[64:67], v[132:135], v[168:171]\n\t"
      "ds_read_b128 v[132:135], v6 offset:34816\n\t"
      "v_cvt_pk_bf16_f32 v40, v32, v33\n\t"
      "v_mfma_f32_16x16x32_bf16 v[168:171], v[56:59], v[128:131], v[168:171]\n\t"
      "ds_read_b128 v[128:131], v7 offset:34816\n\t"
      "v_cvt_pk_bf16_f32 v41, v34, v35\n\t"
      "v_mfma_f32_16x16x32_bf16 v[176:179], v[56:59], v[120:123], v[176:179]\n\t"
      "v_fma_mix_f32 v38, v30, v20, -v22 op_sel:[1,0,0] op_sel_hi:[1,0,0]\n\t"
      "v_fma_mix_f32 v39, v31, v24, -v22 op_sel:[1,0,0] op_sel_hi:[1,0,0]\n\t"
      "v_mfma_f32_16x16x32_bf16 v[176:179], v[64:67], v[124:127], v[176:179]\n\t"
      "v_cvt_pk_bf16_f32 v42, v36, v37\n\t"
      "v_cvt_pk_bf16_f32 v43, v38, v39\n\t"
      "v_mfma_f32_16x16x32_bf16 v[180:183], v[68:71], v[124:127], v[180:183]\n\t"
      "ds_read_b128 v[124:127], v6 offset:36864\n\t"
      "v_lshrrev_b32 v31, 8, v17\n\t"
      "v_mfma_f32_16x16x32_bf16 v[180:183], v[60:63], v[120:123], v[180:183]\n\t"
      "ds_read_b128 v[120:123], v7 offset:36864\n\t"
      "v_and_b32 v28, v13, v17\n\t"
      "v_mfma_f32_16x16x32_bf16 v[188:191], v[60:63], v[112:115], v[188:191]\n\t"
      "v_and_b32 v29, v14, v17\n\t"
      "v_and_b32 v30, v13, v31\n\t"
      "v_mfma_f32_16x16x32_bf16 v[188:191], v[68:71], v[116:119], v[188:191]\n\t"
      "v_and_b32 v31, v14, v31\n\t"
      "ds_read_b64 v[16:17], v10 offset:4096\n\t"
      "v_mfma_f32_16x16x32_bf16 v[184:187], v[64:67], v[116:119], v[184:187]\n\t"
      "ds_read_b128 v[116:119], v6 offset:38912\n\t"
      "v_fma_mix_f32 v32, v28, v21, -v23 op_sel:[0,0,0] op_sel_hi:[1,0,0]\n\t"
      "v_mfma_f32_16x16x32_bf16 v[184:187], v[56:59], v[112:115], v[184:187]\n\t"
      "ds_read_b128 v[112:115], v7 offset:38912\n\t"
      "v_fma_mix_f32 v33, v29, v25, -v23 op_sel:[0,0,0] op_sel_hi:[1,0,0]\n\t"
      "v_mfma_f32_16x16x32_bf16 v[192:195], v[56:59], v[104:107], v[192:195]\n\t"
      "v_fma_mix_f32 v34, v30, v21, -v23 op_sel:[0,0,0] op_sel_hi:[1,0,0]\n\t"
      "v_fma_mix_f32 v35, v31, v25, -v23 op_sel:[0,0,0] op_sel_hi:[1,0,0]\n\t"
      "v_mfma_f32_16x16x32_bf16 v[192:195], v[64:67], v[108:111], v[192:195]\n\t"
      "s_barrier\n\t"
      "v_mfma_f32_16x16x32_bf16 v[196:199], v[68:71], v[108:111], v[196:199]\n\t"
      "ds_read_b128 v[108:111], v6 offset:40960\n\t"
      "v_fma_mix_f32 v36, v28, v21, -v23 op_sel:[1,0,0] op_sel_hi:[1,0,0]\n\t"
      "v_mfma_f32_16x16x32_bf16 v[196:199], v[60:63], v[104:107], v[196:199]\n\t"
      "ds_read_b128 v[104:107], v7 offset:40960\n\t"
      "v_fma_mix_f32 v37, v29, v25, -v23 op_sel:[1,0,0] op_sel_hi:[1,0,0]\n\t"
      "v_mfma_f32_16x16x32_bf16 v[204:207], v[60:63], v[96:99], v[204:207]\n\t"
      "v_cvt_pk_bf16_f32 v44, v32, v33\n\t"
      "v_cvt_pk_bf16_f32 v45, v34, v35\n\t"
      "v_mfma_f32_16x16x32_bf16 v[204:207], v[68:71], v[100:103], v[204:207]\n\t"
      "v_fma_mix_f32 v38, v30, v21, -v23 op_sel:[1,0,0] op_sel_hi:[1,0,0]\n\t"
      "v_fma_mix_f32 v39, v31, v25, -v23 op_sel:[1,0,0] op_sel_hi:[1,0,0]\n\t"
      "v_mfma_f32_16x16x32_bf16 v[200:203], v[64:67], v[100:103], v[200:203]\n\t"
      "ds_read_b128 v[100:103], v6 offset:43008\n\t"
      "v_cvt_pk_bf16_f32 v46, v36, v37\n\t"
      "v_mfma_f32_16x16x32_bf16 v[200:203], v[56:59], v[96:99], v[200:203]\n\t"
      "ds_read_b128 v[96:99], v7 offset:43008\n\t"
      "v_cvt_pk_bf16_f32 v47, v38, v39\n\t"
      "v_mfma_f32_16x16x32_bf16 v[208:211], v[56:59], v[88:91], v[208:211]\n\t"
      "v_lshrrev_b32 v31, 8, v18\n\t"
      "s_waitcnt lgkmcnt(3)\n\t"
      "v_mfma_f32_16x16x32_bf16 v[208:211], v[64:67], v[92:95], v[208:211]\n\t"
      "v_and_b32 v28, v13, v18\n\t"
      "v_and_b32 v29, v14, v18\n\t"
      "v_mfma_f32_16x16x32_bf16 v[212:215], v[68:71], v[92:95], v[212:215]\n\t"
      "ds_read_b128 v[92:95], v6 offset:45056\n\t"
      "v_and_b32 v30, v13, v31\n\t"
      "v_mfma_f32_16x16x32_bf16 v[212:215], v[60:63], v[88:91], v[212:215]\n\t"
      "ds_read_b128 v[88:91], v7 offset:45056\n\t"
      "v_and_b32 v31, v14, v31\n\t"
      "v_mfma_f32_16x16x32_bf16 v[220:223], v[60:63], v[80:83], v[220:223]\n\t"
      "v_fma_mix_f32 v32, v28, v20, -v22 op_sel:[0,0,0] op_sel_hi:[1,0,0]\n\t"
      "v_fma_mix_f32 v33, v29, v24, -v22 op_sel:[0,0,0] op_sel_hi:[1,0,0]\n\t"
      "v_mfma_f32_16x16x32_bf16 v[220:223], v[68:71], v[84:87], v[220:223]\n\t"
      "v_fma_mix_f32 v34, v30, v20, -v22 op_sel:[0,0,0] op_sel_hi:[1,0,0]\n\t"
      "v_fma_mix_f32 v35, v31, v24, -v22 op_sel:[0,0,0] op_sel_hi:[1,0,0]\n\t"
      "v_mfma_f32_16x16x32_bf16 v[216:219], v[64:67], v[84:87], v[216:219]\n\t"
      "ds_read_b128 v[84:87], v6 offset:47104\n\t"
      "v_fma_mix_f32 v36, v28, v20, -v22 op_sel:[1,0,0] op_sel_hi:[1,0,0]\n\t"
      "v_mfma_f32_16x16x32_bf16 v[216:219], v[56:59], v[80:83], v[216:219]\n\t"
      "ds_read_b128 v[80:83], v7 offset:47104\n\t"
      "v_fma_mix_f32 v37, v29, v24, -v22 op_sel:[1,0,0] op_sel_hi:[1,0,0]\n\t"
      "v_mfma_f32_16x16x32_bf16 v[224:227], v[56:59], v[140:143], v[224:227]\n\t"
      "v_cvt_pk_bf16_f32 v48, v32, v33\n\t"
      "v_cvt_pk_bf16_f32 v49, v34, v35\n\t"
      "v_mfma_f32_16x16x32_bf16 v[224:227], v[64:67], v[136:139], v[224:227]\n\t"
      "v_fma_mix_f32 v38, v30, v20, -v22 op_sel:[1,0,0] op_sel_hi:[1,0,0]\n\t"
      "v_fma_mix_f32 v39, v31, v24, -v22 op_sel:[1,0,0] op_sel_hi:[1,0,0]\n\t"
      "v_mfma_f32_16x16x32_bf16 v[228:231], v[68:71], v[136:139], v[228:231]\n\t"
      "ds_read_b128 v[136:139], v6 offset:49152\n\t"
      "v_cvt_pk_bf16_f32 v50, v36, v37\n\t"
      "v_mfma_f32_16x16x32_bf16 v[228:231], v[60:63], v[140:143], v[228:231]\n\t"
      "ds_read_b128 v[140:143], v7 offset:49152\n\t"
      "v_cvt_pk_bf16_f32 v51, v38, v39\n\t"
      "v_mfma_f32_16x16x32_bf16 v[236:239], v[60:63], v[132:135], v[236:239]\n\t"
      "v_lshrrev_b32 v31, 8, v19\n\t"
      "v_and_b32 v28, v13, v19\n\t"
      "v_mfma_f32_16x16x32_bf16 v[236:239], v[68:71], v[128:131], v[236:239]\n\t"
      "v_and_b32 v29, v14, v19\n\t"
      "v_and_b32 v30, v13, v31\n\t"
      "v_mfma_f32_16x16x32_bf16 v[232:235], v[64:67], v[128:131], v[232:235]\n\t"
      "ds_read_b128 v[128:131], v6 offset:51200\n\t"
      "v_and_b32 v31, v14, v31\n\t"
      "v_mfma_f32_16x16x32_bf16 v[232:235], v[56:59], v[132:135], v[232:235]\n\t"
      "ds_read_b128 v[132:135], v7 offset:51200\n\t"
      "ds_read_b64 v[18:19], v10 offset:6144\n\t"
      "v_mfma_f32_16x16x32_bf16 v[240:243], v[56:59], v[124:127], v[240:243]\n\t"
      "v_fma_mix_f32 v32, v28, v21, -v23 op_sel:[0,0,0] op_sel_hi:[1,0,0]\n\t"
      "v_fma_mix_f32 v33, v29, v25, -v23 op_sel:[0,0,0] op_sel_hi:[1,0,0]\n\t"
      "v_mfma_f32_16x16x32_bf16 v[240:243], v[64:67], v[120:123], v[240:243]\n\t"
      "v_fma_mix_f32 v34, v30, v21, -v23 op_sel:[0,0,0] op_sel_hi:[1,0,0]\n\t"
      "v_fma_mix_f32 v35, v31, v25, -v23 op_sel:[0,0,0] op_sel_hi:[1,0,0]\n\t"
      "v_mfma_f32_16x16x32_bf16 v[244:247], v[68:71], v[120:123], v[244:247]\n\t"
      "ds_read_b128 v[120:123], v6 offset:53248\n\t"
      "v_fma_mix_f32 v36, v28, v21, -v23 op_sel:[1,0,0] op_sel_hi:[1,0,0]\n\t"
      "v_mfma_f32_16x16x32_bf16 v[244:247], v[60:63], v[124:127], v[244:247]\n\t"
      "ds_read_b128 v[124:127], v7 offset:53248\n\t"
      "v_fma_mix_f32 v37, v29, v25, -v23 op_sel:[1,0,0] op_sel_hi:[1,0,0]\n\t"
      "v_mfma_f32_16x16x32_bf16 v[252:255], v[60:63], v[116:119], v[252:255]\n\t"
      "v_cvt_pk_bf16_f32 v52, v32, v33\n\t"
      "v_cvt_pk_bf16_f32 v53, v34, v35\n\t"
      "v_mfma_f32_16x16x32_bf16 v[252:255], v[68:71], v[112:115], v[252:255]\n\t"
      "v_fma_mix_f32 v38, v30, v21, -v23 op_sel:[1,0,0] op_sel_hi:[1,0,0]\n\t"
      "v_fma_mix_f32 v39, v31, v25, -v23 op_sel:[1,0,0] op_sel_hi:[1,0,0]\n\t"
      "v_mfma_f32_16x16x32_bf16 v[248:251], v[64:67], v[112:115], v[248:251]\n\t"
      "ds_read_b128 v[112:115], v6 offset:55296\n\t"
      "v_cvt_pk_bf16_f32 v54, v36, v37\n\t"
      "v_mfma_f32_16x16x32_bf16 v[248:251], v[56:59], v[116:119], v[248:251]\n\t"
      "ds_read_b128 v[116:119], v7 offset:55296\n\t"
      "v_cvt_pk_bf16_f32 v55, v38, v39\n\t"
      "v_mfma_f32_16x16x32_bf16 v[144:147], v[40:43], v[108:111], v[144:147]\n\t"
      "v_lshlrev_b32 v28, 16, v26\n\t"
      "s_waitcnt lgkmcnt(3)\n\t"
      "v_mfma_f32_16x16x32_bf16 v[144:147], v[48:51], v[104:107], v[144:147]\n\t"
      "v_and_b32 v29, 0xffff0000, v26\n\t"
      "v_lshlrev_b32 v30, 16, v27\n\t"
      "v_and_b32 v31, 0xffff0000, v27\n\t"
      "v_mfma_f32_16x16x32_bf16 v[148:151], v[52:55], v[104:107], v[148:151]\n\t"
      "ds_read_b128 v[104:107], v6 offset:57344\n\t"
      "v_mul_f32 v20, 0x4b800000, v28\n\t"
      "v_mfma_f32_16x16x32_bf16 v[148:151], v[44:47], v[108:111], v[148:151]\n\t"
      "ds_read_b128 v[108:111], v7 offset:57344\n\t"
      "v_mul_f32 v24, 0x49800000, v28\n\t"
      "v_mfma_f32_16x16x32_bf16 v[156:159], v[44:47], v[100:103], v[156:159]\n\t"
      "v_mul_f32 v22, v28, v30\n\t"
      "v_mul_f32 v21, 0x4b800000, v29\n\t"
      "v_mfma_f32_16x16x32_bf16 v[156:159], v[52:55], v[96:99], v[156:159]\n\t"
      "v_mul_f32 v25, 0x49800000, v29\n\t"
      "v_mul_f32 v23, v29, v31\n\t"
      "v_mfma_f32_16x16x32_bf16 v[152:155], v[48:51], v[96:99], v[152:155]\n\t"
      "ds_read_b128 v[96:99], v6 offset:59392\n\t"
      "ds_read2st64_b32 v[26:27], v11 offset0:8 offset1:24\n\t"
      "v_mfma_f32_16x16x32_bf16 v[152:155], v[40:43], v[100:103], v[152:155]\n\t"
      "ds_read_b128 v[100:103], v7 offset:59392\n\t"
      "v_lshrrev_b32 v31, 8, v16\n\t"
      "v_mfma_f32_16x16x32_bf16 v[160:163], v[40:43], v[92:95], v[160:163]\n\t"
      "v_and_b32 v28, v13, v16\n\t"
      "v_and_b32 v29, v14, v16\n\t"
      "v_mfma_f32_16x16x32_bf16 v[160:163], v[48:51], v[88:91], v[160:163]\n\t"
      "v_and_b32 v30, v13, v31\n\t"
      "v_and_b32 v31, v14, v31\n\t"
      "v_mfma_f32_16x16x32_bf16 v[164:167], v[52:55], v[88:91], v[164:167]\n\t"
      "ds_read_b128 v[88:91], v6 offset:61440\n\t"
      "v_fma_mix_f32 v32, v28, v20, -v22 op_sel:[0,0,0] op_sel_hi:[1,0,0]\n\t"
      "v_mfma_f32_16x16x32_bf16 v[164:167], v[44:47], v[92:95], v[164:167]\n\t"
      "ds_read_b128 v[92:95], v7 offset:61440\n\t"
      "v_fma_mix_f32 v33, v29, v24, -v22 op_sel:[0,0,0] op_sel_hi:[1,0,0]\n\t"
      "v_mfma_f32_16x16x32_bf16 v[172:175], v[44:47], v[84:87], v[172:175]\n\t"
      "v_fma_mix_f32 v34, v30, v20, -v22 op_sel:[0,0,0] op_sel_hi:[1,0,0]\n\t"
      "v_fma_mix_f32 v35, v31, v24, -v22 op_sel:[0,0,0] op_sel_hi:[1,0,0]\n\t"
      "v_mfma_f32_16x16x32_bf16 v[172:175], v[52:55], v[80:83], v[172:175]\n\t"
      "v_fma_mix_f32 v36, v28, v20, -v22 op_sel:[1,0,0] op_sel_hi:[1,0,0]\n\t"
      "v_fma_mix_f32 v37, v29, v24, -v22 op_sel:[1,0,0] op_sel_hi:[1,0,0]\n\t"
      "v_mfma_f32_16x16x32_bf16 v[168:171], v[48:51], v[80:83], v[168:171]\n\t"
      "ds_read_b128 v[80:83], v6 offset:63488\n\t"
      "v_cvt_pk_bf16_f32 v56, v32, v33\n\t"
      "v_mfma_f32_16x16x32_bf16 v[168:171], v[40:43], v[84:87], v[168:171]\n\t"
      "ds_read_b128 v[84:87], v7 offset:63488\n\t"
      "v_cvt_pk_bf16_f32 v57, v34, v35\n\t"
      "v_mfma_f32_16x16x32_bf16 v[176:179], v[40:43], v[136:139], v[176:179]\n\t"
      "v_fma_mix_f32 v38, v30, v20, -v22 op_sel:[1,0,0] op_sel_hi:[1,0,0]\n\t"
      "v_fma_mix_f32 v39, v31, v24, -v22 op_sel:[1,0,0] op_sel_hi:[1,0,0]\n\t"
      "v_mfma_f32_16x16x32_bf16 v[176:179], v[48:51], v[140:143], v[176:179]\n\t"
      "v_cvt_pk_bf16_f32 v58, v36, v37\n\t"
      "v_cvt_pk_bf16_f32 v59, v38, v39\n\t"
      "v_mfma_f32_16x16x32_bf16 v[180:183], v[52:55], v[140:143], v[180:183]\n\t"
      "ds_read_b128 v[140:143], v8 offset:0\n\t"
      "v_lshrrev_b32 v31, 8, v17\n\t"
      "v_mfma_f32_16x16x32_bf16 v[180:183], v[44:47], v[136:139], v[180:183]\n\t"
      "ds_read_b128 v[136:139], v9 offset:0\n\t"
      "v_and_b32 v28, v13, v17\n\t"
      "v_mfma_f32_16x16x32_bf16 v[188:191], v[44:47], v[128:131], v[188:191]\n\t"
      "v_and_b32 v29, v14, v17\n\t"
      "v_and_b32 v30, v13, v31\n\t"
      "v_mfma_f32_16x16x32_bf16 v[188:191], v[52:55], v[132:135], v[188:191]\n\t"
      "v_and_b32 v31, v14, v31\n\t"
      "ds_read_b64 v[16:17], v10 offset:8192\n\t"
      "v_mfma_f32_16x16x32_bf16 v[184:187], v[48:51], v[132:135], v[184:187]\n\t"
      "ds_read_b128 v[132:135], v8 offset:2048\n\t"
      "v_fma_mix_f32 v32, v28, v21, -v23 op_sel:[0,0,0] op_sel_hi:[1,0,0]\n\t"
      "v_mfma_f32_16x16x32_bf16 v[184:187], v[40:43], v[128:131], v[184:187]\n\t"
      "ds_read_b128 v[128:131], v9 offset:2048\n\t"
      "v_fma_mix_f32 v33, v29, v25, -v23 op_sel:[0,0,0] op_sel_hi:[1,0,0]\n\t"
      "v_mfma_f32_16x16x32_bf16 v[192:195], v[40:43], v[120:123], v[192:195]\n\t"
      "v_fma_mix_f32 v34, v30, v21, -v23 op_sel:[0,0,0] op_sel_hi:[1,0,0]\n\t"
      "s_waitcnt lgkmcnt(4)\n\t"
      "v_mfma_f32_16x16x32_bf16 v[192:195], v[48:51], v[124:127], v[192:195]\n\t"
      "s_barrier\n\t"
      "v_mfma_f32_16x16x32_bf16 v[196:199], v[52:55], v[124:127], v[196:199]\n\t"
      "ds_read_b128 v[124:127], v8 offset:4096\n\t"
      "LH14x2_%=:\n\t"
      "s_cbranch_execz LHRET_%=\n\t"
      "v_fma_mix_f32 v35, v31, v25, -v23 op_sel:[0,0,0] op_sel_hi:[1,0,0]\n\t"
      "v_mfma_f32_16x16x32_bf16 v[196:199], v[44:47], v[120:123], v[196:199]\n\t"
      "ds_read_b128 v[120:123], v9 offset:4096\n\t"
      "v_fma_mix_f32 v36, v28, v21, -v23 op_sel:[1,0,0] op_sel_hi:[1,0,0]\n\t"
      "v_mfma_f32_16x16x32_bf16 v[204:207], v[44:47], v[112:115], v[204:207]\n\t"
      "v_fma_mix_f32 v37, v29, v25, -v23 op_sel:[1,0,0] op_sel_hi:[1,0,0]\n\t"
      "v_cvt_pk_bf16_f32 v60, v32, v33\n\t"
      "v_mfma_f32_16x16x32_bf16 v[204:207], v[52:55], v[116:119], v[204:207]\n\t"
      "v_cvt_pk_bf16_f32 v61, v34, v35\n\t"
      "v_fma_mix_f32 v38, v30, v21, -v23 op_sel:[1,0,0] op_sel_hi:[1,0,0]\n\t"
      "v_mfma_f32_16x16x32_bf16 v[200:203], v[48:51], v[116:119], v[200:203]\n\t"
      "ds_read_b128 v[116:119], v8 offset:6144\n\t"
      "v_fma_mix_f32 v39, v31, v25, -v23 op_sel:[1,0,0] op_sel_hi:[1,0,0]\n\t"
      "v_mfma_f32_16x16x32_bf16 v[200:203], v[40:43], v[112:115], v[200:203]\n\t"
      "ds_read_b128 v[112:115], v9 offset:6144\n\t"
      "v_cvt_pk_bf16_f32 v62, v36, v37\n\t"
      "v_mfma_f32_16x16x32_bf16 v[208:211], v[40:43], v[104:107], v[208:211]\n\t"
      "v_cvt_pk_bf16_f32 v63, v38, v39\n\t"
      "v_lshrrev_b32 v31, 8, v18\n\t"
      "v_mfma_f32_16x16x32_bf16 v[208:211], v[48:51], v[108:111], v[208:211]\n\t"
      "v_and_b32 v28, v13, v18\n\t"
      "v_and_b32 v29, v14, v18\n\t"
      "v_mfma_f32_16x16x32_bf16 v[212:215], v[52:55], v[108:111], v[212:215]\n\t"
      "ds_read_b128 v[108:111], v8 offset:8192\n\t"
      "v_and_b32 v30, v13, v31\n\t"
      "v_mfma_f32_16x16x32_bf16 v[212:215], v[44:47], v[104:107], v[212:215]\n\t"
      "ds_read_b128 v[104:107], v9 offset:8192\n\t"
      "v_and_b32 v31, v14, v31\n\t"
      "v_mfma_f32_16x16x32_bf16 v[220:223], v[44:47], v[96:99], v[220:223]\n\t"
      "v_fma_mix_f32 v32, v28, v20, -v22 op_sel:[0,0,0] op_sel_hi:[1,0,0]\n\t"
      "v_fma_mix_f32 v33, v29, v24, -v22 op_sel:[0,0,0] op_sel_hi:[1,0,0]\n\t"
      "v_mfma_f32_16x16x32_bf16 v[220:223], v[52:55], v[100:103], v[220:223]\n\t"
      "v_fma_mix_f32 v34, v30, v20, -v22 op_sel:[0,0,0] op_sel_hi:[1,0,0]\n\t"
      "v_fma_mix_f32 v35, v31, v24, -v22 op_sel:[0,0,0] op_sel_hi:[1,0,0]\n\t"
      "v_mfma_f32_16x16x32_bf16 v[216:219], v[48:51], v[100:103], v[216:219]\n\t"
      "ds_read_b128 v[100:103], v8 offset:10240\n\t"
      "v_fma_mix_f32 v36, v28, v20, -v22 op_sel:[1,0,0] op_sel_hi:[1,0,0]\n\t"
      "v_mfma_f32_16x16x32_bf16 v[216:219], v[40:43], v[96:99], v[216:219]\n\t"
      "ds_read_b128 v[96:99], v9 offset:10240\n\t"
      "v_fma_mix_f32 v37, v29, v24, -v22 op_sel:[1,0,0] op_sel_hi:[1,0,0]\n\t"
      "v_mfma_f32_16x16x32_bf16 v[224:227], v[40:43], v[88:91], v[224:227]\n\t"
      "v_cvt_pk_bf16_f32 v64, v32, v33\n\t"
      "v_cvt_pk_bf16_f32 v65, v34, v35\n\t"
      "v_mfma_f32_16x16x32_bf16 v[224:227], v[48:51], v[92:95], v[224:227]\n\t"
      "v_fma_mix_f32 v38, v30, v20, -v22 op_sel:[1,0,0] op_sel_hi:[1,0,0]\n\t"
      "v_fma_mix_f32 v39, v31, v24, -v22 op_sel:[1,0,0] op_sel_hi:[1,0,0]\n\t"
      "v_mfma_f32_16x16x32_bf16 v[228:231], v[52:55], v[92:95], v[228:231]\n\t"
      "ds_read_b128 v[92:95], v8 offset:12288\n\t"
      "v_cvt_pk_bf16_f32 v66, v36, v37\n\t"
      "v_mfma_f32_16x16x32_bf16 v[228:231], v[44:47], v[88:91], v[228:231]\n\t"
      "ds_read_b128 v[88:91], v9 offset:12288\n\t"
      "v_cvt_pk_bf16_f32 v67, v38, v39\n\t"
      "v_mfma_f32_16x16x32_bf16 v[236:239], v[44:47], v[80:83], v[236:239]\n\t"
      "v_lshrrev_b32 v31, 8, v19\n\t"
      "v_and_b32 v28, v13, v19\n\t"
      "v_mfma_f32_16x16x32_bf16 v[236:239], v[52:55], v[84:87], v[236:239]\n\t"
      "v_and_b32 v29, v14, v19\n\t"
      "v_and_b32 v30, v13, v31\n\t"
      "v_mfma_f32_16x16x32_bf16 v[232:235], v[48:51], v[84:87], v[232:235]\n\t"
      "ds_read_b128 v[84:87], v8 offset:14336\n\t"
      "v_and_b32 v31, v14, v31\n\t"
      "v_mfma_f32_16x16x32_bf16 v[232:235], v[40:43], v[80:83], v[232:235]\n\t"
      "ds_read_b128 v[80:83], v9 offset:14336\n\t"
      "ds_read_b64 v[18:19], v10 offset:10240\n\t"
      "v_mfma_f32_16x16x32_bf16 v[240:243], v[40:43], v[140:143], v[240:243]\n\t"
      "v_fma_mix_f32 v32, v28, v21, -v23 op_sel:[0,0,0] op_sel_hi:[1,0,0]\n\t"
      "s_waitcnt lgkmcnt(4)\n\t"
      "v_mfma_f32_16x16x32_bf16 v[240:243], v[48:51], v[136:139], v[240:243]\n\t"
      "v_fma_mix_f32 v33, v29, v25, -v23 op_sel:[0,0,0] op_sel_hi:[1,0,0]\n\t"
      "v_fma_mix_f32 v34, v30, v21, -v23 op_sel:[0,0,0] op_sel_hi:[1,0,0]\n\t"
      "v_mfma_f32_16x16x32_bf16 v[244:247], v[52:55], v[136:139], v[244:247]\n\t"
      "ds_read_b128 v[136:139], v8 offset:16384\n\t"
      "v_fma_mix_f32 v35, v31, v25, -v23 op_sel:[0,0,0] op_sel_hi:[1,0,0]\n\t"
      "v_mfma_f32_16x16x32_bf16 v[244:247], v[44:47], v[140:143], v[244:247]\n\t"
      "ds_read_b128 v[140:143], v9 offset:16384\n\t"
      "v_fma_mix_f32 v36, v28, v21, -v23 op_sel:[1,0,0] op_sel_hi:[1,0,0]\n\t"
      "v_mfma_f32_16x16x32_bf16 v[252:255], v[44:47], v[132:135], v[252:255]\n\t"
      "v_fma_mix_f32 v37, v29, v25, -v23 op_sel:[1,0,0] op_sel_hi:[1,0,0]\n\t"
      "v_cvt_pk_bf16_f32 v68, v32, v33\n\t"
      "v_mfma_f32_16x16x32_bf16 v[252:255], v[52:55], v[128:131], v[252:255]\n\t"
      "v_cvt_pk_bf16_f32 v69, v34, v35\n\t"
      "v_fma_mix_f32 v38, v30, v21, -v23 op_sel:[1,0,0] op_sel_hi:[1,0,0]\n\t"
      "v_mfma_f32_16x16x32_bf16 v[248:251], v[48:51], v[128:131], v[248:251]\n\t"
      "ds_read_b128 v[128:131], v8 offset:18432\n\t"
      "v_fma_mix_f32 v39, v31, v25, -v23 op_sel:[1,0,0] op_sel_hi:[1,0,0]\n\t"
      "v_mfma_f32_16x16x32_bf16 v[248:251], v[40:43], v[132:135], v[248:251]\n\t"
      "ds_read_b128 v[132:135], v9 offset:18432\n\t"
      "v_cvt_pk_bf16_f32 v70, v36, v37\n\t"
      "v_cvt_pk_bf16_f32 v71, v38, v39\n\t"
      "v_mfma_f32_16x16x32_bf16 v[144:147], v[56:59], v[124:127], v[144:147]\n\t"
      "v_lshlrev_b32 v28, 16, v26\n\t"
      "v_and_b32 v29, 0xffff0000, v26\n\t"
      "v_mfma_f32_16x16x32_bf16 v[144:147], v[64:67], v[120:123], v[144:147]\n\t"
      "v_lshlrev_b32 v30, 16, v27\n\t"
      "v_and_b32 v31, 0xffff0000, v27\n\t"
      "v_mfma_f32_16x16x32_bf16 v[148:151], v[68:71], v[120:123], v[148:151]\n\t"
      "ds_read_b128 v[120:123], v8 offset:20480\n\t"
      "v_mul_f32 v20, 0x4b800000, v28\n\t"
      "v_mfma_f32_16x16x32_bf16 v[148:151], v[60:63], v[124:127], v[148:151]\n\t"
      "ds_read_b128 v[124:127], v9 offset:20480\n\t"
      "v_mul_f32 v24, 0x49800000, v28\n\t"
      "v_mfma_f32_16x16x32_bf16 v[156:159], v[60:63], v[116:119], v[156:159]\n\t"
      "v_mul_f32 v22, v28, v30\n\t"
      "v_mul_f32 v21, 0x4b800000, v29\n\t"
      "v_mfma_f32_16x16x32_bf16 v[156:159], v[68:71], v[112:115], v[156:159]\n\t"
      "v_mul_f32 v25, 0x49800000, v29\n\t"
      "v_mul_f32 v23, v29, v31\n\t"
      "v_mfma_f32_16x16x32_bf16 v[152:155], v[64:67], v[112:115], v[152:155]\n\t"
      "ds_read_b128 v[112:115], v8 offset:22528\n\t"
      "ds_read2st64_b32 v[26:27], v11 offset0:9 offset1:25\n\t"
      "v_mfma_f32_16x16x32_bf16 v[152:155], v[56:59], v[116:119], v[152:155]\n\t"
      "ds_read_b128 v[116:119], v9 offset:22528\n\t"
      "v_lshrrev_b32 v31, 8, v16\n\t"
      "v_mfma_f32_16x16x32_bf16 v[160:163], v[56:59], v[108:111], v[160:163]\n\t"
      "v_and_b32 v28, v13, v16\n\t"
      "v_and_b32 v29, v14, v16\n\t"
      "v_mfma_f32_16x16x32_bf16 v[160:163], v[64:67], v[104:107], v[160:163]\n\t"
      "v_and_b32 v30, v13, v31\n\t"
      "v_and_b32 v31, v14, v31\n\t"
      "v_mfma_f32_16x16x32_bf16 v[164:167], v[68:71], v[104:107], v[164:167]\n\t"
      "ds_read_b128 v[104:107], v8 offset:24576\n\t"
      "v_fma_mix_f32 v32, v28, v20, -v22 op_sel:[0,0,0] op_sel_hi:[1,0,0]\n\t"
      "v_mfma_f32_16x16x32_bf16 v[164:167], v[60:63], v[108:111], v[164:167]\n\t"
      "ds_read_b128 v[108:111], v9 offset:24576\n\t"
      "v_fma_mix_f32 v33, v29, v24, -v22 op_sel:[0,0,0] op_sel_hi:[1,0,0]\n\t"
      "v_mfma_f32_16x16x32_bf16 v[172:175], v[60:63], v[100:103], v[172:175]\n\t"
      "v_fma_mix_f32 v34, v30, v20, -v22 op_sel:[0,0,0] op_sel_hi:[1,0,0]\n\t"
      "v_fma_mix_f32 v35, v31, v24, -v22 op_sel:[0,0,0] op_sel_hi:[1,0,0]\n\t"
      "v_mfma_f32_16x16x32_bf16 v[172:175], v[68:71], v[96:99], v[172:175]\n\t"
      "v_fma_mix_f32 v36, v28, v20, -v22 op_sel:[1,0,0] op_sel_hi:[1,0,0]\n\t"
      "v_fma_mix_f32 v37, v29, v24, -v22 op_sel:[1,0,0] op_sel_hi:[1,0,0]\n\t"
      "v_mfma_f32_16x16x32_bf16 v[168:171], v[64:67], v[96:99], v[168:171]\n\t"
      "ds_read_b128 v[96:99], v8 offset:26624\n\t"
      "v_cvt_pk_bf16_f32 v40, v32, v33\n\t"
      "v_mfma_f32_16x16x32_bf16 v[168:171], v[56:59], v[100:103], v[168:171]\n\t"
      "ds_read_b128 v[100:103], v9 offset:26624\n\t"
      "v_cvt_pk_bf16_f32 v41, v34, v35\n\t"
      "v_mfma_f32_16x16x32_bf16 v[176:179], v[56:59], v[92:95], v[176:179]\n\t"
      "v_fma_mix_f32 v38, v30, v20, -v22 op_sel:[1,0,0] op_sel_hi:[1,0,0]\n\t"
      "s_waitcnt lgkmcnt(3)\n\t"
      "v_mfma_f32_16x16x32_bf16 v[176:179], v[64:67], v[88:91], v[176:179]\n\t"
      "v_fma_mix_f32 v39, v31, v24, -v22 op_sel:[1,0,0] op_sel_hi:[1,0,0]\n\t"
      "v_cvt_pk_bf16_f32 v42, v36, v37\n\t"
      "v_mfma_f32_16x16x32_bf16 v[180:183], v[68:71], v[88:91], v[180:183]\n\t"
      "ds_read_b128 v[88:91], v8 offset:28672\n\t"
      "v_cvt_pk_bf16_f32 v43, v38, v39\n\t"
      "v_mfma_f32_16x16x32_bf16 v[180:183], v[60:63], v[92:95], v[180:183]\n\t"
      "ds_read_b128 v[92:95], v9 offset:28672\n\t"
      "v_lshrrev_b32 v31, 8, v17\n\t"
      "v_mfma_f32_16x16x32_bf16 v[188:191], v[60:63], v[84:87], v[188:191]\n\t"
      "v_and_b32 v28, v13, v17\n\t"
      "v_and_b32 v29, v14, v17\n\t"
      "v_mfma_f32_16x16x32_bf16 v[188:191], v[68:71], v[80:83], v[188:191]\n\t"
      "v_and_b32 v30, v13, v31\n\t"
      "v_and_b32 v31, v14, v31\n\t"
      "v_mfma_f32_16x16x32_bf16 v[184:187], v[64:67], v[80:83], v[184:187]\n\t"
      "ds_read_b128 v[80:83], v8 offset:30720\n\t"
      "ds_read_b64 v[16:17], v10 offset:12288\n\t"
      "v_mfma_f32_16x16x32_bf16 v[184:187], v[56:59], v[84:87], v[184:187]\n\t"
      "ds_read_b128 v[84:87], v9 offset:30720\n\t"
      "v_fma_mix_f32 v32, v28, v21, -v23 op_sel:[0,0,0] op_sel_hi:[1,0,0]\n\t"
      "v_mfma_f32_16x16x32_bf16 v[192:195], v[56:59], v[136:139], v[192:195]\n\t"
      "v_fma_mix_f32 v33, v29, v25, -v23 op_sel:[0,0,0] op_sel_hi:[1,0,0]\n\t"
      "v_fma_mix_f32 v34, v30, v21, -v23 op_sel:[0,0,0] op_sel_hi:[1,0,0]\n\t"
      "v_mfma_f32_16x16x32_bf16 v[192:195], v[64:67], v[140:143], v[192:195]\n\t"
      "s_barrier\n\t"
      "v_mfma_f32_16x16x32_bf16 v[196:199], v[68:71], v[140:143], v[196:199]\n\t"
      "ds_read_b128 v[140:143], v8 offset:32768\n\t"
      "v_fma_mix_f32 v35, v31, v25, -v23 op_sel:[0,0,0] op_sel_hi:[1,0,0]\n\t"
      "v_mfma_f32_16x16x32_bf16 v[196:199], v[60:63], v[136:139], v[196:199]\n\t"
      "ds_read_b128 v[136:139], v9 offset:32768\n\t"
      "v_fma_mix_f32 v36, v28, v21, -v23 op_sel:[1,0,0] op_sel_hi:[1,0,0]\n\t"
      "v_mfma_f32_16x16x32_bf16 v[204:207], v[60:63], v[128:131], v[204:207]\n\t"
      "v_fma_mix_f32 v37, v29, v25, -v23 op_sel:[1,0,0] op_sel_hi:[1,0,0]\n\t"
      "v_cvt_pk_bf16_f32 v44, v32, v33\n\t"
      "v_mfma_f32_16x16x32_bf16 v[204:207], v[68:71], v[132:135], v[204:207]\n\t"
      "v_cvt_pk_bf16_f32 v45, v34, v35\n\t"
      "v_fma_mix_f32 v38, v30, v21, -v23 op_sel:[1,0,0] op_sel_hi:[1,0,0]\n\t"
      "v_mfma_f32_16x16x32_bf16 v[200:203], v[64:67], v[132:135], v[200:203]\n\t"
      "ds_read_b128 v[132:135], v8 offset:34816\n\t"
      "v_fma_mix_f32 v39, v31, v25, -v23 op_sel:[1,0,0] op_sel_hi:[1,0,0]\n\t"
      "v_mfma_f32_16x16x32_bf16 v[200:203], v[56:59], v[128:131], v[200:203]\n\t"
      "ds_read_b128 v[128:131], v9 offset:34816\n\t"
      "v_cvt_pk_bf16_f32 v46, v36, v37\n\t"
      "v_mfma_f32_16x16x32_bf16 v[208:211], v[56:59], v[120:123], v[208:211]\n\t"
      "v_cvt_pk_bf16_f32 v47, v38, v39\n\t"
      "v_lshrrev_b32 v31, 8, v18\n\t"
      "v_mfma_f32_16x16x32_bf16 v[208:211], v[64:67], v[124:127], v[208:211]\n\t"
      "v_and_b32 v28, v13, v18\n\t"
      "v_and_b32 v29, v14, v18\n\t"
      "v_mfma_f32_16x16x32_bf16 v[212:215], v[68:71], v[124:127], v[212:215]\n\t"
      "ds_read_b128 v[124:127], v8 offset:36864\n\t"
      "v_and_b32 v30, v13, v31\n\t"
      "v_mfma_f32_16x16x32_bf16 v[212:215], v[60:63], v[120:123], v[212:215]\n\t"
      "ds_read_b128 v[120:123], v9 offset:36864\n\t"
      "v_and_b32 v31, v14, v31\n\t"
      "v_mfma_f32_16x16x32_bf16 v[220:223], v[60:63], v[112:115], v[220:223]\n\t"
      "v_fma_mix_f32 v32, v28, v20, -v22 op_sel:[0,0,0] op_sel_hi:[1,0,0]\n\t"
      "v_fma_mix_f32 v33, v29, v24, -v22 op_sel:[0,0,0] op_sel_hi:[1,0,0]\n\t"
      "v_mfma_f32_16x16x32_bf16 v[220:223], v[68:71], v[116:119], v[220:223]\n\t"
      "v_fma_mix_f32 v34, v30, v20, -v22 op_sel:[0,0,0] op_sel_hi:[1,0,0]\n\t"
      "v_fma_mix_f32 v35, v31, v24, -v22 op_sel:[0,0,0] op_sel_hi:[1,0,0]\n\t"
      "v_mfma_f32_16x16x32_bf16 v[216:219], v[64:67], v[116:119], v[216:219]\n\t"
      "ds_read_b128 v[116:119], v8 offset:38912\n\t"
      "v_fma_mix_f32 v36, v28, v20, -v22 op_sel:[1,0,0] op_sel_hi:[1,0,0]\n\t"
      "v_mfma_f32_16x16x32_bf16 v[216:219], v[56:59], v[112:115], v[216:219]\n\t"
      "ds_read_b128 v[112:115], v9 offset:38912\n\t"
      "v_fma_mix_f32 v37, v29, v24, -v22 op_sel:[1,0,0] op_sel_hi:[1,0,0]\n\t"
      "v_mfma_f32_16x16x32_bf16 v[224:227], v[56:59], v[104:107], v[224:227]\n\t"
      "v_cvt_pk_bf16_f32 v48, v32, v33\n\t"
      "s_waitcnt lgkmcnt(3)\n\t"
      "v_mfma_f32_16x16x32_bf16 v[224:227], v[64:67], v[108:111], v[224:227]\n\t"
      "v_cvt_pk_bf16_f32 v49, v34, v35\n\t"
      "v_fma_mix_f32 v38, v30, v20, -v22 op_sel:[1,0,0] op_sel_hi:[1,0,0]\n\t"
      "v_mfma_f32_16x16x32_bf16 v[228:231], v[68:71], v[108:111], v[228:231]\n\t"
      "ds_read_b128 v[108:111], v8 offset:40960\n\t"
      "v_fma_mix_f32 v39, v31, v24, -v22 op_sel:[1,0,0] op_sel_hi:[1,0,0]\n\t"
      "v_mfma_f32_16x16x32_bf16 v[228:231], v[60:63], v[104:107], v[228:231]\n\t"
      "ds_read_b128 v[104:107], v9 offset:40960\n\t"
      "v_cvt_pk_bf16_f32 v50, v36, v37\n\t"
      "v_mfma_f32_16x16x32_bf16 v[236:239], v[60:63], v[96:99], v[236:239]\n\t"
      "v_cvt_pk_bf16_f32 v51, v38, v39\n\t"
      "v_lshrrev_b32 v31, 8, v19\n\t"
      "v_mfma_f32_16x16x32_bf16 v[236:239], v[68:71], v[100:103], v[236:239]\n\t"
      "v_and_b32 v28, v13, v19\n\t"
      "v_and_b32 v29, v14, v19\n\t"
      "v_mfma_f32_16x16x32_bf16 v[232:235], v[64:67], v[100:103], v[232:235]\n\t"
      "ds_read_b128 v[100:103], v8 offset:43008\n\t"
      "v_and_b32 v30, v13, v31\n\t"
      "v_mfma_f32_16x16x32_bf16 v[232:235], v[56:59], v[96:99], v[232:235]\n\t"
      "ds_read_b128 v[96:99], v9 offset:43008\n\t"
      "v_and_b32 v31, v14, v31\n\t"
      "v_mfma_f32_16x16x32_bf16 v[240:243], v[56:59], v[88:91], v[240:243]\n\t"
      "ds_read_b64 v[18:19], v10 offset:14336\n\t"
      "v_fma_mix_f32 v32, v28, v21, -v23 op_sel:[0,0,0] op_sel_hi:[1,0,0]\n\t"
      "v_mfma_f32_16x16x32_bf16 v[240:243], v[64:67], v[92:95], v[240:243]\n\t"
      "v_fma_mix_f32 v33, v29, v25, -v23 op_sel:[0,0,0] op_sel_hi:[1,0,0]\n\t"
      "v_fma_mix_f32 v34, v30, v21, -v23 op_sel:[0,0,0] op_sel_hi:[1,0,0]\n\t"
      "v_mfma_f32_16x16x32_bf16 v[244:247], v[68:71], v[92:95], v[244:247]\n\t"
      "ds_read_b128 v[92:95], v8 offset:45056\n\t"
      "v_fma_mix_f32 v35, v31, v25, -v23 op_sel:[0,0,0] op_sel_hi:[1,0,0]\n\t"
      "v_mfma_f32_16x16x32_bf16 v[244:247], v[60:63], v[88:91], v[244:247]\n\t"
      "ds_read_b128 v[88:91], v9 offset:45056\n\t"
      "v_fma_mix_f32 v36, v28, v21, -v23 op_sel:[1,0,0] op_sel_hi:[1,0,0]\n\t"
      "v_mfma_f32_16x16x32_bf16 v[252:255], v[60:63], v[80:83], v[252:255]\n\t"
      "v_fma_mix_f32 v37, v29, v25, -v23 op_sel:[1,0,0] op_sel_hi:[1,0,0]\n\t"
      "v_cvt_pk_bf16_f32 v52, v32, v33\n\t"
      "v_mfma_f32_16x16x32_bf16 v[252:255], v[68:71], v[84:87], v[252:255]\n\t"
      "v_cvt_pk_bf16_f32 v53, v34, v35\n\t"
      "v_fma_mix_f32 v38, v30, v21, -v23 op_sel:[1,0,0] op_sel_hi:[1,0,0]\n\t"
      "v_mfma_f32_16x16x32_bf16 v[248:251], v[64:67], v[84:87], v[248:251]\n\t"
      "ds_read_b128 v[84:87], v8 offset:47104\n\t"
      "v_fma_mix_f32 v39, v31, v25, -v23 op_sel:[1,0,0] op_sel_hi:[1,0,0]\n\t"
      "v_mfma_f32_16x16x32_bf16 v[248:251], v[56:59], v[80:83], v[248:251]\n\t"
      "ds_read_b128 v[80:83], v9 offset:47104\n\t"
      "v_cvt_pk_bf16_f32 v54, v36, v37\n\t"
      "v_cvt_pk_bf16_f32 v55, v38, v39\n\t"
      "v_mfma_f32_16x16x32_bf16 v[144:147], v[40:43], v[140:143], v[144:147]\n\t"
      "v_lshlrev_b32 v28, 16, v26\n\t"
      "v_and_b32 v29, 0xffff0000, v26\n\t"
      "v_mfma_f32_16x16x32_bf16 v[144:147], v[48:51], v[136:139], v[144:147]\n\t"
      "v_lshlrev_b32 v30, 16, v27\n\t"
      "v_and_b32 v31, 0xffff0000, v27\n\t"
      "v_mfma_f32_16x16x32_bf16 v[148:151], v[52:55], v[136:139], v[148:151]\n\t"
      "ds_read_b128 v[136:139], v8 offset:49152\n\t"
      "v_mul_f32 v20, 0x4b800000, v28\n\t"
      "v_mfma_f32_16x16x32_bf16 v[148:151], v[44:47], v[140:143], v[148:151]\n\t"
      "ds_read_b128 v[140:143], v9 offset:49152\n\t"
      "v_mul_f32 v24, 0x49800000, v28\n\t"
      "v_mfma_f32_16x16x32_bf16 v[156:159], v[44:47], v[132:135], v[156:159]\n\t"
      "v_mul_f32 v22, v28, v30\n\t"
      "v_mul_f32 v21, 0x4b800000, v29\n\t"
      "v_mfma_f32_16x16x32_bf16 v[156:159], v[52:55], v[128:131], v[156:159]\n\t"
      "v_mul_f32 v25, 0x49800000, v29\n\t"
      "v_mul_f32 v23, v29, v31\n\t"
      "v_mfma_f32_16x16x32_bf16 v[152:155], v[48:51], v[128:131], v[152:155]\n\t"
      "ds_read_b128 v[128:131], v8 offset:51200\n\t"
      "ds_read2st64_b32 v[26:27], v11 offset0:10 offset1:26\n\t"
      "v_mfma_f32_16x16x32_bf16 v[152:155], v[40:43], v[132:135], v[152:155]\n\t"
      "ds_read_b128 v[132:135], v9 offset:51200\n\t"
      "v_lshrrev_b32 v31, 8, v16\n\t"
      "v_mfma_f32_16x16x32_bf16 v[160:163], v[40:43], v[124:127], v[160:163]\n\t"
      "v_and_b32 v28, v13, v16\n\t"
      "s_waitcnt lgkmcnt(4)\n\t"
      "v_mfma_f32_16x16x32_bf16 v[160:163], v[48:51], v[120:123], v[160:163]\n\t"
      "v_and_b32 v29, v14, v16\n\t"
      "v_and_b32 v30, v13, v31\n\t"
      "v_and_b32 v31, v14, v31\n\t"
      "v_mfma_f32_16x16x32_bf16 v[164:167], v[52:55], v[120:123], v[164:167]\n\t"
      "ds_read_b128 v[120:123], v8 offset:53248\n\t"
      "v_fma_mix_f32 v32, v28, v20, -v22 op_sel:[0,0,0] op_sel_hi:[1,0,0]\n\t"
      "v_mfma_f32_16x16x32_bf16 v[164:167], v[44:47], v[124:127], v[164:167]\n\t"
      "ds_read_b128 v[124:127], v9 offset:53248\n\t"
      "v_fma_mix_f32 v33, v29, v24, -v22 op_sel:[0,0,0] op_sel_hi:[1,0,0]\n\t"
      "v_mfma_f32_16x16x32_bf16 v[172:175], v[44:47], v[116:119], v[172:175]\n\t"
      "v_fma_mix_f32 v34, v30, v20, -v22 op_sel:[0,0,0] op_sel_hi:[1,0,0]\n\t"
      "v_fma_mix_f32 v35, v31, v24, -v22 op_sel:[0,0,0] op_sel_hi:[1,0,0]\n\t"
      "v_mfma_f32_16x16x32_bf16 v[172:175], v[52:55], v[112:115], v[172:175]\n\t"
      "v_fma_mix_f32 v36, v28, v20, -v22 op_sel:[1,0,0] op_sel_hi:[1,0,0]\n\t"
      "v_fma_mix_f32 v37, v29, v24, -v22 op_sel:[1,0,0] op_sel_hi:[1,0,0]\n\t"
      "v_mfma_f32_16x16x32_bf16 v[168:171], v[48:51], v[112:115], v[168:171]\n\t"
      "ds_read_b128 v[112:115], v8 offset:55296\n\t"
      "v_cvt_pk_bf16_f32 v56, v32, v33\n\t"
      "v_mfma_f32_16x16x32_bf16 v[168:171], v[40:43], v[116:119], v[168:171]\n\t"
      "ds_read_b128 v[116:119], v9 offset:55296\n\t"
      "v_cvt_pk_bf16_f32 v57, v34, v35\n\t"
      "v_mfma_f32_16x16x32_bf16 v[176:179], v[40:43], v[108:111], v[176:179]\n\t"
      "v_fma_mix_f32 v38, v30, v20, -v22 op_sel:[1,0,0] op_sel_hi:[1,0,0]\n\t"
      "v_fma_mix_f32 v39, v31, v24, -v22 op_sel:[1,0,0] op_sel_hi:[1,0,0]\n\t"
      "v_mfma_f32_16x16x32_bf16 v[176:179], v[48:51], v[104:107], v[176:179]\n\t"
      "v_cvt_pk_bf16_f32 v58, v36, v37\n\t"
      "v_cvt_pk_bf16_f32 v59, v38, v39\n\t"
      "v_mfma_f32_16x16x32_bf16 v[180:183], v[52:55], v[104:107], v[180:183]\n\t"
      "ds_read_b128 v[104:107], v8 offset:57344\n\t"
      "v_lshrrev_b32 v31, 8, v17\n\t"
      "v_mfma_f32_16x16x32_bf16 v[180:183], v[44:47], v[108:111], v[180:183]\n\t"
      "ds_read_b128 v[108:111], v9 offset:57344\n\t"
      "v_and_b32 v28, v13, v17\n\t"
      "v_mfma_f32_16x16x32_bf16 v[188:191], v[44:47], v[100:103], v[188:191]\n\t"
      "v_and_b32 v29, v14, v17\n\t"
      "v_and_b32 v30, v13, v31\n\t"
      "v_mfma_f32_16x16x32_bf16 v[188:191], v[52:55], v[96:99], v[188:191]\n\t"
      "v_and_b32 v31, v14, v31\n\t"
      "ds_read_b64 v[16:17], v10 offset:16384\n\t"
      "v_mfma_f32_16x16x32_bf16 v[184:187], v[48:51], v[96:99], v[184:187]\n\t"
      "ds_read_b128 v[96:99], v8 offset:59392\n\t"
      "v_fma_mix_f32 v32, v28, v21, -v23 op_sel:[0,0,0] op_sel_hi:[1,0,0]\n\t"
      "v_mfma_f32_16x16x32_bf16 v[184:187], v[40:43], v[100:103], v[184:187]\n\t"
      "ds_read_b128 v[100:103], v9 offset:59392\n\t"
      "v_fma_mix_f32 v33, v29, v25, -v23 op_sel:[0,0,0] op_sel_hi:[1,0,0]\n\t"
      "v_mfma_f32_16x16x32_bf16 v[192:195], v[40:43], v[92:95], v[192:195]\n\t"
      "v_fma_mix_f32 v34, v30, v21, -v23 op_sel:[0,0,0] op_sel_hi:[1,0,0]\n\t"
      "v_fma_mix_f32 v35, v31, v25, -v23 op_sel:[0,0,0] op_sel_hi:[1,0,0]\n\t"
      "v_mfma_f32_16x16x32_bf16 v[192:195], v[48:51], v[88:91], v[192:195]\n\t"
      "s_barrier\n\t"
      "v_mfma_f32_16x16x32_bf16 v[196:199], v[52:55], v[88:91], v[196:199]\n\t"
      "ds_read_b128 v[88:91], v8 offset:61440\n\t"
      "v_fma_mix_f32 v36, v28, v21, -v23 op_sel:[1,0,0] op_sel_hi:[1,0,0]\n\t"
      "v_mfma_f32_16x16x32_bf16 v[196:199], v[44:47], v[92:95], v[196:199]\n\t"
      "ds_read_b128 v[92:95], v9 offset:61440\n\t"
      "v_fma_mix_f32 v37, v29, v25, -v23 op_sel:[1,0,0] op_sel_hi:[1,0,0]\n\t"
      "v_mfma_f32_16x16x32_bf16 v[204:207], v[44:47], v[84:87], v[204:207]\n\t"
      "v_cvt_pk_bf16_f32 v60, v32, v33\n\t"
      "v_cvt_pk_bf16_f32 v61, v34, v35\n\t"
      "v_mfma_f32_16x16x32_bf16 v[204:207], v[52:55], v[80:83], v[204:207]\n\t"
      "v_fma_mix_f32 v38, v30, v21, -v23 op_sel:[1,0,0] op_sel_hi:[1,0,0]\n\t"
      "v_fma_mix_f32 v39, v31, v25, -v23 op_sel:[1,0,0] op_sel_hi:[1,0,0]\n\t"
      "v_mfma_f32_16x16x32_bf16 v[200:203], v[48:51], v[80:83], v[200:203]\n\t"
      "ds_read_b128 v[80:83], v8 offset:63488\n\t"
      "v_cvt_pk_bf16_f32 v62, v36, v37\n\t"
      "v_mfma_f32_16x16x32_bf16 v[200:203], v[40:43], v[84:87], v[200:203]\n\t"
      "ds_read_b128 v[84:87], v9 offset:63488\n\t"
      "v_cvt_pk_bf16_f32 v63, v38, v39\n\t"
      "v_mfma_f32_16x16x32_bf16 v[208:211], v[40:43], v[136:139], v[208:211]\n\t"
      "v_lshrrev_b32 v31, 8, v18\n\t"
      "s_waitcnt lgkmcnt(3)\n\t"
      "v_mfma_f32_16x16x32_bf16 v[208:211], v[48:51], v[140:143], v[208:211]\n\t"
      "v_and_b32 v28, v13, v18\n\t"
      "v_and_b32 v29, v14, v18\n\t"
      "v_mfma_f32_16x16x32_bf16 v[212:215], v[52:55], v[140:143], v[212:215]\n\t"
      "ds_read_b128 v[140:143], v6 offset:0\n\t"
      "v_and_b32 v30, v13, v31\n\t"
      "v_mfma_f32_16x16x32_bf16 v[212:215], v[44:47], v[136:139], v[212:215]\n\t"
      "ds_read_b128 v[136:139], v7 offset:0\n\t"
      "v_and_b32 v31, v14, v31\n\t"
      "v_mfma_f32_16x16x32_bf16 v[220:223], v[44:47], v[128:131], v[220:223]\n\t"
      "v_fma_mix_f32 v32, v28, v20, -v22 op_sel:[0,0,0] op_sel_hi:[1,0,0]\n\t"
      "v_fma_mix_f32 v33, v29, v24, -v22 op_sel:[0,0,0] op_sel_hi:[1,0,0]\n\t"
      "v_mfma_f32_16x16x32_bf16 v[220:223], v[52:55], v[132:135], v[220:223]\n\t"
      "v_fma_mix_f32 v34, v30, v20, -v22 op_sel:[0,0,0] op_sel_hi:[1,0,0]\n\t"
      "v_fma_mix_f32 v35, v31, v24, -v22 op_sel:[0,0,0] op_sel_hi:[1,0,0]\n\t"
      "v_mfma_f32_16x16x32_bf16 v[216:219], v[48:51], v[132:135], v[216:219]\n\t"
      "ds_read_b128 v[132:135], v6 offset:2048\n\t"
      "v_fma_mix_f32 v36, v28, v20, -v22 op_sel:[1,0,0] op_sel_hi:[1,0,0]\n\t"
      "v_mfma_f32_16x16x32_bf16 v[216:219], v[40:43], v[128:131], v[216:219]\n\t"
      "ds_read_b128 v[128:131], v7 offset:2048\n\t"
      "v_fma_mix_f32 v37, v29, v24, -v22 op_sel:[1,0,0] op_sel_hi:[1,0,0]\n\t"
      "v_mfma_f32_16x16x32_bf16 v[224:227], v[40:43], v[120:123], v[224:227]\n\t"
      "v_cvt_pk_bf16_f32 v64, v32, v33\n\t"
      "v_cvt_pk_bf16_f32 v65, v34, v35\n\t"
      "v_mfma_f32_16x16x32_bf16 v[224:227], v[48:51], v[124:127], v[224:227]\n\t"
      "v_fma_mix_f32 v38, v30, v20, -v22 op_sel:[1,0,0] op_sel_hi:[1,0,0]\n\t"
      "v_fma_mix_f32 v39, v31, v24, -v22 op_sel:[1,0,0] op_sel_hi:[1,0,0]\n\t"
      "v_mfma_f32_16x16x32_bf16 v[228:231], v[52:55], v[124:127], v[228:231]\n\t"
      "ds_read_b128 v[124:127], v6 offset:4096\n\t"
      "v_cvt_pk_bf16_f32 v66, v36, v37\n\t"
      "v_mfma_f32_16x16x32_bf16 v[228:231], v[44:47], v[120:123], v[228:231]\n\t"
      "ds_read_b128 v[120:123], v7 offset:4096\n\t"
      "v_cvt_pk_bf16_f32 v67, v38, v39\n\t"
      "v_mfma_f32_16x16x32_bf16 v[236:239], v[44:47], v[112:115], v[236:239]\n\t"
      "v_lshrrev_b32 v31, 8, v19\n\t"
      "v_and_b32 v28, v13, v19\n\t"
      "v_mfma_f32_16x16x32_bf16 v[236:239], v[52:55], v[116:119], v[236:239]\n\t"
      "v_and_b32 v29, v14, v19\n\t"
      "v_and_b32 v30, v13, v31\n\t"
      "v_mfma_f32_16x16x32_bf16 v[232:235], v[48:51], v[116:119], v[232:235]\n\t"
      "ds_read_b128 v[116:119], v6 offset:6144\n\t"
      "v_and_b32 v31, v14, v31\n\t"
      "v_mfma_f32_16x16x32_bf16 v[232:235], v[40:43], v[112:115], v[232:235]\n\t"
      "ds_read_b128 v[112:115], v7 offset:6144\n\t"
      "ds_read_b64 v[18:19], v10 offset:18432\n\t"
      "v_mfma_f32_16x16x32_bf16 v[240:243], v[40:43], v[104:107], v[240:243]\n\t"
      "v_fma_mix_f32 v32, v28, v21, -v23 op_sel:[0,0,0] op_sel_hi:[1,0,0]\n\t"
      "v_fma_mix_f32 v33, v29, v25, -v23 op_sel:[0,0,0] op_sel_hi:[1,0,0]\n\t"
      "v_mfma_f32_16x16x32_bf16 v[240:243], v[48:51], v[108:111], v[240:243]\n\t"
      "v_fma_mix_f32 v34, v30, v21, -v23 op_sel:[0,0,0] op_sel_hi:[1,0,0]\n\t"
      "v_fma_mix_f32 v35, v31, v25, -v23 op_sel:[0,0,0] op_sel_hi:[1,0,0]\n\t"
      "v_mfma_f32_16x16x32_bf16 v[244:247], v[52:55], v[108:111], v[244:247]\n\t"
      "ds_read_b128 v[108:111], v6 offset:8192\n\t"
      "v_fma_mix_f32 v36, v28, v21, -v23 op_sel:[1,0,0] op_sel_hi:[1,0,0]\n\t"
      "v_mfma_f32_16x16x32_bf16 v[244:247], v[44:47], v[104:107], v[244:247]\n\t"
      "ds_read_b128 v[104:107], v7 offset:8192\n\t"
      "v_fma_mix_f32 v37, v29, v25, -v23 op_sel:[1,0,0] op_sel_hi:[1,0,0]\n\t"
      "v_mfma_f32_16x16x32_bf16 v[252:255], v[44:47], v[96:99], v[252:255]\n\t"
      "v_cvt_pk_bf16_f32 v68, v32, v33\n\t"
      "v_cvt_pk_bf16_f32 v69, v34, v35\n\t"
      "v_mfma_f32_16x16x32_bf16 v[252:255], v[52:55], v[100:103], v[252:255]\n\t"
      "v_fma_mix_f32 v38, v30, v21, -v23 op_sel:[1,0,0] op_sel_hi:[1,0,0]\n\t"
      "v_fma_mix_f32 v39, v31, v25, -v23 op_sel:[1,0,0] op_sel_hi:[1,0,0]\n\t"
      "v_mfma_f32_16x16x32_bf16 v[248:251], v[48:51], v[100:103], v[248:251]\n\t"
      "ds_read_b128 v[100:103], v6 offset:10240\n\t"
      "v_cvt_pk_bf16_f32 v70, v36, v37\n\t"
      "v_mfma_f32_16x16x32_bf16 v[248:251], v[40:43], v[96:99], v[248:251]\n\t"
      "ds_read_b128 v[96:99], v7 offset:10240\n\t"
      "v_cvt_pk_bf16_f32 v71, v38, v39\n\t"
      "v_mfma_f32_16x16x32_bf16 v[144:147], v[56:59], v[88:91], v[144:147]\n\t"
      "v_lshlrev_b32 v28, 16, v26\n\t"
      "s_waitcnt lgkmcnt(3)\n\t"
      "v_mfma_f32_16x16x32_bf16 v[144:147], v[64:67], v[92:95], v[144:147]\n\t"
      "v_and_b32 v29, 0xffff0000, v26\n\t"
      "v_lshlrev_b32 v30, 16, v27\n\t"
      "v_and_b32 v31, 0xffff0000, v27\n\t"
      "v_mfma_f32_16x16x32_bf16 v[148:151], v[68:71], v[92:95], v[148:151]\n\t"
      "ds_read_b128 v[92:95], v6 offset:12288\n\t"
      "v_mul_f32 v20, 0x4b800000, v28\n\t"
      "v_mfma_f32_16x16x32_bf16 v[148:151], v[60:63], v[88:91], v[148:151]\n\t"
      "ds_read_b128 v[88:91], v7 offset:12288\n\t"
      "v_mul_f32 v24, 0x49800000, v28\n\t"
      "v_mfma_f32_16x16x32_bf16 v[156:159], v[60:63], v[80:83], v[156:159]\n\t"
      "v_mul_f32 v22, v28, v30\n\t"
      "v_mul_f32 v21, 0x4b800000, v29\n\t"
      "v_mfma_f32_16x16x32_bf16 v[156:159], v[68:71], v[84:87], v[156:159]\n\t"
      "v_mul_f32 v25, 0x49800000, v29\n\t"
      "v_mul_f32 v23, v29, v31\n\t"
      "v_mfma_f32_16x16x32_bf16 v[152:155], v[64:67], v[84:87], v[152:155]\n\t"
      "ds_read_b128 v[84:87], v6 offset:14336\n\t"
      "ds_read2st64_b32 v[26:27], v11 offset0:11 offset1:27\n\t"
      "v_mfma_f32_16x16x32_bf16 v[152:155], v[56:59], v[80:83], v[152:155]\n\t"
      "ds_read_b128 v[80:83], v7 offset:14336\n\t"
      "v_lshrrev_b32 v31, 8, v16\n\t"
      "v_mfma_f32_16x16x32_bf16 v[160:163], v[56:59], v[140:143], v[160:163]\n\t"
      "v_and_b32 v28, v13, v16\n\t"
      "v_and_b32 v29, v14, v16\n\t"
      "v_mfma_f32_16x16x32_bf16 v[160:163], v[64:67], v[136:139], v[160:163]\n\t"
      "v_and_b32 v30, v13, v31\n\t"
      "v_and_b32 v31, v14, v31\n\t"
      "v_mfma_f32_16x16x32_bf16 v[164:167], v[68:71], v[136:139], v[164:167]\n\t"
      "ds_read_b128 v[136:139], v6 offset:16384\n\t"
      "v_fma_mix_f32 v32, v28, v20, -v22 op_sel:[0,0,0] op_sel_hi:[1,0,0]\n\t"
      "v_mfma_f32_16x16x32_bf16 v[164:167], v[60:63], v[140:143], v[164:167]\n\t"
      "ds_read_b128 v[140:143], v7 offset:16384\n\t"
      "v_fma_mix_f32 v33, v29, v24, -v22 op_sel:[0,0,0] op_sel_hi:[1,0,0]\n\t"
      "v_mfma_f32_16x16x32_bf16 v[172:175], v[60:63], v[132:135], v[172:175]\n\t"
      "v_fma_mix_f32 v34, v30, v20, -v22 op_sel:[0,0,0] op_sel_hi:[1,0,0]\n\t"
      "v_fma_mix_f32 v35, v31, v24, -v22 op_sel:[0,0,0] op_sel_hi:[1,0,0]\n\t"
      "v_mfma_f32_16x16x32_bf16 v[172:175], v[68:71], v[128:131], v[172:175]\n\t"
      "v_fma_mix_f32 v36, v28, v20, -v22 op_sel:[1,0,0] op_sel_hi:[1,0,0]\n\t"
      "v_fma_mix_f32 v37, v29, v24, -v22 op_sel:[1,0,0] op_sel_hi:[1,0,0]\n\t"
      "v_mfma_f32_16x16x32_bf16 v[168:171], v[64:67], v[128:131], v[168:171]\n\t"
      "ds_read_b128 v[128:131], v6 offset:18432\n\t"
      "v_cvt_pk_bf16_f32 v40, v32, v33\n\t"
      "v_mfma_f32_16x16x32_bf16 v[168:171], v[56:59], v[132:135], v[168:171]\n\t"
      "ds_read_b128 v[132:135], v7 offset:18432\n\t"
      "v_cvt_pk_bf16_f32 v41, v34, v35\n\t"
      "v_mfma_f32_16x16x32_bf16 v[176:179], v[56:59], v[124:127], v[176:179]\n\t"
      "v_fma_mix_f32 v38, v30, v20, -v22 op_sel:[1,0,0] op_sel_hi:[1,0,0]\n\t"
      "v_fma_mix_f32 v39, v31, v24, -v22 op_sel:[1,0,0] op_sel_hi:[1,0,0]\n\t"
      "v_mfma_f32_16x16x32_bf16 v[176:179], v[64:67], v[120:123], v[176:179]\n\t"
      "v_cvt_pk_bf16_f32 v42, v36, v37\n\t"
      "v_cvt_pk_bf16_f32 v43, v38, v39\n\t"
      "v_mfma_f32_16x16x32_bf16 v[180:183], v[68:71], v[120:123], v[180:183]\n\t"
      "ds_read_b128 v[120:123], v6 offset:20480\n\t"
      "v_lshrrev_b32 v31, 8, v17\n\t"
      "v_mfma_f32_16x16x32_bf16 v[180:183], v[60:63], v[124:127], v[180:183]\n\t"
      "ds_read_b128 v[124:127], v7 offset:20480\n\t"
      "v_and_b32 v28, v13, v17\n\t"
      "v_mfma_f32_16x16x32_bf16 v[188:191], v[60:63], v[116:119], v[188:191]\n\t"
      "v_and_b32 v29, v14, v17\n\t"
      "v_and_b32 v30, v13, v31\n\t"
      "v_mfma_f32_16x16x32_bf16 v[188:191], v[68:71], v[112:115], v[188:191]\n\t"
      "v_and_b32 v31, v14, v31\n\t"
      "ds_read_b64 v[16:17], v10 offset:20480\n\t"
      "v_mfma_f32_16x16x32_bf16 v[184:187], v[64:67], v[112:115], v[184:187]\n\t"
      "ds_read_b128 v[112:115], v6 offset:22528\n\t"
      "v_fma_mix_f32 v32, v28, v21, -v23 op_sel:[0,0,0] op_sel_hi:[1,0,0]\n\t"
      "v_mfma_f32_16x16x32_bf16 v[184:187], v[56:59], v[116:119], v[184:187]\n\t"
      "ds_read_b128 v[116:119], v7 offset:22528\n\t"
      "v_fma_mix_f32 v33, v29, v25, -v23 op_sel:[0,0,0] op_sel_hi:[1,0,0]\n\t"
      "v_mfma_f32_16x16x32_bf16 v[192:195], v[56:59], v[108:111], v[192:195]\n\t"
      "v_fma_mix_f32 v34, v30, v21, -v23 op_sel:[0,0,0] op_sel_hi:[1,0,0]\n\t"
      "s_waitcnt lgkmcnt(4)\n\t"
      "v_mfma_f32_16x16x32_bf16 v[192:195], v[64:67], v[104:107], v[192:195]\n\t"
      "s_barrier\n\t"
      "v_mfma_f32_16x16x32_bf16 v[196:199], v[68:71], v[104:107], v[196:199]\n\t"
      "ds_read_b128 v[104:107], v6 offset:24576\n\t"
      "v_fma_mix_f32 v35, v31, v25, -v23 op_sel:[0,0,0] op_sel_hi:[1,0,0]\n\t"
      "v_mfma_f32_16x16x32_bf16 v[196:199], v[60:63], v[108:111], v[196:199]\n\t"
      "ds_read_b128 v[108:111], v7 offset:24576\n\t"
      "v_fma_mix_f32 v36, v28, v21, -v23 op_sel:[1,0,0] op_sel_hi:[1,0,0]\n\t"
      "v_mfma_f32_16x16x32_bf16 v[204:207], v[60:63], v[100:103], v[204:207]\n\t"
      "v_fma_mix_f32 v37, v29, v25, -v23 op_sel:[1,0,0] op_sel_hi:[1,0,0]\n\t"
      "v_cvt_pk_bf16_f32 v44, v32, v33\n\t"
      "v_mfma_f32_16x16x32_bf16 v[204:207], v[68:71], v[96:99], v[204:207]\n\t"
      "v_cvt_pk_bf16_f32 v45, v34, v35\n\t"
      "v_fma_mix_f32 v38, v30, v21, -v23 op_sel:[1,0,0] op_sel_hi:[1,0,0]\n\t"
      "v_mfma_f32_16x16x32_bf16 v[200:203], v[64:67], v[96:99], v[200:203]\n\t"
      "ds_read_b128 v[96:99], v6 offset:26624\n\t"
      "v_fma_mix_f32 v39, v31, v25, -v23 op_sel:[1,0,0] op_sel_hi:[1,0,0]\n\t"
      "v_mfma_f32_16x16x32_bf16 v[200:203], v[56:59], v[100:103], v[200:203]\n\t"
      "ds_read_b128 v[100:103], v7 offset:26624\n\t"
      "v_cvt_pk_bf16_f32 v46, v36, v37\n\t"
      "v_mfma_f32_16x16x32_bf16 v[208:211], v[56:59], v[92:95], v[208:211]\n\t"
      "v_cvt_pk_bf16_f32 v47, v38, v39\n\t"
      "v_lshrrev_b32 v31, 8, v18\n\t"
      "v_mfma_f32_16x16x32_bf16 v[208:211], v[64:67], v[88:91], v[208:211]\n\t"
      "v_and_b32 v28, v13, v18\n\t"
      "v_and_b32 v29, v14, v18\n\t"
      "v_mfma_f32_16x16x32_bf16 v[212:215], v[68:71], v[88:91], v[212:215]\n\t"
      "ds_read_b128 v[88:91], v6 offset:28672\n\t"
      "v_and_b32 v30, v13, v31\n\t"
      "v_mfma_f32_16x16x32_bf16 v[212:215], v[60:63], v[92:95], v[212:215]\n\t"
      "ds_read_b128 v[92:95], v7 offset:28672\n\t"
      "v_and_b32 v31, v14, v31\n\t"
      "v_mfma_f32_16x16x32_bf16 v[220:223], v[60:63], v[84:87], v[220:223]\n\t"
      "v_fma_mix_f32 v32, v28, v20, -v22 op_sel:[0,0,0] op_sel_hi:[1,0,0]\n\t"
      "v_fma_mix_f32 v33, v29, v24, -v22 op_sel:[0,0,0] op_sel_hi:[1,0,0]\n\t"
      "v_mfma_f32_16x16x32_bf16 v[220:223], v[68:71], v[80:83], v[220:223]\n\t"
      "v_fma_mix_f32 v34, v30, v20, -v22 op_sel:[0,0,0] op_sel_hi:[1,0,0]\n\t"
      "v_fma_mix_f32 v35, v31, v24, -v22 op_sel:[0,0,0] op_sel_hi:[1,0,0]\n\t"
      "v_mfma_f32_16x16x32_bf16 v[216:219], v[64:67], v[80:83], v[216:219]\n\t"
      "ds_read_b128 v[80:83], v6 offset:30720\n\t"
      "v_fma_mix_f32 v36, v28, v20, -v22 op_sel:[1,0,0] op_sel_hi:[1,0,0]\n\t"
      "v_mfma_f32_16x16x32_bf16 v[216:219], v[56:59], v[84:87], v[216:219]\n\t"
      "ds_read_b128 v[84:87], v7 offset:30720\n\t"
      "v_fma_mix_f32 v37, v29, v24, -v22 op_sel:[1,0,0] op_sel_hi:[1,0,0]\n\t"
      "v_mfma_f32_16x16x32_bf16 v[224:227], v[56:59], v[136:139], v[224:227]\n\t"
      "v_cvt_pk_bf16_f32 v48, v32, v33\n\t"
      "v_cvt_pk_bf16_f32 v49, v34, v35\n\t"
      "v_mfma_f32_16x16x32_bf16 v[224:227], v[64:67], v[140:143], v[224:227]\n\t"
      "v_fma_mix_f32 v38, v30, v20, -v22 op_sel:[1,0,0] op_sel_hi:[1,0,0]\n\t"
      "v_fma_mix_f32 v39, v31, v24, -v22 op_sel:[1,0,0] op_sel_hi:[1,0,0]\n\t"
      "v_mfma_f32_16x16x32_bf16 v[228:231], v[68:71], v[140:143], v[228:231]\n\t"
      "ds_read_b128 v[140:143], v6 offset:32768\n\t"
      "v_cvt_pk_bf16_f32 v50, v36, v37\n\t"
      "v_mfma_f32_16x16x32_bf16 v[228:231], v[60:63], v[136:139], v[228:231]\n\t"
      "LH14x3_%=:\n\t"
      "s_cbranch_execz LHRET_%=\n\t"
      "ds_read_b128 v[136:139], v7 offset:32768\n\t"
      "v_cvt_pk_bf16_f32 v51, v38, v39\n\t"
      "v_mfma_f32_16x16x32_bf16 v[236:239], v[60:63], v[128:131], v[236:239]\n\t"
      "v_lshrrev_b32 v31, 8, v19\n\t"
      "v_and_b32 v28, v13, v19\n\t"
      "v_mfma_f32_16x16x32_bf16 v[236:239], v[68:71], v[132:135], v[236:239]\n\t"
      "v_and_b32 v29, v14, v19\n\t"
      "v_and_b32 v30, v13, v31\n\t"
      "v_mfma_f32_16x16x32_bf16 v[232:235], v[64:67], v[132:135], v[232:235]\n\t"
      "ds_read_b128 v[132:135], v6 offset:34816\n\t"
      "v_and_b32 v31, v14, v31\n\t"
      "v_mfma_f32_16x16x32_bf16 v[232:235], v[56:59], v[128:131], v[232:235]\n\t"
      "ds_read_b128 v[128:131], v7 offset:34816\n\t"
      "ds_read_b64 v[18:19], v10 offset:22528\n\t"
      "v_mfma_f32_16x16x32_bf16 v[240:243], v[56:59], v[120:123], v[240:243]\n\t"
      "v_fma_mix_f32 v32, v28, v21, -v23 op_sel:[0,0,0] op_sel_hi:[1,0,0]\n\t"
      "s_waitcnt lgkmcnt(4)\n\t"
      "v_mfma_f32_16x16x32_bf16 v[240:243], v[64:67], v[124:127], v[240:243]\n\t"
      "v_fma_mix_f32 v33, v29, v25, -v23 op_sel:[0,0,0] op_sel_hi:[1,0,0]\n\t"
      "v_fma_mix_f32 v34, v30, v21, -v23 op_sel:[0,0,0] op_sel_hi:[1,0,0]\n\t"
      "v_mfma_f32_16x16x32_bf16 v[244:247], v[68:71], v[124:127], v[244:247]\n\t"
      "ds_read_b128 v[124:127], v6 offset:36864\n\t"
      "v_fma_mix_f32 v35, v31, v25, -v23 op_sel:[0,0,0] op_sel_hi:[1,0,0]\n\t"
      "v_mfma_f32_16x16x32_bf16 v[244:247], v[60:63], v[120:123], v[244:247]\n\t"
      "ds_read_b128 v[120:123], v7 offset:36864\n\t"
      "v_fma_mix_f32 v36, v28, v21, -v23 op_sel:[1,0,0] op_sel_hi:[1,0,0]\n\t"
      "v_mfma_f32_16x16x32_bf16 v[252:255], v[60:63], v[112:115], v[252:255]\n\t"
      "v_fma_mix_f32 v37, v29, v25, -v23 op_sel:[1,0,0] op_sel_hi:[1,0,0]\n\t"
      "v_cvt_pk_bf16_f32 v52, v32, v33\n\t"
      "v_mfma_f32_16x16x32_bf16 v[252:255], v[68:71], v[116:119], v[252:255]\n\t"
      "v_cvt_pk_bf16_f32 v53, v34, v35\n\t"
      "v_fma_mix_f32 v38, v30, v21, -v23 op_sel:[1,0,0] op_sel_hi:[1,0,0]\n\t"
      "v_mfma_f32_16x16x32_bf16 v[248:251], v[64:67], v[116:119], v[248:251]\n\t"
      "ds_read_b128 v[116:119], v6 offset:38912\n\t"
      "v_fma_mix_f32 v39, v31, v25, -v23 op_sel:[1,0,0] op_sel_hi:[1,0,0]\n\t"
      "v_mfma_f32_16x16x32_bf16 v[248:251], v[56:59], v[112:115], v[248:251]\n\t"
      "ds_read_b128 v[112:115], v7 offset:38912\n\t"
      "v_cvt_pk_bf16_f32 v54, v36, v37\n\t"
      "v_cvt_pk_bf16_f32 v55, v38, v39\n\t"
      "v_mfma_f32_16x16x32_bf16 v[144:147], v[40:43], v[104:107], v[144:147]\n\t"
      "v_lshlrev_b32 v28, 16, v26\n\t"
      "v_and_b32 v29, 0xffff0000, v26\n\t"
      "v_mfma_f32_16x16x32_bf16 v[144:147], v[48:51], v[108:111], v[144:147]\n\t"
      "v_lshlrev_b32 v30, 16, v27\n\t"
      "v_and_b32 v31, 0xffff0000, v27\n\t"
      "v_mfma_f32_16x16x32_bf16 v[148:151], v[52:55], v[108:111], v[148:151]\n\t"
      "ds_read_b128 v[108:111], v6 offset:40960\n\t"
      "v_mul_f32 v20, 0x4b800000, v28\n\t"
      "v_mfma_f32_16x16x32_bf16 v[148:151], v[44:47], v[104:107], v[148:151]\n\t"
      "ds_read_b128 v[104:107], v7 offset:40960\n\t"
      "v_mul_f32 v24, 0x49800000, v28\n\t"
      "v_mfma_f32_16x16x32_bf16 v[156:159], v[44:47], v[96:99], v[156:159]\n\t"
      "v_mul_f32 v22, v28, v30\n\t"
      "v_mul_f32 v21, 0x4b800000, v29\n\t"
      "v_mfma_f32_16x16x32_bf16 v[156:159], v[52:55], v[100:103], v[156:159]\n\t"
      "v_mul_f32 v25, 0x49800000, v29\n\t"
      "v_mul_f32 v23, v29, v31\n\t"
      "v_mfma_f32_16x16x32_bf16 v[152:155], v[48:51], v[100:103], v[152:155]\n\t"
      "ds_read_b128 v[100:103], v6 offset:43008\n\t"
      "ds_read2st64_b32 v[26:27], v11 offset0:12 offset1:28\n\t"
      "v_mfma_f32_16x16x32_bf16 v[152:155], v[40:43], v[96:99], v[152:155]\n\t"
      "ds_read_b128 v[96:99], v7 offset:43008\n\t"
      "v_lshrrev_b32 v31, 8, v16\n\t"
      "v_mfma_f32_16x16x32_bf16 v[160:163], v[40:43], v[88:91], v[160:163]\n\t"
      "v_and_b32 v28, v13, v16\n\t"
      "v_and_b32 v29, v14, v16\n\t"
      "v_mfma_f32_16x16x32_bf16 v[160:163], v[48:51], v[92:95], v[160:163]\n\t"
      "v_and_b32 v30, v13, v31\n\t"
      "v_and_b32 v31, v14, v31\n\t"
      "v_mfma_f32_16x16x32_bf16 v[164:167], v[52:55], v[92:95], v[164:167]\n\t"
      "ds_read_b128 v[92:95], v6 offset:45056\n\t"
      "v_fma_mix_f32 v32, v28, v20, -v22 op_sel:[0,0,0] op_sel_hi:[1,0,0]\n\t"
      "v_mfma_f32_16x16x32_bf16 v[164:167], v[44:47], v[88:91], v[164:167]\n\t"
      "ds_read_b128 v[88:91], v7 offset:45056\n\t"
      "v_fma_mix_f32 v33, v29, v24, -v22 op_sel:[0,0,0] op_sel_hi:[1,0,0]\n\t"
      "v_mfma_f32_16x16x32_bf16 v[172:175], v[44:47], v[80:83], v[172:175]\n\t"
      "v_fma_mix_f32 v34, v30, v20, -v22 op_sel:[0,0,0] op_sel_hi:[1,0,0]\n\t"
      "v_fma_mix_f32 v35, v31, v24, -v22 op_sel:[0,0,0] op_sel_hi:[1,0,0]\n\t"
      "v_mfma_f32_16x16x32_bf16 v[172:175], v[52:55], v[84:87], v[172:175]\n\t"
      "v_fma_mix_f32 v36, v28, v20, -v22 op_sel:[1,0,0] op_sel_hi:[1,0,0]\n\t"
      "v_fma_mix_f32 v37, v29, v24, -v22 op_sel:[1,0,0] op_sel_hi:[1,0,0]\n\t"
      "v_mfma_f32_16x16x32_bf16 v[168:171], v[48:51], v[84:87], v[168:171]\n\t"
      "ds_read_b128 v[84:87], v6 offset:47104\n\t"
      "v_cvt_pk_bf16_f32 v56, v32, v33\n\t"
      "v_mfma_f32_16x16x32_bf16 v[168:171], v[40:43], v[80:83], v[168:171]\n\t"
      "ds_read_b128 v[80:83], v7 offset:47104\n\t"
      "v_cvt_pk_bf16_f32 v57, v34, v35\n\t"
      "v_mfma_f32_16x16x32_bf16 v[176:179], v[40:43], v[140:143], v[176:179]\n\t"
      "v_fma_mix_f32 v38, v30, v20, -v22 op_sel:[1,0,0] op_sel_hi:[1,0,0]\n\t"
      "s_waitcnt lgkmcnt(3)\n\t"
      "v_mfma_f32_16x16x32_bf16 v[176:179], v[48:51], v[136:139], v[176:179]\n\t"
      "v_fma_mix_f32 v39, v31, v24, -v22 op_sel:[1,0,0] op_sel_hi:[1,0,0]\n\t"
      "v_cvt_pk_bf16_f32 v58, v36, v37\n\t"
      "v_mfma_f32_16x16x32_bf16 v[180:183], v[52:55], v[136:139], v[180:183]\n\t"
      "ds_read_b128 v[136:139], v6 offset:49152\n\t"
      "v_cvt_pk_bf16_f32 v59, v38, v39\n\t"
      "v_mfma_f32_16x16x32_bf16 v[180:183], v[44:47], v[140:143], v[180:183]\n\t"
      "ds_read_b128 v[140:143], v7 offset:49152\n\t"
      "v_lshrrev_b32 v31, 8, v17\n\t"
      "v_mfma_f32_16x16x32_bf16 v[188:191], v[44:47], v[132:135], v[188:191]\n\t"
      "v_and_b32 v28, v13, v17\n\t"
      "v_and_b32 v29, v14, v17\n\t"
      "v_mfma_f32_16x16x32_bf16 v[188:191], v[52:55], v[128:131], v[188:191]\n\t"
      "v_and_b32 v30, v13, v31\n\t"
      "v_and_b32 v31, v14, v31\n\t"
      "v_mfma_f32_16x16x32_bf16 v[184:187], v[48:51], v[128:131], v[184:187]\n\t"
      "ds_read_b128 v[128:131], v6 offset:51200\n\t"
      "ds_read_b64 v[16:17], v10 offset:0\n\t"
      "v_mfma_f32_16x16x32_bf16 v[184:187], v[40:43], v[132:135], v[184:187]\n\t"
      "ds_read_b128 v[132:135], v7 offset:51200\n\t"
      "v_fma_mix_f32 v32, v28, v21, -v23 op_sel:[0,0,0] op_sel_hi:[1,0,0]\n\t"
      "v_mfma_f32_16x16x32_bf16 v[192:195], v[40:43], v[124:127], v[192:195]\n\t"
      "v_fma_mix_f32 v33, v29, v25, -v23 op_sel:[0,0,0] op_sel_hi:[1,0,0]\n\t"
      "v_fma_mix_f32 v34, v30, v21, -v23 op_sel:[0,0,0] op_sel_hi:[1,0,0]\n\t"
      "v_mfma_f32_16x16x32_bf16 v[192:195], v[48:51], v[120:123], v[192:195]\n\t"
      "s_barrier\n\t"
      "v_mfma_f32_16x16x32_bf16 v[196:199], v[52:55], v[120:123], v[196:199]\n\t"
      "ds_read_b128 v[120:123], v6 offset:53248\n\t"
      "v_fma_mix_f32 v35, v31, v25, -v23 op_sel:[0,0,0] op_sel_hi:[1,0,0]\n\t"
      "v_mfma_f32_16x16x32_bf16 v[196:199], v[44:47], v[124:127], v[196:199]\n\t"
      "ds_read_b128 v[124:127], v7 offset:53248\n\t"
      "v_fma_mix_f32 v36, v28, v21, -v23 op_sel:[1,0,0] op_sel_hi:[1,0,0]\n\t"
      "v_mfma_f32_16x16x32_bf16 v[204:207], v[44:47], v[116:119], v[204:207]\n\t"
      "v_fma_mix_f32 v37, v29, v25, -v23 op_sel:[1,0,0] op_sel_hi:[1,0,0]\n\t"
      "v_cvt_pk_bf16_f32 v60, v32, v33\n\t"
      "v_mfma_f32_16x16x32_bf16 v[204:207], v[52:55], v[112:115], v[204:207]\n\t"
      "v_cvt_pk_bf16_f32 v61, v34, v35\n\t"
      "v_fma_mix_f32 v38, v30, v21, -v23 op_sel:[1,0,0] op_sel_hi:[1,0,0]\n\t"
      "v_mfma_f32_16x16x32_bf16 v[200:203], v[48:51], v[112:115], v[200:203]\n\t"
      "ds_read_b128 v[112:115], v6 offset:55296\n\t"
      "v_fma_mix_f32 v39, v31, v25, -v23 op_sel:[1,0,0] op_sel_hi:[1,0,0]\n\t"
      "v_mfma_f32_16x16x32_bf16 v[200:203], v[40:43], v[116:119], v[200:203]\n\t"
      "ds_read_b128 v[116:119], v7 offset:55296\n\t"
      "v_cvt_pk_bf16_f32 v62, v36, v37\n\t"
      "v_mfma_f32_16x16x32_bf16 v[208:211], v[40:43], v[108:111], v[208:211]\n\t"
      "v_cvt_pk_bf16_f32 v63, v38, v39\n\t"
      "v_lshrrev_b32 v31, 8, v18\n\t"
      "v_mfma_f32_16x16x32_bf16 v[208:211], v[48:51], v[104:107], v[208:211]\n\t"
      "v_and_b32 v28, v13, v18\n\t"
      "v_and_b32 v29, v14, v18\n\t"
      "v_mfma_f32_16x16x32_bf16 v[212:215], v[52:55], v[104:107], v[212:215]\n\t"
      "ds_read_b128 v[104:107], v6 offset:57344\n\t"
      "v_and_b32 v30, v13, v31\n\t"
      "v_mfma_f32_16x16x32_bf16 v[212:215], v[44:47], v[108:111], v[212:215]\n\t"
      "ds_read_b128 v[108:111], v7 offset:57344\n\t"
      "v_and_b32 v31, v14, v31\n\t"
      "v_mfma_f32_16x16x32_bf16 v[220:223], v[44:47], v[100:103], v[220:223]\n\t"
      "v_fma_mix_f32 v32, v28, v20, -v22 op_sel:[0,0,0] op_sel_hi:[1,0,0]\n\t"
      "v_fma_mix_f32 v33, v29, v24, -v22 op_sel:[0,0,0] op_sel_hi:[1,0,0]\n\t"
      "v_mfma_f32_16x16x32_bf16 v[220:223], v[52:55], v[96:99], v[220:223]\n\t"
      "v_fma_mix_f32 v34, v30, v20, -v22 op_sel:[0,0,0] op_sel_hi:[1,0,0]\n\t"
      "v_fma_mix_f32 v35, v31, v24, -v22 op_sel:[0,0,0] op_sel_hi:[1,0,0]\n\t"
      "v_mfma_f32_16x16x32_bf16 v[216:219], v[48:51], v[96:99], v[216:219]\n\t"
      "ds_read_b128 v[96:99], v6 offset:59392\n\t"
      "v_fma_mix_f32 v36, v28, v20, -v22 op_sel:[1,0,0] op_sel_hi:[1,0,0]\n\t"
      "v_mfma_f32_16x16x32_bf16 v[216:219], v[40:43], v[100:103], v[216:219]\n\t"
      "ds_read_b128 v[100:103], v7 offset:59392\n\t"
      "v_fma_mix_f32 v37, v29, v24, -v22 op_sel:[1,0,0] op_sel_hi:[1,0,0]\n\t"
      "v_mfma_f32_16x16x32_bf16 v[224:227], v[40:43], v[92:95], v[224:227]\n\t"
      "v_cvt_pk_bf16_f32 v64, v32, v33\n\t"
      "s_waitcnt lgkmcnt(3)\n\t"
      "v_mfma_f32_16x16x32_bf16 v[224:227], v[48:51], v[88:91], v[224:227]\n\t"
      "v_cvt_pk_bf16_f32 v65, v34, v35\n\t"
      "v_fma_mix_f32 v38, v30, v20, -v22 op_sel:[1,0,0] op_sel_hi:[1,0,0]\n\t"
      "v_mfma_f32_16x16x32_bf16 v[228:231], v[52:55], v[88:91], v[228:231]\n\t"
      "ds_read_b128 v[88:91], v6 offset:61440\n\t"
      "v_fma_mix_f32 v39, v31, v24, -v22 op_sel:[1,0,0] op_sel_hi:[1,0,0]\n\t"
      "v_mfma_f32_16x16x32_bf16 v[228:231], v[44:47], v[92:95], v[228:231]\n\t"
      "ds_read_b128 v[92:95], v7 offset:61440\n\t"
      "v_cvt_pk_bf16_f32 v66, v36, v37\n\t"
      "v_mfma_f32_16x16x32_bf16 v[236:239], v[44:47], v[84:87], v[236:239]\n\t"
      "v_cvt_pk_bf16_f32 v67, v38, v39\n\t"
      "v_lshrrev_b32 v31, 8, v19\n\t"
      "v_mfma_f32_16x16x32_bf16 v[236:239], v[52:55], v[80:83], v[236:239]\n\t"
      "v_and_b32 v28, v13, v19\n\t"
      "v_and_b32 v29, v14, v19\n\t"
      "v_mfma_f32_16x16x32_bf16 v[232:235], v[48:51], v[80:83], v[232:235]\n\t"
      "ds_read_b128 v[80:83], v6 offset:63488\n\t"
      "v_and_b32 v30, v13, v31\n\t"
      "v_mfma_f32_16x16x32_bf16 v[232:235], v[40:43], v[84:87], v[232:235]\n\t"
      "ds_read_b128 v[84:87], v7 offset:63488\n\t"
      "v_and_b32 v31, v14, v31\n\t"
      "v_mfma_f32_16x16x32_bf16 v[240:243], v[40:43], v[136:139], v[240:243]\n\t"
      "ds_read_b64 v[18:19], v10 offset:2048\n\t"
      "v_fma_mix_f32 v32, v28, v21, -v23 op_sel:[0,0,0] op_sel_hi:[1,0,0]\n\t"
      "v_mfma_f32_16x16x32_bf16 v[240:243], v[48:51], v[140:143], v[240:243]\n\t"
      "v_fma_mix_f32 v33, v29, v25, -v23 op_sel:[0,0,0] op_sel_hi:[1,0,0]\n\t"
      "v_fma_mix_f32 v34, v30, v21, -v23 op_sel:[0,0,0] op_sel_hi:[1,0,0]\n\t"
      "v_mfma_f32_16x16x32_bf16 v[244:247], v[52:55], v[140:143], v[244:247]\n\t"
      "ds_read_b128 v[140:143], v8 offset:0\n\t"
      "v_fma_mix_f32 v35, v31, v25, -v23 op_sel:[0,0,0] op_sel_hi:[1,0,0]\n\t"
      "v_mfma_f32_16x16x32_bf16 v[244:247], v[44:47], v[136:139], v[244:247]\n\t"
      "ds_read_b128 v[136:139], v9 offset:0\n\t"
      "v_fma_mix_f32 v36, v28, v21, -v23 op_sel:[1,0,0] op_sel_hi:[1,0,0]\n\t"
      "v_mfma_f32_16x16x32_bf16 v[252:255], v[44:47], v[128:131], v[252:255]\n\t"
      "v_fma_mix_f32 v37, v29, v25, -v23 op_sel:[1,0,0] op_sel_hi:[1,0,0]\n\t"
      "v_cvt_pk_bf16_f32 v68, v32, v33\n\t"
      "v_mfma_f32_16x16x32_bf16 v[252:255], v[52:55], v[132:135], v[252:255]\n\t"
      "v_cvt_pk_bf16_f32 v69, v34, v35\n\t"
      "v_fma_mix_f32 v38, v30, v21, -v23 op_sel:[1,0,0] op_sel_hi:[1,0,0]\n\t"
      "v_mfma_f32_16x16x32_bf16 v[248:251], v[48:51], v[132:135], v[248:251]\n\t"
      "ds_read_b128 v[132:135], v8 offset:2048\n\t"
      "v_fma_mix_f32 v39, v31, v25, -v23 op_sel:[1,0,0] op_sel_hi:[1,0,0]\n\t"
      "v_mfma_f32_16x16x32_bf16 v[248:251], v[40:43], v[128:131], v[248:251]\n\t"
      "ds_read_b128 v[128:131], v9 offset:2048\n\t"
      "v_cvt_pk_bf16_f32 v70, v36, v37\n\t"
      "v_cvt_pk_bf16_f32 v71, v38, v39\n\t"
      "v_mfma_f32_16x16x32_bf16 v[144:147], v[56:59], v[120:123], v[144:147]\n\t"
      "v_lshlrev_b32 v28, 16, v26\n\t"
      "v_and_b32 v29, 0xffff0000, v26\n\t"
      "v_mfma_f32_16x16x32_bf16 v[144:147], v[64:67], v[124:127], v[144:147]\n\t"
      "v_lshlrev_b32 v30, 16, v27\n\t"
      "v_and_b32 v31, 0xffff0000, v27\n\t"
      "v_mfma_f32_16x16x32_bf16 v[148:151], v[68:71], v[124:127], v[148:151]\n\t"
      "ds_read_b128 v[124:127], v8 offset:4096\n\t"
      "v_mul_f32 v20, 0x4b800000, v28\n\t"
      "v_mfma_f32_16x16x32_bf16 v[148:151], v[60:63], v[120:123], v[148:151]\n\t"
      "ds_read_b128 v[120:123], v9 offset:4096\n\t"
      "v_mul_f32 v24, 0x49800000, v28\n\t"
      "v_mfma_f32_16x16x32_bf16 v[156:159], v[60:63], v[112:115], v[156:159]\n\t"
      "v_mul_f32 v22, v28, v30\n\t"
      "v_mul_f32 v21, 0x4b800000, v29\n\t"
      "v_mfma_f32_16x16x32_bf16 v[156:159], v[68:71], v[116:119], v[156:159]\n\t"
      "v_mul_f32 v25, 0x49800000, v29\n\t"
      "v_mul_f32 v23, v29, v31\n\t"
      "v_mfma_f32_16x16x32_bf16 v[152:155], v[64:67], v[116:119], v[152:155]\n\t"
      "ds_read_b128 v[116:119], v8 offset:6144\n\t"
      "ds_read2st64_b32 v[26:27], v11 offset0:13 offset1:29\n\t"
      "v_mfma_f32_16x16x32_bf16 v[152:155], v[56:59], v[112:115], v[152:155]\n\t"
      "ds_read_b128 v[112:115], v9 offset:6144\n\t"
      "v_lshrrev_b32 v31, 8, v16\n\t"
      "v_mfma_f32_16x16x32_bf16 v[160:163], v[56:59], v[104:107], v[160:163]\n\t"
      "v_and_b32 v28, v13, v16\n\t"
      "s_waitcnt lgkmcnt(4)\n\t"
      "v_mfma_f32_16x16x32_bf16 v[160:163], v[64:67], v[108:111], v[160:163]\n\t"
      "v_and_b32 v29, v14, v16\n\t"
      "v_and_b32 v30, v13, v31\n\t"
      "v_and_b32 v31, v14, v31\n\t"
      "v_mfma_f32_16x16x32_bf16 v[164:167], v[68:71], v[108:111], v[164:167]\n\t"
      "ds_read_b128 v[108:111], v8 offset:8192\n\t"
      "v_fma_mix_f32 v32, v28, v20, -v22 op_sel:[0,0,0] op_sel_hi:[1,0,0]\n\t"
      "v_mfma_f32_16x16x32_bf16 v[164:167], v[60:63], v[104:107], v[164:167]\n\t"
      "ds_read_b128 v[104:107], v9 offset:8192\n\t"
      "v_fma_mix_f32 v33, v29, v24, -v22 op_sel:[0,0,0] op_sel_hi:[1,0,0]\n\t"
      "v_mfma_f32_16x16x32_bf16 v[172:175], v[60:63], v[96:99], v[172:175]\n\t"
      "v_fma_mix_f32 v34, v30, v20, -v22 op_sel:[0,0,0] op_sel_hi:[1,0,0]\n\t"
      "v_fma_mix_f32 v35, v31, v24, -v22 op_sel:[0,0,0] op_sel_hi:[1,0,0]\n\t"
      "v_mfma_f32_16x16x32_bf16 v[172:175], v[68:71], v[100:103], v[172:175]\n\t"
      "v_fma_mix_f32 v36, v28, v20, -v22 op_sel:[1,0,0] op_sel_hi:[1,0,0]\n\t"
      "v_fma_mix_f32 v37, v29, v24, -v22 op_sel:[1,0,0] op_sel_hi:[1,0,0]\n\t"
      "v_mfma_f32_16x16x32_bf16 v[168:171], v[64:67], v[100:103], v[168:171]\n\t"
      "ds_read_b128 v[100:103], v8 offset:10240\n\t"
      "v_cvt_pk_bf16_f32 v40, v32, v33\n\t"
      "v_mfma_f32_16x16x32_bf16 v[168:171], v[56:59], v[96:99], v[168:171]\n\t"
      "ds_read_b128 v[96:99], v9 offset:10240\n\t"
      "v_cvt_pk_bf16_f32 v41, v34, v35\n\t"
      "v_mfma_f32_16x16x32_bf16 v[176:179], v[56:59], v[88:91], v[176:179]\n\t"
      "v_fma_mix_f32 v38, v30, v20, -v22 op_sel:[1,0,0] op_sel_hi:[1,0,0]\n\t"
      "v_fma_mix_f32 v39, v31, v24, -v22 op_sel:[1,0,0] op_sel_hi:[1,0,0]\n\t"
      "v_mfma_f32_16x16x32_bf16 v[176:179], v[64:67], v[92:95], v[176:179]\n\t"
      "v_cvt_pk_bf16_f32 v42, v36, v37\n\t"
      "v_cvt_pk_bf16_f32 v43, v38, v39\n\t"
      "v_mfma_f32_16x16x32_bf16 v[180:183], v[68:71], v[92:95], v[180:183]\n\t"
      "ds_read_b128 v[92:95], v8 offset:12288\n\t"
      "v_lshrrev_b32 v31, 8, v17\n\t"
      "v_mfma_f32_16x16x32_bf16 v[180:183], v[60:63], v[88:91], v[180:183]\n\t"
      "ds_read_b128 v[88:91], v9 offset:12288\n\t"
      "v_and_b32 v28, v13, v17\n\t"
      "v_mfma_f32_16x16x32_bf16 v[188:191], v[60:63], v[80:83], v[188:191]\n\t"
      "v_and_b32 v29, v14, v17\n\t"
      "v_and_b32 v30, v13, v31\n\t"
      "v_mfma_f32_16x16x32_bf16 v[188:191], v[68:71], v[84:87], v[188:191]\n\t"
      "v_and_b32 v31, v14, v31\n\t"
      "ds_read_b64 v[16:17], v10 offset:4096\n\t"
      "v_mfma_f32_16x16x32_bf16 v[184:187], v[64:67], v[84:87], v[184:187]\n\t"
      "ds_read_b128 v[84:87], v8 offset:14336\n\t"
      "v_fma_mix_f32 v32, v28, v21, -v23 op_sel:[0,0,0] op_sel_hi:[1,0,0]\n\t"
      "v_mfma_f32_16x16x32_bf16 v[184:187], v[56:59], v[80:83], v[184:187]\n\t"
      "ds_read_b128 v[80:83], v9 offset:14336\n\t"
      "v_fma_mix_f32 v33, v29, v25, -v23 op_sel:[0,0,0] op_sel_hi:[1,0,0]\n\t"
      "v_mfma_f32_16x16x32_bf16 v[192:195], v[56:59], v[140:143], v[192:195]\n\t"
      "v_fma_mix_f32 v34, v30, v21, -v23 op_sel:[0,0,0] op_sel_hi:[1,0,0]\n\t"
      "v_fma_mix_f32 v35, v31, v25, -v23 op_sel:[0,0,0] op_sel_hi:[1,0,0]\n\t"
      "v_mfma_f32_16x16x32_bf16 v[192:195], v[64:67], v[136:139], v[192:195]\n\t"
      "s_barrier\n\t"
      "v_mfma_f32_16x16x32_bf16 v[196:199], v[68:71], v[136:139], v[196:199]\n\t"
      "ds_read_b128 v[136:139], v8 offset:16384\n\t"
      "v_fma_mix_f32 v36, v28, v21, -v23 op_sel:[1,0,0] op_sel_hi:[1,0,0]\n\t"
      "v_mfma_f32_16x16x32_bf16 v[196:199], v[60:63], v[140:143], v[196:199]\n\t"
      "ds_read_b128 v[140:143], v9 offset:16384\n\t"
      "v_fma_mix_f32 v37, v29, v25, -v23 op_sel:[1,0,0] op_sel_hi:[1,0,0]\n\t"
      "v_mfma_f32_16x16x32_bf16 v[204:207], v[60:63], v[132:135], v[204:207]\n\t"
      "v_cvt_pk_bf16_f32 v44, v32, v33\n\t"
      "v_cvt_pk_bf16_f32 v45, v34, v35\n\t"
      "v_mfma_f32_16x16x32_bf16 v[204:207], v[68:71], v[128:131], v[204:207]\n\t"
      "v_fma_mix_f32 v38, v30, v21, -v23 op_sel:[1,0,0] op_sel_hi:[1,0,0]\n\t"
      "v_fma_mix_f32 v39, v31, v25, -v23 op_sel:[1,0,0] op_sel_hi:[1,0,0]\n\t"
      "v_mfma_f32_16x16x32_bf16 v[200:203], v[64:67], v[128:131], v[200:203]\n\t"
      "ds_read_b128 v[128:131], v8 offset:18432\n\t"
      "v_cvt_pk_bf16_f32 v46, v36, v37\n\t"
      "v_mfma_f32_16x16x32_bf16 v[200:203], v[56:59], v[132:135], v[200:203]\n\t"
      "ds_read_b128 v[132:135], v9 offset:18432\n\t"
      "v_cvt_pk_bf16_f32 v47, v38, v39\n\t"
      "v_mfma_f32_16x16x32_bf16 v[208:211], v[56:59], v[124:127], v[208:211]\n\t"
      "v_lshrrev_b32 v31, 8, v18\n\t"
      "s_waitcnt lgkmcnt(3)\n\t"
      "v_mfma_f32_16x16x32_bf16 v[208:211], v[64:67], v[120:123], v[208:211]\n\t"
      "v_and_b32 v28, v13, v18\n\t"
      "v_and_b32 v29, v14, v18\n\t"
      "v_mfma_f32_16x16x32_bf16 v[212:215], v[68:71], v[120:123], v[212:215]\n\t"
      "ds_read_b128 v[120:123], v8 offset:20480\n\t"
      "v_and_b32 v30, v13, v31\n\t"
      "v_mfma_f32_16x16x32_bf16 v[212:215], v[60:63], v[124:127], v[212:215]\n\t"
      "ds_read_b128 v[124:127], v9 offset:20480\n\t"
      "v_and_b32 v31, v14, v31\n\t"
      "v_mfma_f32_16x16x32_bf16 v[220:223], v[60:63], v[116:119], v[220:223]\n\t"
      "v_fma_mix_f32 v32, v28, v20, -v22 op_sel:[0,0,0] op_sel_hi:[1,0,0]\n\t"
      "v_fma_mix_f32 v33, v29, v24, -v22 op_sel:[0,0,0] op_sel_hi:[1,0,0]\n\t"
      "v_mfma_f32_16x16x32_bf16 v[220:223], v[68:71], v[112:115], v[220:223]\n\t"
      "v_fma_mix_f32 v34, v30, v20, -v22 op_sel:[0,0,0] op_sel_hi:[1,0,0]\n\t"
      "v_fma_mix_f32 v35, v31, v24, -v22 op_sel:[0,0,0] op_sel_hi:[1,0,0]\n\t"
      "v_mfma_f32_16x16x32_bf16 v[216:219], v[64:67], v[112:115], v[216:219]\n\t"
      "ds_read_b128 v[112:115], v8 offset:22528\n\t"
      "v_fma_mix_f32 v36, v28, v20, -v22 op_sel:[1,0,0] op_sel_hi:[1,0,0]\n\t"
      "v_mfma_f32_16x16x32_bf16 v[216:219], v[56:59], v[116:119], v[216:219]\n\t"
      "ds_read_b128 v[116:119], v9 offset:22528\n\t"
      "v_fma_mix_f32 v37, v29, v24, -v22 op_sel:[1,0,0] op_sel_hi:[1,0,0]\n\t"
      "v_mfma_f32_16x16x32_bf16 v[224:227], v[56:59], v[108:111], v[224:227]\n\t"
      "v_cvt_pk_bf16_f32 v48, v32, v33\n\t"
      "v_cvt_pk_bf16_f32 v49, v34, v35\n\t"
      "v_mfma_f32_16x16x32_bf16 v[224:227], v[64:67], v[104:107], v[224:227]\n\t"
      "v_fma_mix_f32 v38, v30, v20, -v22 op_sel:[1,0,0] op_sel_hi:[1,0,0]\n\t"
      "v_fma_mix_f32 v39, v31, v24, -v22 op_sel:[1,0,0] op_sel_hi:[1,0,0]\n\t"
      "v_mfma_f32_16x16x32_bf16 v[228:231], v[68:71], v[104:107], v[228:231]\n\t"
      "v_cvt_pk_bf16_f32 v50, v36, v37\n\t"
      "v_cvt_pk_bf16_f32 v51, v38, v39\n\t"
      "v_mfma_f32_16x16x32_bf16 v[228:231], v[60:63], v[108:111], v[228:231]\n\t"
      "ds_read_b128 v[108:111], v8 offset:24576\n\t"
      "v_lshrrev_b32 v31, 8, v19\n\t"
      "v_mfma_f32_16x16x32_bf16 v[236:239], v[60:63], v[100:103], v[236:239]\n\t"
      "v_and_b32 v28, v13, v19\n\t"
      "v_and_b32 v29, v14, v19\n\t"
      "v_mfma_f32_16x16x32_bf16 v[236:239], v[68:71], v[96:99], v[236:239]\n\t"
      "v_and_b32 v30, v13, v31\n\t"
      "v_and_b32 v31, v14, v31\n\t"
      "v_mfma_f32_16x16x32_bf16 v[232:235], v[64:67], v[96:99], v[232:235]\n\t"
      "ds_read_b64 v[18:19], v10 offset:6144\n\t"
      "v_fma_mix_f32 v32, v28, v21, -v23 op_sel:[0,0,0] op_sel_hi:[1,0,0]\n\t"
      "v_mfma_f32_16x16x32_bf16 v[232:235], v[56:59], v[100:103], v[232:235]\n\t"
      "v_fma_mix_f32 v33, v29, v25, -v23 op_sel:[0,0,0] op_sel_hi:[1,0,0]\n\t"
      "v_fma_mix_f32 v34, v30, v21, -v23 op_sel:[0,0,0] op_sel_hi:[1,0,0]\n\t"
      "v_mfma_f32_16x16x32_bf16 v[240:243], v[56:59], v[92:95], v[240:243]\n\t"
      "v_fma_mix_f32 v35, v31, v25, -v23 op_sel:[0,0,0] op_sel_hi:[1,0,0]\n\t"
      "v_fma_mix_f32 v36, v28, v21, -v23 op_sel:[1,0,0] op_sel_hi:[1,0,0]\n\t"
      "v_mfma_f32_16x16x32_bf16 v[240:243], v[64:67], v[88:91], v[240:243]\n\t"
      "v_fma_mix_f32 v37, v29, v25, -v23 op_sel:[1,0,0] op_sel_hi:[1,0,0]\n\t"
      "v_cvt_pk_bf16_f32 v52, v32, v33\n\t"
      "v_mfma_f32_16x16x32_bf16 v[244:247], v[68:71], v[88:91], v[244:247]\n\t"
      "v_cvt_pk_bf16_f32 v53, v34, v35\n\t"
      "v_fma_mix_f32 v38, v30, v21, -v23 op_sel:[1,0,0] op_sel_hi:[1,0,0]\n\t"
      "v_mfma_f32_16x16x32_bf16 v[244:247], v[60:63], v[92:95], v[244:247]\n\t"
      "v_fma_mix_f32 v39, v31, v25, -v23 op_sel:[1,0,0] op_sel_hi:[1,0,0]\n\t"
      "v_cvt_pk_bf16_f32 v54, v36, v37\n\t"
      "v_mfma_f32_16x16x32_bf16 v[252:255], v[60:63], v[84:87], v[252:255]\n\t"
      "v_cvt_pk_bf16_f32 v55, v38, v39\n\t"
      "v_mfma_f32_16x16x32_bf16 v[252:255], v[68:71], v[80:83], v[252:255]\n\t"
      "v_mfma_f32_16x16x32_bf16 v[248:251], v[64:67], v[80:83], v[248:251]\n\t"
      "v_mfma_f32_16x16x32_bf16 v[248:251], v[56:59], v[84:87], v[248:251]\n\t"
      "v_mfma_f32_16x16x32_bf16 v[144:147], v[40:43], v[136:139], v[144:147]\n\t"
      "v_lshlrev_b32 v28, 16, v26\n\t"
      "v_and_b32 v29, 0xffff0000, v26\n\t"
      "v_lshlrev_b32 v30, 16, v27\n\t"
      "v_and_b32 v31, 0xffff0000, v27\n\t"
      "v_mul_f32 v20, 0x4b800000, v28\n\t"
      "v_mul_f32 v24, 0x49800000, v28\n\t"
      "v_mul_f32 v22, v28, v30\n\t"
      "v_mul_f32 v21, 0x4b800000, v29\n\t"
      "v_mul_f32 v25, 0x49800000, v29\n\t"
      "v_mul_f32 v23, v29, v31\n\t"
      "s_waitcnt lgkmcnt(0)\n\t"
      "v_mfma_f32_16x16x32_bf16 v[144:147], v[48:51], v[140:143], v[144:147]\n\t"
      "ds_read2st64_b32 v[26:27], v11 offset0:14 offset1:30\n\t"
      "v_lshrrev_b32 v31, 8, v16\n\t"
      "v_and_b32 v28, v13, v16\n\t"
      "v_and_b32 v29, v14, v16\n\t"
      "v_and_b32 v30, v13, v31\n\t"
      "v_and_b32 v31, v14, v31\n\t"
      "v_fma_mix_f32 v32, v28, v20, -v22 op_sel:[0,0,0] op_sel_hi:[1,0,0]\n\t"
      "v_fma_mix_f32 v33, v29, v24, -v22 op_sel:[0,0,0] op_sel_hi:[1,0,0]\n\t"
      "v_fma_mix_f32 v34, v30, v20, -v22 op_sel:[0,0,0] op_sel_hi:[1,0,0]\n\t"
      "v_fma_mix_f32 v35, v31, v24, -v22 op_sel:[0,0,0] op_sel_hi:[1,0,0]\n\t"
      "v_fma_mix_f32 v36, v28, v20, -v22 op_sel:[1,0,0] op_sel_hi:[1,0,0]\n\t"
      "v_mfma_f32_16x16x32_bf16 v[148:151], v[52:55], v[140:143], v[148:151]\n\t"
      "ds_read_b128 v[140:143], v9 offset:24576\n\t"
      "v_fma_mix_f32 v37, v29, v24, -v22 op_sel:[1,0,0] op_sel_hi:[1,0,0]\n\t"
      "v_cvt_pk_bf16_f32 v56, v32, v33\n\t"
      "v_cvt_pk_bf16_f32 v57, v34, v35\n\t"
      "v_fma_mix_f32 v38, v30, v20, -v22 op_sel:[1,0,0] op_sel_hi:[1,0,0]\n\t"
      "v_fma_mix_f32 v39, v31, v24, -v22 op_sel:[1,0,0] op_sel_hi:[1,0,0]\n\t"
      "v_cvt_pk_bf16_f32 v58, v36, v37\n\t"
      "v_cvt_pk_bf16_f32 v59, v38, v39\n\t"
      "v_lshrrev_b32 v31, 8, v17\n\t"
      "v_and_b32 v28, v13, v17\n\t"
      "v_and_b32 v29, v14, v17\n\t"
      "v_mfma_f32_16x16x32_bf16 v[148:151], v[44:47], v[136:139], v[148:151]\n\t"
      "ds_read_b128 v[136:139], v8 offset:26624\n\t"
      "v_and_b32 v30, v13, v31\n\t"
      "v_and_b32 v31, v14, v31\n\t"
      "ds_read_b64 v[16:17], v10 offset:8192\n\t"
      "v_fma_mix_f32 v32, v28, v21, -v23 op_sel:[0,0,0] op_sel_hi:[1,0,0]\n\t"
      "v_fma_mix_f32 v33, v29, v25, -v23 op_sel:[0,0,0] op_sel_hi:[1,0,0]\n\t"
      "v_fma_mix_f32 v34, v30, v21, -v23 op_sel:[0,0,0] op_sel_hi:[1,0,0]\n\t"
      "v_fma_mix_f32 v35, v31, v25, -v23 op_sel:[0,0,0] op_sel_hi:[1,0,0]\n\t"
      "v_fma_mix_f32 v36, v28, v21, -v23 op_sel:[1,0,0] op_sel_hi:[1,0,0]\n\t"
      "v_fma_mix_f32 v37, v29, v25, -v23 op_sel:[1,0,0] op_sel_hi:[1,0,0]\n\t"
      "v_cvt_pk_bf16_f32 v60, v32, v33\n\t"
      "v_mfma_f32_16x16x32_bf16 v[156:159], v[44:47], v[128:131], v[156:159]\n\t"
      "v_cvt_pk_bf16_f32 v61, v34, v35\n\t"
      "v_fma_mix_f32 v38, v30, v21, -v23 op_sel:[1,0,0] op_sel_hi:[1,0,0]\n\t"
      "v_fma_mix_f32 v39, v31, v25, -v23 op_sel:[1,0,0] op_sel_hi:[1,0,0]\n\t"
      "v_cvt_pk_bf16_f32 v62, v36, v37\n\t"
      "v_cvt_pk_bf16_f32 v63, v38, v39\n\t"
      "v_lshrrev_b32 v31, 8, v18\n\t"
      "v_and_b32 v28, v13, v18\n\t"
      "v_and_b32 v29, v14, v18\n\t"
      "v_and_b32 v30, v13, v31\n\t"
      "v_and_b32 v31, v14, v31\n\t"
      "v_fma_mix_f32 v32, v28, v20, -v22 op_sel:[0,0,0] op_sel_hi:[1,0,0]\n\t"
      "v_mfma_f32_16x16x32_bf16 v[156:159], v[52:55], v[132:135], v[156:159]\n\t"
      "v_fma_mix_f32 v33, v29, v24, -v22 op_sel:[0,0,0] op_sel_hi:[1,0,0]\n\t"
      "v_fma_mix_f32 v34, v30, v20, -v22 op_sel:[0,0,0] op_sel_hi:[1,0,0]\n\t"
      "v_fma_mix_f32 v35, v31, v24, -v22 op_sel:[0,0,0] op_sel_hi:[1,0,0]\n\t"
      "v_fma_mix_f32 v36, v28, v20, -v22 op_sel:[1,0,0] op_sel_hi:[1,0,0]\n\t"
      "v_fma_mix_f32 v37, v29, v24, -v22 op_sel:[1,0,0] op_sel_hi:[1,0,0]\n\t"
      "v_cvt_pk_bf16_f32 v64, v32, v33\n\t"
      "v_cvt_pk_bf16_f32 v65, v34, v35\n\t"
      "v_fma_mix_f32 v38, v30, v20, -v22 op_sel:[1,0,0] op_sel_hi:[1,0,0]\n\t"
      "v_fma_mix_f32 v39, v31, v24, -v22 op_sel:[1,0,0] op_sel_hi:[1,0,0]\n\t"
      "v_cvt_pk_bf16_f32 v66, v36, v37\n\t"
      "v_mfma_f32_16x16x32_bf16 v[152:155], v[48:51], v[132:135], v[152:155]\n\t"
      "ds_read_b128 v[132:135], v9 offset:26624\n\t"
      "v_cvt_pk_bf16_f32 v67, v38, v39\n\t"
      "v_lshrrev_b32 v31, 8, v19\n\t"
      "v_and_b32 v28, v13, v19\n\t"
      "v_and_b32 v29, v14, v19\n\t"
      "v_and_b32 v30, v13, v31\n\t"
      "v_and_b32 v31, v14, v31\n\t"
      "ds_read_b64 v[18:19], v10 offset:10240\n\t"
      "v_fma_mix_f32 v32, v28, v21, -v23 op_sel:[0,0,0] op_sel_hi:[1,0,0]\n\t"
      "v_fma_mix_f32 v33, v29, v25, -v23 op_sel:[0,0,0] op_sel_hi:[1,0,0]\n\t"
      "v_mfma_f32_16x16x32_bf16 v[152:155], v[40:43], v[128:131], v[152:155]\n\t"
      "ds_read_b128 v[128:131], v8 offset:28672\n\t"
      "v_fma_mix_f32 v34, v30, v21, -v23 op_sel:[0,0,0] op_sel_hi:[1,0,0]\n\t"
      "v_fma_mix_f32 v35, v31, v25, -v23 op_sel:[0,0,0] op_sel_hi:[1,0,0]\n\t"
      "v_fma_mix_f32 v36, v28, v21, -v23 op_sel:[1,0,0] op_sel_hi:[1,0,0]\n\t"
      "v_fma_mix_f32 v37, v29, v25, -v23 op_sel:[1,0,0] op_sel_hi:[1,0,0]\n\t"
      "v_cvt_pk_bf16_f32 v68, v32, v33\n\t"
      "v_cvt_pk_bf16_f32 v69, v34, v35\n\t"
      "v_fma_mix_f32 v38, v30, v21, -v23 op_sel:[1,0,0] op_sel_hi:[1,0,0]\n\t"
      "v_fma_mix_f32 v39, v31, v25, -v23 op_sel:[1,0,0] op_sel_hi:[1,0,0]\n\t"
      "v_cvt_pk_bf16_f32 v70, v36, v37\n\t"
      "v_cvt_pk_bf16_f32 v71, v38, v39\n\t"
      "v_mfma_f32_16x16x32_bf16 v[144:147], v[56:59], v[120:123], v[144:147]\n\t"
      "s_waitcnt lgkmcnt(1)\n\t"
      "v_lshlrev_b32 v28, 16, v26\n\t"
      "v_and_b32 v29, 0xffff0000, v26\n\t"
      "v_lshlrev_b32 v30, 16, v27\n\t"
      "v_and_b32 v31, 0xffff0000, v27\n\t"
      "v_mul_f32 v20, 0x4b800000, v28\n\t"
      "v_mul_f32 v24, 0x49800000, v28\n\t"
      "v_mul_f32 v22, v28, v30\n\t"
      "v_mul_f32 v21, 0x4b800000, v29\n\t"
      "v_mul_f32 v25, 0x49800000, v29\n\t"
      "v_mul_f32 v23, v29, v31\n\t"
      "v_mfma_f32_16x16x32_bf16 v[144:147], v[64:67], v[124:127], v[144:147]\n\t"
      "ds_read2st64_b32 v[26:27], v11 offset0:15 offset1:31\n\t"
      "v_lshrrev_b32 v31, 8, v16\n\t"
      "v_and_b32 v28, v13, v16\n\t"
      "v_and_b32 v29, v14, v16\n\t"
      "v_and_b32 v30, v13, v31\n\t"
      "v_and_b32 v31, v14, v31\n\t"
      "v_fma_mix_f32 v32, v28, v20, -v22 op_sel:[0,0,0] op_sel_hi:[1,0,0]\n\t"
      "v_fma_mix_f32 v33, v29, v24, -v22 op_sel:[0,0,0] op_sel_hi:[1,0,0]\n\t"
      "v_fma_mix_f32 v34, v30, v20, -v22 op_sel:[0,0,0] op_sel_hi:[1,0,0]\n\t"
      "v_fma_mix_f32 v35, v31, v24, -v22 op_sel:[0,0,0] op_sel_hi:[1,0,0]\n\t"
      "v_fma_mix_f32 v36, v28, v20, -v22 op_sel:[1,0,0] op_sel_hi:[1,0,0]\n\t"
      "v_mfma_f32_16x16x32_bf16 v[148:151], v[68:71], v[124:127], v[148:151]\n\t"
      "ds_read_b128 v[124:127], v9 offset:28672\n\t"
      "v_fma_mix_f32 v37, v29, v24, -v22 op_sel:[1,0,0] op_sel_hi:[1,0,0]\n\t"
      "v_cvt_pk_bf16_f32 v72, v32, v33\n\t"
      "v_cvt_pk_bf16_f32 v73, v34, v35\n\t"
      "v_fma_mix_f32 v38, v30, v20, -v22 op_sel:[1,0,0] op_sel_hi:[1,0,0]\n\t"
      "v_fma_mix_f32 v39, v31, v24, -v22 op_sel:[1,0,0] op_sel_hi:[1,0,0]\n\t"
      "v_cvt_pk_bf16_f32 v74, v36, v37\n\t"
      "v_cvt_pk_bf16_f32 v75, v38, v39\n\t"
      "v_lshrrev_b32 v31, 8, v17\n\t"
      "v_and_b32 v28, v13, v17\n\t"
      "v_and_b32 v29, v14, v17\n\t"
      "v_mfma_f32_16x16x32_bf16 v[148:151], v[60:63], v[120:123], v[148:151]\n\t"
      "ds_read_b128 v[120:123], v8 offset:30720\n\t"
      "v_and_b32 v30, v13, v31\n\t"
      "v_and_b32 v31, v14, v31\n\t"
      "ds_read_b64 v[16:17], v10 offset:12288\n\t"
      "v_fma_mix_f32 v32, v28, v21, -v23 op_sel:[0,0,0] op_sel_hi:[1,0,0]\n\t"
      "v_fma_mix_f32 v33, v29, v25, -v23 op_sel:[0,0,0] op_sel_hi:[1,0,0]\n\t"
      "v_fma_mix_f32 v34, v30, v21, -v23 op_sel:[0,0,0] op_sel_hi:[1,0,0]\n\t"
      "v_fma_mix_f32 v35, v31, v25, -v23 op_sel:[0,0,0] op_sel_hi:[1,0,0]\n\t"
      "v_fma_mix_f32 v36, v28, v21, -v23 op_sel:[1,0,0] op_sel_hi:[1,0,0]\n\t"
      "v_fma_mix_f32 v37, v29, v25, -v23 op_sel:[1,0,0] op_sel_hi:[1,0,0]\n\t"
      "v_cvt_pk_bf16_f32 v76, v32, v33\n\t"
      "v_mfma_f32_16x16x32_bf16 v[156:159], v[60:63], v[112:115], v[156:159]\n\t"
      "v_cvt_pk_bf16_f32 v77, v34, v35\n\t"
      "v_fma_mix_f32 v38, v30, v21, -v23 op_sel:[1,0,0] op_sel_hi:[1,0,0]\n\t"
      "v_fma_mix_f32 v39, v31, v25, -v23 op_sel:[1,0,0] op_sel_hi:[1,0,0]\n\t"
      "v_cvt_pk_bf16_f32 v78, v36, v37\n\t"
      "v_cvt_pk_bf16_f32 v79, v38, v39\n\t"
      "v_lshrrev_b32 v31, 8, v18\n\t"
      "v_and_b32 v28, v13, v18\n\t"
      "v_and_b32 v29, v14, v18\n\t"
      "v_and_b32 v30, v13, v31\n\t"
      "v_and_b32 v31, v14, v31\n\t"
      "v_fma_mix_f32 v32, v28, v20, -v22 op_sel:[0,0,0] op_sel_hi:[1,0,0]\n\t"
      "v_mfma_f32_16x16x32_bf16 v[156:159], v[68:71], v[116:119], v[156:159]\n\t"
      "v_fma_mix_f32 v33, v29, v24, -v22 op_sel:[0,0,0] op_sel_hi:[1,0,0]\n\t"
      "v_fma_mix_f32 v34, v30, v20, -v22 op_sel:[0,0,0] op_sel_hi:[1,0,0]\n\t"
      "v_fma_mix_f32 v35, v31, v24, -v22 op_sel:[0,0,0] op_sel_hi:[1,0,0]\n\t"
      "v_fma_mix_f32 v36, v28, v20, -v22 op_sel:[1,0,0] op_sel_hi:[1,0,0]\n\t"
      "v_fma_mix_f32 v37, v29, v24, -v22 op_sel:[1,0,0] op_sel_hi:[1,0,0]\n\t"
      "v_cvt_pk_bf16_f32 v80, v32, v33\n\t"
      "v_cvt_pk_bf16_f32 v81, v34, v35\n\t"
      "v_fma_mix_f32 v38, v30, v20, -v22 op_sel:[1,0,0] op_sel_hi:[1,0,0]\n\t"
      "v_fma_mix_f32 v39, v31, v24, -v22 op_sel:[1,0,0] op_sel_hi:[1,0,0]\n\t"
      "v_cvt_pk_bf16_f32 v82, v36, v37\n\t"
      "v_mfma_f32_16x16x32_bf16 v[152:155], v[64:67], v[116:119], v[152:155]\n\t"
      "ds_read_b128 v[116:119], v9 offset:30720\n\t"
      "v_cvt_pk_bf16_f32 v83, v38, v39\n\t"
      "v_lshrrev_b32 v31, 8, v19\n\t"
      "v_and_b32 v28, v13, v19\n\t"
      "v_and_b32 v29, v14, v19\n\t"
      "v_and_b32 v30, v13, v31\n\t"
      "v_and_b32 v31, v14, v31\n\t"
      "ds_read_b64 v[18:19], v10 offset:14336\n\t"
      "v_fma_mix_f32 v32, v28, v21, -v23 op_sel:[0,0,0] op_sel_hi:[1,0,0]\n\t"
      "v_fma_mix_f32 v33, v29, v25, -v23 op_sel:[0,0,0] op_sel_hi:[1,0,0]\n\t"
      "v_mfma_f32_16x16x32_bf16 v[152:155], v[56:59], v[112:115], v[152:155]\n\t"
      "v_fma_mix_f32 v34, v30, v21, -v23 op_sel:[0,0,0] op_sel_hi:[1,0,0]\n\t"
      "v_fma_mix_f32 v35, v31, v25, -v23 op_sel:[0,0,0] op_sel_hi:[1,0,0]\n\t"
      "v_fma_mix_f32 v36, v28, v21, -v23 op_sel:[1,0,0] op_sel_hi:[1,0,0]\n\t"
      "v_fma_mix_f32 v37, v29, v25, -v23 op_sel:[1,0,0] op_sel_hi:[1,0,0]\n\t"
      "v_cvt_pk_bf16_f32 v84, v32, v33\n\t"
      "v_cvt_pk_bf16_f32 v85, v34, v35\n\t"
      "v_fma_mix_f32 v38, v30, v21, -v23 op_sel:[1,0,0] op_sel_hi:[1,0,0]\n\t"
      "v_fma_mix_f32 v39, v31, v25, -v23 op_sel:[1,0,0] op_sel_hi:[1,0,0]\n\t"
      "v_cvt_pk_bf16_f32 v86, v36, v37\n\t"
      "v_cvt_pk_bf16_f32 v87, v38, v39\n\t"
      "v_mfma_f32_16x16x32_bf16 v[144:147], v[72:75], v[108:111], v[144:147]\n\t"
      "s_waitcnt lgkmcnt(0)\n\t"
      "v_lshlrev_b32 v28, 16, v26\n\t"
      "v_and_b32 v29, 0xffff0000, v26\n\t"
      "v_lshlrev_b32 v30, 16, v27\n\t"
      "v_and_b32 v31, 0xffff0000, v27\n\t"
      "v_mul_f32 v20, 0x4b800000, v28\n\t"
      "v_mul_f32 v24, 0x49800000, v28\n\t"
      "v_mul_f32 v22, v28, v30\n\t"
      "v_mul_f32 v21, 0x4b800000, v29\n\t"
      "v_mul_f32 v25, 0x49800000, v29\n\t"
      "v_mul_f32 v23, v29, v31\n\t"
      "v_mfma_f32_16x16x32_bf16 v[144:147], v[80:83], v[140:143], v[144:147]\n\t"
      "s_barrier\n\t"
      "s_nop 0\n\t"
      "LH14x4_%=:\n\t"
      "s_cbranch_execz LHRET_%=\n\t"
      "v_mfma_f32_16x16x32_bf16 v[148:151], v[84:87], v[140:143], v[148:151]\n\t"
      "ds_read_b128 v[112:115], v8 offset:32768\n\t"
      "v_lshrrev_b32 v31, 8, v16\n\t"
      "v_and_b32 v28, v13, v16\n\t"
      "v_and_b32 v29, v14, v16\n\t"
      "v_and_b32 v30, v13, v31\n\t"
      "v_and_b32 v31, v14, v31\n\t"
      "v_fma_mix_f32 v32, v28, v20, -v22 op_sel:[0,0,0] op_sel_hi:[1,0,0]\n\t"
      "v_fma_mix_f32 v33, v29, v24, -v22 op_sel:[0,0,0] op_sel_hi:[1,0,0]\n\t"
      "v_fma_mix_f32 v34, v30, v20, -v22 op_sel:[0,0,0] op_sel_hi:[1,0,0]\n\t"
      "v_fma_mix_f32 v35, v31, v24, -v22 op_sel:[0,0,0] op_sel_hi:[1,0,0]\n\t"
      "v_fma_mix_f32 v36, v28, v20, -v22 op_sel:[1,0,0] op_sel_hi:[1,0,0]\n\t"
      "v_fma_mix_f32 v37, v29, v24, -v22 op_sel:[1,0,0] op_sel_hi:[1,0,0]\n\t"
      "v_mfma_f32_16x16x32_bf16 v[148:151], v[76:79], v[108:111], v[148:151]\n\t"
      "ds_read_b128 v[140:143], v9 offset:32768\n\t"
      "v_cvt_pk_bf16_f32 v88, v32, v33\n\t"
      "v_cvt_pk_bf16_f32 v89, v34, v35\n\t"
      "v_fma_mix_f32 v38, v30, v20, -v22 op_sel:[1,0,0] op_sel_hi:[1,0,0]\n\t"
      "v_fma_mix_f32 v39, v31, v24, -v22 op_sel:[1,0,0] op_sel_hi:[1,0,0]\n\t"
      "v_cvt_pk_bf16_f32 v90, v36, v37\n\t"
      "v_cvt_pk_bf16_f32 v91, v38, v39\n\t"
      "v_lshrrev_b32 v31, 8, v17\n\t"
      "v_and_b32 v28, v13, v17\n\t"
      "v_and_b32 v29, v14, v17\n\t"
      "v_and_b32 v30, v13, v31\n\t"
      "v_and_b32 v31, v14, v31\n\t"
      "v_mfma_f32_16x16x32_bf16 v[156:159], v[76:79], v[136:139], v[156:159]\n\t"
      "ds_read_b128 v[108:111], v8 offset:34816\n\t"
      "v_fma_mix_f32 v32, v28, v21, -v23 op_sel:[0,0,0] op_sel_hi:[1,0,0]\n\t"
      "v_fma_mix_f32 v33, v29, v25, -v23 op_sel:[0,0,0] op_sel_hi:[1,0,0]\n\t"
      "v_fma_mix_f32 v34, v30, v21, -v23 op_sel:[0,0,0] op_sel_hi:[1,0,0]\n\t"
      "v_fma_mix_f32 v35, v31, v25, -v23 op_sel:[0,0,0] op_sel_hi:[1,0,0]\n\t"
      "v_fma_mix_f32 v36, v28, v21, -v23 op_sel:[1,0,0] op_sel_hi:[1,0,0]\n\t"
      "v_fma_mix_f32 v37, v29, v25, -v23 op_sel:[1,0,0] op_sel_hi:[1,0,0]\n\t"
      "v_cvt_pk_bf16_f32 v92, v32, v33\n\t"
      "v_cvt_pk_bf16_f32 v93, v34, v35\n\t"
      "v_fma_mix_f32 v38, v30, v21, -v23 op_sel:[1,0,0] op_sel_hi:[1,0,0]\n\t"
      "v_fma_mix_f32 v39, v31, v25, -v23 op_sel:[1,0,0] op_sel_hi:[1,0,0]\n\t"
      "v_cvt_pk_bf16_f32 v94, v36, v37\n\t"
      "v_mfma_f32_16x16x32_bf16 v[156:159], v[84:87], v[132:135], v[156:159]\n\t"
      "v_cvt_pk_bf16_f32 v95, v38, v39\n\t"
      "v_lshrrev_b32 v31, 8, v18\n\t"
      "v_and_b32 v28, v13, v18\n\t"
      "v_and_b32 v29, v14, v18\n\t"
      "v_and_b32 v30, v13, v31\n\t"
      "v_and_b32 v31, v14, v31\n\t"
      "v_fma_mix_f32 v32, v28, v20, -v22 op_sel:[0,0,0] op_sel_hi:[1,0,0]\n\t"
      "v_fma_mix_f32 v33, v29, v24, -v22 op_sel:[0,0,0] op_sel_hi:[1,0,0]\n\t"
      "v_fma_mix_f32 v34, v30, v20, -v22 op_sel:[0,0,0] op_sel_hi:[1,0,0]\n\t"
      "v_fma_mix_f32 v35, v31, v24, -v22 op_sel:[0,0,0] op_sel_hi:[1,0,0]\n\t"
      "v_fma_mix_f32 v36, v28, v20, -v22 op_sel:[1,0,0] op_sel_hi:[1,0,0]\n\t"
      "v_fma_mix_f32 v37, v29, v24, -v22 op_sel:[1,0,0] op_sel_hi:[1,0,0]\n\t"
      "v_mfma_f32_16x16x32_bf16 v[152:155], v[80:83], v[132:135], v[152:155]\n\t"
      "ds_read_b128 v[132:135], v9 offset:34816\n\t"
      "v_cvt_pk_bf16_f32 v96, v32, v33\n\t"
      "v_cvt_pk_bf16_f32 v97, v34, v35\n\t"
      "v_fma_mix_f32 v38, v30, v20, -v22 op_sel:[1,0,0] op_sel_hi:[1,0,0]\n\t"
      "v_fma_mix_f32 v39, v31, v24, -v22 op_sel:[1,0,0] op_sel_hi:[1,0,0]\n\t"
      "v_cvt_pk_bf16_f32 v98, v36, v37\n\t"
      "v_cvt_pk_bf16_f32 v99, v38, v39\n\t"
      "v_lshrrev_b32 v31, 8, v19\n\t"
      "v_and_b32 v28, v13, v19\n\t"
      "v_and_b32 v29, v14, v19\n\t"
      "v_and_b32 v30, v13, v31\n\t"
      "v_and_b32 v31, v14, v31\n\t"
      "v_mfma_f32_16x16x32_bf16 v[152:155], v[72:75], v[136:139], v[152:155]\n\t"
      "ds_read_b128 v[136:139], v8 offset:36864\n\t"
      "v_fma_mix_f32 v32, v28, v21, -v23 op_sel:[0,0,0] op_sel_hi:[1,0,0]\n\t"
      "v_fma_mix_f32 v33, v29, v25, -v23 op_sel:[0,0,0] op_sel_hi:[1,0,0]\n\t"
      "v_fma_mix_f32 v34, v30, v21, -v23 op_sel:[0,0,0] op_sel_hi:[1,0,0]\n\t"
      "v_fma_mix_f32 v35, v31, v25, -v23 op_sel:[0,0,0] op_sel_hi:[1,0,0]\n\t"
      "v_fma_mix_f32 v36, v28, v21, -v23 op_sel:[1,0,0] op_sel_hi:[1,0,0]\n\t"
      "v_fma_mix_f32 v37, v29, v25, -v23 op_sel:[1,0,0] op_sel_hi:[1,0,0]\n\t"
      "v_cvt_pk_bf16_f32 v100, v32, v33\n\t"
      "v_cvt_pk_bf16_f32 v101, v34, v35\n\t"
      "v_fma_mix_f32 v38, v30, v21, -v23 op_sel:[1,0,0] op_sel_hi:[1,0,0]\n\t"
      "v_fma_mix_f32 v39, v31, v25, -v23 op_sel:[1,0,0] op_sel_hi:[1,0,0]\n\t"
      "v_cvt_pk_bf16_f32 v102, v36, v37\n\t"
      "v_cvt_pk_bf16_f32 v103, v38, v39\n\t"
      "v_mfma_f32_16x16x32_bf16 v[144:147], v[88:91], v[128:131], v[144:147]\n\t"
      "ds_read_b128 v[24:27], v9 offset:36864\n\t"
      "ds_read_b128 v[28:31], v8 offset:38912\n\t"
      "v_mfma_f32_16x16x32_bf16 v[144:147], v[96:99], v[124:127], v[144:147]\n\t"
      "ds_read_b128 v[32:35], v9 offset:38912\n\t"
      "ds_read_b128 v[36:39], v8 offset:40960\n\t"
      "v_mfma_f32_16x16x32_bf16 v[148:151], v[100:103], v[124:127], v[148:151]\n\t"
      "ds_read_b128 v[124:127], v9 offset:40960\n\t"
      "v_mfma_f32_16x16x32_bf16 v[148:151], v[92:95], v[128:131], v[148:151]\n\t"
      "ds_read_b128 v[128:131], v8 offset:43008\n\t"
      "v_mfma_f32_16x16x32_bf16 v[156:159], v[92:95], v[120:123], v[156:159]\n\t"
      "v_mfma_f32_16x16x32_bf16 v[156:159], v[100:103], v[116:119], v[156:159]\n\t"
      "v_mfma_f32_16x16x32_bf16 v[152:155], v[96:99], v[116:119], v[152:155]\n\t"
      "ds_read_b128 v[116:119], v9 offset:43008\n\t"
      "v_mfma_f32_16x16x32_bf16 v[152:155], v[88:91], v[120:123], v[152:155]\n\t"
      "ds_read_b128 v[120:123], v8 offset:45056\n\t"
      "s_waitcnt lgkmcnt(4)\n\t"
      "v_mfma_f32_16x16x32_bf16 v[160:163], v[40:43], v[112:115], v[160:163]\n\t"
      "v_mfma_f32_16x16x32_bf16 v[160:163], v[48:51], v[140:143], v[160:163]\n\t"
      "v_mfma_f32_16x16x32_bf16 v[164:167], v[52:55], v[140:143], v[164:167]\n\t"
      "ds_read_b128 v[140:143], v9 offset:45056\n\t"
      "v_cvt_pk_bf16_f32 v16, v144, v148\n\t"
      "v_mfma_f32_16x16x32_bf16 v[164:167], v[44:47], v[112:115], v[164:167]\n\t"
      "ds_read_b128 v[112:115], v8 offset:47104\n\t"
      "v_cvt_pk_bf16_f32 v17, v145, v149\n\t"
      "v_mfma_f32_16x16x32_bf16 v[172:175], v[44:47], v[108:111], v[172:175]\n\t"
      "v_cvt_pk_bf16_f32 v18, v146, v150\n\t"
      "v_mfma_f32_16x16x32_bf16 v[172:175], v[52:55], v[132:135], v[172:175]\n\t"
      "ds_write_b64 v2, v[16:17] offset:0\n\t"
      "v_mfma_f32_16x16x32_bf16 v[168:171], v[48:51], v[132:135], v[168:171]\n\t"
      "ds_read_b128 v[132:135], v9 offset:47104\n\t"
      "v_cvt_pk_bf16_f32 v19, v147, v151\n\t"
      "v_mfma_f32_16x16x32_bf16 v[168:171], v[40:43], v[108:111], v[168:171]\n\t"
      "ds_read_b128 v[108:111], v8 offset:49152\n\t"
      "v_mfma_f32_16x16x32_bf16 v[160:163], v[56:59], v[136:139], v[160:163]\n\t"
      "ds_write_b64 v2, v[18:19] offset:8\n\t"
      "v_mfma_f32_16x16x32_bf16 v[160:163], v[64:67], v[24:27], v[160:163]\n\t"
      "v_cvt_pk_bf16_f32 v20, v152, v156\n\t"
      "v_cvt_pk_bf16_f32 v21, v153, v157\n\t"
      "v_mfma_f32_16x16x32_bf16 v[164:167], v[68:71], v[24:27], v[164:167]\n\t"
      "ds_read_b128 v[24:27], v9 offset:49152\n\t"
      "v_cvt_pk_bf16_f32 v22, v154, v158\n\t"
      "v_mfma_f32_16x16x32_bf16 v[164:167], v[60:63], v[136:139], v[164:167]\n\t"
      "ds_read_b128 v[136:139], v8 offset:51200\n\t"
      "v_mfma_f32_16x16x32_bf16 v[172:175], v[60:63], v[28:31], v[172:175]\n\t"
      "ds_write_b64 v2, v[20:21] offset:4096\n\t"
      "v_mfma_f32_16x16x32_bf16 v[172:175], v[68:71], v[32:35], v[172:175]\n\t"
      "v_cvt_pk_bf16_f32 v23, v155, v159\n\t"
      "v_mfma_f32_16x16x32_bf16 v[168:171], v[64:67], v[32:35], v[168:171]\n\t"
      "ds_read_b128 v[32:35], v9 offset:51200\n\t"
      "v_mfma_f32_16x16x32_bf16 v[168:171], v[56:59], v[28:31], v[168:171]\n\t"
      "ds_read_b128 v[28:31], v8 offset:53248\n\t"
      "v_mfma_f32_16x16x32_bf16 v[160:163], v[72:75], v[36:39], v[160:163]\n\t"
      "ds_write_b64 v2, v[22:23] offset:4104\n\t"
      "s_waitcnt lgkmcnt(5)\n\t"
      "v_mfma_f32_16x16x32_bf16 v[160:163], v[80:83], v[124:127], v[160:163]\n\t"
      "v_mfma_f32_16x16x32_bf16 v[164:167], v[84:87], v[124:127], v[164:167]\n\t"
      "ds_read_b128 v[124:127], v9 offset:53248\n\t"
      "v_mfma_f32_16x16x32_bf16 v[164:167], v[76:79], v[36:39], v[164:167]\n\t"
      "ds_read_b128 v[36:39], v8 offset:55296\n\t"
      "v_mfma_f32_16x16x32_bf16 v[172:175], v[76:79], v[128:131], v[172:175]\n\t"
      "v_mfma_f32_16x16x32_bf16 v[172:175], v[84:87], v[116:119], v[172:175]\n\t"
      "v_mfma_f32_16x16x32_bf16 v[168:171], v[80:83], v[116:119], v[168:171]\n\t"
      "ds_read_b128 v[116:119], v9 offset:55296\n\t"
      "v_mfma_f32_16x16x32_bf16 v[168:171], v[72:75], v[128:131], v[168:171]\n\t"
      "ds_read_b128 v[128:131], v8 offset:57344\n\t"
      "v_mfma_f32_16x16x32_bf16 v[160:163], v[88:91], v[120:123], v[160:163]\n\t"
      "s_waitcnt lgkmcnt(4)\n\t"
      "s_barrier\n\t"
      "v_mfma_f32_16x16x32_bf16 v[160:163], v[96:99], v[140:143], v[160:163]\n\t"
      "v_mfma_f32_16x16x32_bf16 v[164:167], v[100:103], v[140:143], v[164:167]\n\t"
      "ds_read_b128 v[140:143], v9 offset:57344\n\t"
      "v_mfma_f32_16x16x32_bf16 v[164:167], v[92:95], v[120:123], v[164:167]\n\t"
      "ds_read_b128 v[120:123], v8 offset:59392\n\t"
      "v_mfma_f32_16x16x32_bf16 v[172:175], v[92:95], v[112:115], v[172:175]\n\t"
      "v_mfma_f32_16x16x32_bf16 v[172:175], v[100:103], v[132:135], v[172:175]\n\t"
      "v_mfma_f32_16x16x32_bf16 v[168:171], v[96:99], v[132:135], v[168:171]\n\t"
      "ds_read_b128 v[132:135], v9 offset:59392\n\t"
      "v_mfma_f32_16x16x32_bf16 v[168:171], v[88:91], v[112:115], v[168:171]\n\t"
      "ds_read_b128 v[112:115], v8 offset:61440\n\t"
      "v_mfma_f32_16x16x32_bf16 v[176:179], v[40:43], v[108:111], v[176:179]\n\t"
      "v_mfma_f32_16x16x32_bf16 v[176:179], v[48:51], v[24:27], v[176:179]\n\t"
      "v_mfma_f32_16x16x32_bf16 v[180:183], v[52:55], v[24:27], v[180:183]\n\t"
      "ds_read_b128 v[24:27], v9 offset:61440\n\t"
      "v_cvt_pk_bf16_f32 v16, v160, v164\n\t"
      "v_mfma_f32_16x16x32_bf16 v[180:183], v[44:47], v[108:111], v[180:183]\n\t"
      "ds_read_b128 v[108:111], v8 offset:63488\n\t"
      "v_cvt_pk_bf16_f32 v17, v161, v165\n\t"
      "v_mfma_f32_16x16x32_bf16 v[188:191], v[44:47], v[136:139], v[188:191]\n\t"
      "v_cvt_pk_bf16_f32 v18, v162, v166\n\t"
      "v_mfma_f32_16x16x32_bf16 v[188:191], v[52:55], v[32:35], v[188:191]\n\t"
      "ds_write_b64 v2, v[16:17] offset:8192\n\t"
      "v_mfma_f32_16x16x32_bf16 v[184:187], v[48:51], v[32:35], v[184:187]\n\t"
      "ds_read_b128 v[32:35], v9 offset:63488\n\t"
      "v_cvt_pk_bf16_f32 v19, v163, v167\n\t"
      "v_mfma_f32_16x16x32_bf16 v[184:187], v[40:43], v[136:139], v[184:187]\n\t"
      "ds_read_b128 v[136:139], v6 offset:0\n\t"
      "v_mfma_f32_16x16x32_bf16 v[176:179], v[56:59], v[28:31], v[176:179]\n\t"
      "ds_write_b64 v2, v[18:19] offset:8200\n\t"
      "s_waitcnt lgkmcnt(5)\n\t"
      "v_mfma_f32_16x16x32_bf16 v[176:179], v[64:67], v[124:127], v[176:179]\n\t"
      "v_cvt_pk_bf16_f32 v20, v168, v172\n\t"
      "v_cvt_pk_bf16_f32 v21, v169, v173\n\t"
      "v_mfma_f32_16x16x32_bf16 v[180:183], v[68:71], v[124:127], v[180:183]\n\t"
      "ds_read_b128 v[124:127], v7 offset:0\n\t"
      "v_cvt_pk_bf16_f32 v22, v170, v174\n\t"
      "v_mfma_f32_16x16x32_bf16 v[180:183], v[60:63], v[28:31], v[180:183]\n\t"
      "ds_read_b128 v[28:31], v6 offset:2048\n\t"
      "v_mfma_f32_16x16x32_bf16 v[188:191], v[60:63], v[36:39], v[188:191]\n\t"
      "ds_write_b64 v2, v[20:21] offset:12288\n\t"
      "v_mfma_f32_16x16x32_bf16 v[188:191], v[68:71], v[116:119], v[188:191]\n\t"
      "v_cvt_pk_bf16_f32 v23, v171, v175\n\t"
      "v_mfma_f32_16x16x32_bf16 v[184:187], v[64:67], v[116:119], v[184:187]\n\t"
      "ds_read_b128 v[116:119], v7 offset:2048\n\t"
      "v_mfma_f32_16x16x32_bf16 v[184:187], v[56:59], v[36:39], v[184:187]\n\t"
      "ds_read_b128 v[36:39], v6 offset:4096\n\t"
      "v_mfma_f32_16x16x32_bf16 v[176:179], v[72:75], v[128:131], v[176:179]\n\t"
      "ds_write_b64 v2, v[22:23] offset:12296\n\t"
      "v_mfma_f32_16x16x32_bf16 v[176:179], v[80:83], v[140:143], v[176:179]\n\t"
      "v_mfma_f32_16x16x32_bf16 v[180:183], v[84:87], v[140:143], v[180:183]\n\t"
      "ds_read_b128 v[140:143], v7 offset:4096\n\t"
      "v_mfma_f32_16x16x32_bf16 v[180:183], v[76:79], v[128:131], v[180:183]\n\t"
      "ds_read_b128 v[128:131], v6 offset:6144\n\t"
      "v_mfma_f32_16x16x32_bf16 v[188:191], v[76:79], v[120:123], v[188:191]\n\t"
      "v_mfma_f32_16x16x32_bf16 v[188:191], v[84:87], v[132:135], v[188:191]\n\t"
      "v_mfma_f32_16x16x32_bf16 v[184:187], v[80:83], v[132:135], v[184:187]\n\t"
      "ds_read_b128 v[132:135], v7 offset:6144\n\t"
      "v_mfma_f32_16x16x32_bf16 v[184:187], v[72:75], v[120:123], v[184:187]\n\t"
      "ds_read_b128 v[120:123], v6 offset:8192\n\t"
      "v_mfma_f32_16x16x32_bf16 v[176:179], v[88:91], v[112:115], v[176:179]\n\t"
      "s_waitcnt lgkmcnt(4)\n\t"
      "s_barrier\n\t"
      "v_mfma_f32_16x16x32_bf16 v[176:179], v[96:99], v[24:27], v[176:179]\n\t"
      "v_mfma_f32_16x16x32_bf16 v[180:183], v[100:103], v[24:27], v[180:183]\n\t"
      "ds_read_b128 v[24:27], v7 offset:8192\n\t"
      "v_mfma_f32_16x16x32_bf16 v[180:183], v[92:95], v[112:115], v[180:183]\n\t"
      "ds_read_b128 v[112:115], v6 offset:10240\n\t"
      "v_mfma_f32_16x16x32_bf16 v[188:191], v[92:95], v[108:111], v[188:191]\n\t"
      "v_mfma_f32_16x16x32_bf16 v[188:191], v[100:103], v[32:35], v[188:191]\n\t"
      "v_mfma_f32_16x16x32_bf16 v[184:187], v[96:99], v[32:35], v[184:187]\n\t"
      "ds_read_b128 v[32:35], v7 offset:10240\n\t"
      "v_mfma_f32_16x16x32_bf16 v[184:187], v[88:91], v[108:111], v[184:187]\n\t"
      "ds_read_b128 v[108:111], v6 offset:12288\n\t"
      "v_mfma_f32_16x16x32_bf16 v[192:195], v[40:43], v[136:139], v[192:195]\n\t"
      "v_mfma_f32_16x16x32_bf16 v[192:195], v[48:51], v[124:127], v[192:195]\n\t"
      "v_mfma_f32_16x16x32_bf16 v[196:199], v[52:55], v[124:127], v[196:199]\n\t"
      "ds_read_b128 v[124:127], v7 offset:12288\n\t"
      "v_cvt_pk_bf16_f32 v16, v176, v180\n\t"
      "v_mfma_f32_16x16x32_bf16 v[196:199], v[44:47], v[136:139], v[196:199]\n\t"
      "ds_read_b128 v[136:139], v6 offset:14336\n\t"
      "v_cvt_pk_bf16_f32 v17, v177, v181\n\t"
      "v_mfma_f32_16x16x32_bf16 v[204:207], v[44:47], v[28:31], v[204:207]\n\t"
      "v_cvt_pk_bf16_f32 v18, v178, v182\n\t"
      "v_mfma_f32_16x16x32_bf16 v[204:207], v[52:55], v[116:119], v[204:207]\n\t"
      "ds_write_b64 v12, v[16:17] offset:0\n\t"
      "v_mfma_f32_16x16x32_bf16 v[200:203], v[48:51], v[116:119], v[200:203]\n\t"
      "ds_read_b128 v[116:119], v7 offset:14336\n\t"
      "v_cvt_pk_bf16_f32 v19, v179, v183\n\t"
      "v_mfma_f32_16x16x32_bf16 v[200:203], v[40:43], v[28:31], v[200:203]\n\t"
      "ds_read_b128 v[28:31], v6 offset:16384\n\t"
      "v_mfma_f32_16x16x32_bf16 v[192:195], v[56:59], v[36:39], v[192:195]\n\t"
      "ds_write_b64 v12, v[18:19] offset:8\n\t"
      "s_waitcnt lgkmcnt(5)\n\t"
      "v_mfma_f32_16x16x32_bf16 v[192:195], v[64:67], v[140:143], v[192:195]\n\t"
      "v_cvt_pk_bf16_f32 v20, v184, v188\n\t"
      "v_cvt_pk_bf16_f32 v21, v185, v189\n\t"
      "v_mfma_f32_16x16x32_bf16 v[196:199], v[68:71], v[140:143], v[196:199]\n\t"
      "ds_read_b128 v[140:143], v7 offset:16384\n\t"
      "v_cvt_pk_bf16_f32 v22, v186, v190\n\t"
      "v_mfma_f32_16x16x32_bf16 v[196:199], v[60:63], v[36:39], v[196:199]\n\t"
      "ds_read_b128 v[36:39], v6 offset:18432\n\t"
      "v_mfma_f32_16x16x32_bf16 v[204:207], v[60:63], v[128:131], v[204:207]\n\t"
      "ds_write_b64 v2, v[20:21] offset:16384\n\t"
      "v_mfma_f32_16x16x32_bf16 v[204:207], v[68:71], v[132:135], v[204:207]\n\t"
      "v_cvt_pk_bf16_f32 v23, v187, v191\n\t"
      "v_mfma_f32_16x16x32_bf16 v[200:203], v[64:67], v[132:135], v[200:203]\n\t"
      "ds_read_b128 v[132:135], v7 offset:18432\n\t"
      "v_mfma_f32_16x16x32_bf16 v[200:203], v[56:59], v[128:131], v[200:203]\n\t"
      "ds_read_b128 v[128:131], v6 offset:20480\n\t"
      "v_mfma_f32_16x16x32_bf16 v[192:195], v[72:75], v[120:123], v[192:195]\n\t"
      "ds_write_b64 v2, v[22:23] offset:16392\n\t"
      "v_mfma_f32_16x16x32_bf16 v[192:195], v[80:83], v[24:27], v[192:195]\n\t"
      "v_mfma_f32_16x16x32_bf16 v[196:199], v[84:87], v[24:27], v[196:199]\n\t"
      "ds_read_b128 v[24:27], v7 offset:20480\n\t"
      "v_mfma_f32_16x16x32_bf16 v[196:199], v[76:79], v[120:123], v[196:199]\n\t"
      "ds_read_b128 v[120:123], v6 offset:22528\n\t"
      "v_mfma_f32_16x16x32_bf16 v[204:207], v[76:79], v[112:115], v[204:207]\n\t"
      "v_mfma_f32_16x16x32_bf16 v[204:207], v[84:87], v[32:35], v[204:207]\n\t"
      "v_mfma_f32_16x16x32_bf16 v[200:203], v[80:83], v[32:35], v[200:203]\n\t"
      "ds_read_b128 v[32:35], v7 offset:22528\n\t"
      "v_mfma_f32_16x16x32_bf16 v[200:203], v[72:75], v[112:115], v[200:203]\n\t"
      "ds_read_b128 v[112:115], v6 offset:24576\n\t"
      "v_mfma_f32_16x16x32_bf16 v[192:195], v[88:91], v[108:111], v[192:195]\n\t"
      "s_waitcnt lgkmcnt(4)\n\t"
      "s_barrier\n\t"
      "v_mfma_f32_16x16x32_bf16 v[192:195], v[96:99], v[124:127], v[192:195]\n\t"
      "v_mfma_f32_16x16x32_bf16 v[196:199], v[100:103], v[124:127], v[196:199]\n\t"
      "ds_read_b128 v[124:127], v7 offset:24576\n\t"
      "v_mfma_f32_16x16x32_bf16 v[196:199], v[92:95], v[108:111], v[196:199]\n\t"
      "ds_read_b128 v[108:111], v6 offset:26624\n\t"
      "v_mfma_f32_16x16x32_bf16 v[204:207], v[92:95], v[136:139], v[204:207]\n\t"
      "v_mfma_f32_16x16x32_bf16 v[204:207], v[100:103], v[116:119], v[204:207]\n\t"
      "v_mfma_f32_16x16x32_bf16 v[200:203], v[96:99], v[116:119], v[200:203]\n\t"
      "ds_read_b128 v[116:119], v7 offset:26624\n\t"
      "v_mfma_f32_16x16x32_bf16 v[200:203], v[88:91], v[136:139], v[200:203]\n\t"
      "ds_read_b128 v[136:139], v6 offset:28672\n\t"
      "v_mfma_f32_16x16x32_bf16 v[208:211], v[40:43], v[28:31], v[208:211]\n\t"
      "v_mfma_f32_16x16x32_bf16 v[208:211], v[48:51], v[140:143], v[208:211]\n\t"
      "v_mfma_f32_16x16x32_bf16 v[212:215], v[52:55], v[140:143], v[212:215]\n\t"
      "ds_read_b128 v[140:143], v7 offset:28672\n\t"
      "v_cvt_pk_bf16_f32 v16, v192, v196\n\t"
      "v_mfma_f32_16x16x32_bf16 v[212:215], v[44:47], v[28:31], v[212:215]\n\t"
      "ds_read_b128 v[28:31], v6 offset:30720\n\t"
      "v_cvt_pk_bf16_f32 v17, v193, v197\n\t"
      "v_mfma_f32_16x16x32_bf16 v[220:223], v[44:47], v[36:39], v[220:223]\n\t"
      "v_cvt_pk_bf16_f32 v18, v194, v198\n\t"
      "v_mfma_f32_16x16x32_bf16 v[220:223], v[52:55], v[132:135], v[220:223]\n\t"
      "ds_write_b64 v12, v[16:17] offset:4096\n\t"
      "v_mfma_f32_16x16x32_bf16 v[216:219], v[48:51], v[132:135], v[216:219]\n\t"
      "ds_read_b128 v[132:135], v7 offset:30720\n\t"
      "v_cvt_pk_bf16_f32 v19, v195, v199\n\t"
      "v_mfma_f32_16x16x32_bf16 v[216:219], v[40:43], v[36:39], v[216:219]\n\t"
      "ds_read_b128 v[36:39], v6 offset:32768\n\t"
      "v_mfma_f32_16x16x32_bf16 v[208:211], v[56:59], v[128:131], v[208:211]\n\t"
      "ds_write_b64 v12, v[18:19] offset:4104\n\t"
      "s_waitcnt lgkmcnt(5)\n\t"
      "v_mfma_f32_16x16x32_bf16 v[208:211], v[64:67], v[24:27], v[208:211]\n\t"
      "v_cvt_pk_bf16_f32 v20, v200, v204\n\t"
      "v_cvt_pk_bf16_f32 v21, v201, v205\n\t"
      "v_mfma_f32_16x16x32_bf16 v[212:215], v[68:71], v[24:27], v[212:215]\n\t"
      "ds_read_b128 v[24:27], v7 offset:32768\n\t"
      "v_cvt_pk_bf16_f32 v22, v202, v206\n\t"
      "v_mfma_f32_16x16x32_bf16 v[212:215], v[60:63], v[128:131], v[212:215]\n\t"
      "ds_read_b128 v[128:131], v6 offset:34816\n\t"
      "v_mfma_f32_16x16x32_bf16 v[220:223], v[60:63], v[120:123], v[220:223]\n\t"
      "ds_write_b64 v12, v[20:21] offset:8192\n\t"
      "v_mfma_f32_16x16x32_bf16 v[220:223], v[68:71], v[32:35], v[220:223]\n\t"
      "v_cvt_pk_bf16_f32 v23, v203, v207\n\t"
      "v_mfma_f32_16x16x32_bf16 v[216:219], v[64:67], v[32:35], v[216:219]\n\t"
      "ds_read_b128 v[32:35], v7 offset:34816\n\t"
      "v_mfma_f32_16x16x32_bf16 v[216:219], v[56:59], v[120:123], v[216:219]\n\t"
      "ds_read_b128 v[120:123], v6 offset:36864\n\t"
      "v_mfma_f32_16x16x32_bf16 v[208:211], v[72:75], v[112:115], v[208:211]\n\t"
      "ds_write_b64 v12, v[22:23] offset:8200\n\t"
      "v_mfma_f32_16x16x32_bf16 v[208:211], v[80:83], v[124:127], v[208:211]\n\t"
      "v_mfma_f32_16x16x32_bf16 v[212:215], v[84:87], v[124:127], v[212:215]\n\t"
      "ds_read_b128 v[124:127], v7 offset:36864\n\t"
      "v_mfma_f32_16x16x32_bf16 v[212:215], v[76:79], v[112:115], v[212:215]\n\t"
      "ds_read_b128 v[112:115], v6 offset:38912\n\t"
      "v_mfma_f32_16x16x32_bf16 v[220:223], v[76:79], v[108:111], v[220:223]\n\t"
      "v_mfma_f32_16x16x32_bf16 v[220:223], v[84:87], v[116:119], v[220:223]\n\t"
      "v_mfma_f32_16x16x32_bf16 v[216:219], v[80:83], v[116:119], v[216:219]\n\t"
      "ds_read_b128 v[116:119], v7 offset:38912\n\t"
      "v_mfma_f32_16x16x32_bf16 v[216:219], v[72:75], v[108:111], v[216:219]\n\t"
      "ds_read_b128 v[108:111], v6 offset:40960\n\t"
      "v_mfma_f32_16x16x32_bf16 v[208:211], v[88:91], v[136:139], v[208:211]\n\t"
      "s_waitcnt lgkmcnt(4)\n\t"
      "s_barrier\n\t"
      "v_mfma_f32_16x16x32_bf16 v[208:211], v[96:99], v[140:143], v[208:211]\n\t"
      "v_mfma_f32_16x16x32_bf16 v[212:215], v[100:103], v[140:143], v[212:215]\n\t"
      "ds_read_b128 v[140:143], v7 offset:40960\n\t"
      "v_mfma_f32_16x16x32_bf16 v[212:215], v[92:95], v[136:139], v[212:215]\n\t"
      "ds_read_b128 v[136:139], v6 offset:43008\n\t"
      "v_mfma_f32_16x16x32_bf16 v[220:223], v[92:95], v[28:31], v[220:223]\n\t"
      "v_mfma_f32_16x16x32_bf16 v[220:223], v[100:103], v[132:135], v[220:223]\n\t"
      "v_mfma_f32_16x16x32_bf16 v[216:219], v[96:99], v[132:135], v[216:219]\n\t"
      "ds_read_b128 v[132:135], v7 offset:43008\n\t"
      "v_mfma_f32_16x16x32_bf16 v[216:219], v[88:91], v[28:31], v[216:219]\n\t"
      "ds_read_b128 v[28:31], v6 offset:45056\n\t"
      "v_mfma_f32_16x16x32_bf16 v[224:227], v[40:43], v[36:39], v[224:227]\n\t"
      "v_mfma_f32_16x16x32_bf16 v[224:227], v[48:51], v[24:27], v[224:227]\n\t"
      "v_mfma_f32_16x16x32_bf16 v[228:231], v[52:55], v[24:27], v[228:231]\n\t"
      "ds_read_b128 v[24:27], v7 offset:45056\n\t"
      "v_cvt_pk_bf16_f32 v16, v208, v212\n\t"
      "v_mfma_f32_16x16x32_bf16 v[228:231], v[44:47], v[36:39], v[228:231]\n\t"
      "ds_read_b128 v[36:39], v6 offset:47104\n\t"
      "v_cvt_pk_bf16_f32 v17, v209, v213\n\t"
      "v_mfma_f32_16x16x32_bf16 v[236:239], v[44:47], v[128:131], v[236:239]\n\t"
      "v_cvt_pk_bf16_f32 v18, v210, v214\n\t"
      "v_mfma_f32_16x16x32_bf16 v[236:239], v[52:55], v[32:35], v[236:239]\n\t"
      "ds_write_b64 v12, v[16:17] offset:12288\n\t"
      "v_mfma_f32_16x16x32_bf16 v[232:235], v[48:51], v[32:35], v[232:235]\n\t"
      "ds_read_b128 v[32:35], v7 offset:47104\n\t"
      "v_cvt_pk_bf16_f32 v19, v211, v215\n\t"
      "v_mfma_f32_16x16x32_bf16 v[232:235], v[40:43], v[128:131], v[232:235]\n\t"
      "ds_read_b128 v[128:131], v6 offset:49152\n\t"
      "v_mfma_f32_16x16x32_bf16 v[224:227], v[56:59], v[120:123], v[224:227]\n\t"
      "ds_write_b64 v12, v[18:19] offset:12296\n\t"
      "s_waitcnt lgkmcnt(5)\n\t"
      "v_mfma_f32_16x16x32_bf16 v[224:227], v[64:67], v[124:127], v[224:227]\n\t"
      "v_cvt_pk_bf16_f32 v20, v216, v220\n\t"
      "v_cvt_pk_bf16_f32 v21, v217, v221\n\t"
      "v_mfma_f32_16x16x32_bf16 v[228:231], v[68:71], v[124:127], v[228:231]\n\t"
      "ds_read_b128 v[124:127], v7 offset:49152\n\t"
      "v_cvt_pk_bf16_f32 v22, v218, v222\n\t"
      "v_mfma_f32_16x16x32_bf16 v[228:231], v[60:63], v[120:123], v[228:231]\n\t"
      "ds_read_b128 v[120:123], v6 offset:51200\n\t"
      "v_mfma_f32_16x16x32_bf16 v[236:239], v[60:63], v[112:115], v[236:239]\n\t"
      "ds_write_b64 v12, v[20:21] offset:16384\n\t"
      "v_mfma_f32_16x16x32_bf16 v[236:239], v[68:71], v[116:119], v[236:239]\n\t"
      "v_cvt_pk_bf16_f32 v23, v219, v223\n\t"
      "v_mfma_f32_16x16x32_bf16 v[232:235], v[64:67], v[116:119], v[232:235]\n\t"
      "ds_read_b128 v[116:119], v7 offset:51200\n\t"
      "v_mfma_f32_16x16x32_bf16 v[232:235], v[56:59], v[112:115], v[232:235]\n\t"
      "ds_read_b128 v[112:115], v6 offset:53248\n\t"
      "v_mfma_f32_16x16x32_bf16 v[224:227], v[72:75], v[108:111], v[224:227]\n\t"
      "ds_write_b64 v12, v[22:23] offset:16392\n\t"
      "v_mfma_f32_16x16x32_bf16 v[224:227], v[80:83], v[140:143], v[224:227]\n\t"
      "v_mfma_f32_16x16x32_bf16 v[228:231], v[84:87], v[140:143], v[228:231]\n\t"
      "ds_read_b128 v[140:143], v7 offset:53248\n\t"
      "v_mfma_f32_16x16x32_bf16 v[228:231], v[76:79], v[108:111], v[228:231]\n\t"
      "ds_read_b128 v[108:111], v6 offset:55296\n\t"
      "v_mfma_f32_16x16x32_bf16 v[236:239], v[76:79], v[136:139], v[236:239]\n\t"
      "v_mfma_f32_16x16x32_bf16 v[236:239], v[84:87], v[132:135], v[236:239]\n\t"
      "v_mfma_f32_16x16x32_bf16 v[232:235], v[80:83], v[132:135], v[232:235]\n\t"
      "ds_read_b128 v[132:135], v7 offset:55296\n\t"
      "v_mfma_f32_16x16x32_bf16 v[232:235], v[72:75], v[136:139], v[232:235]\n\t"
      "ds_read_b128 v[136:139], v6 offset:57344\n\t"
      "v_mfma_f32_16x16x32_bf16 v[224:227], v[88:91], v[28:31], v[224:227]\n\t"
      "s_waitcnt lgkmcnt(4)\n\t"
      "s_barrier\n\t"
      "v_mfma_f32_16x16x32_bf16 v[224:227], v[96:99], v[24:27], v[224:227]\n\t"
      "v_mfma_f32_16x16x32_bf16 v[228:231], v[100:103], v[24:27], v[228:231]\n\t"
      "ds_read_b128 v[24:27], v7 offset:57344\n\t"
      "v_mfma_f32_16x16x32_bf16 v[228:231], v[92:95], v[28:31], v[228:231]\n\t"
      "ds_read_b128 v[28:31], v6 offset:59392\n\t"
      "v_mfma_f32_16x16x32_bf16 v[236:239], v[92:95], v[36:39], v[236:239]\n\t"
      "v_mfma_f32_16x16x32_bf16 v[236:239], v[100:103], v[32:35], v[236:239]\n\t"
      "v_mfma_f32_16x16x32_bf16 v[232:235], v[96:99], v[32:35], v[232:235]\n\t"
      "ds_read_b128 v[32:35], v7 offset:59392\n\t"
      "v_mfma_f32_16x16x32_bf16 v[232:235], v[88:91], v[36:39], v[232:235]\n\t"
      "ds_read_b128 v[36:39], v6 offset:61440\n\t"
      "v_mfma_f32_16x16x32_bf16 v[240:243], v[40:43], v[128:131], v[240:243]\n\t"
      "v_mfma_f32_16x16x32_bf16 v[240:243], v[48:51], v[124:127], v[240:243]\n\t"
      "v_mfma_f32_16x16x32_bf16 v[244:247], v[52:55], v[124:127], v[244:247]\n\t"
      "ds_read_b128 v[124:127], v7 offset:61440\n\t"
      "v_cvt_pk_bf16_f32 v16, v224, v228\n\t"
      "v_mfma_f32_16x16x32_bf16 v[244:247], v[44:47], v[128:131], v[244:247]\n\t"
      "ds_read_b128 v[128:131], v6 offset:63488\n\t"
      "v_cvt_pk_bf16_f32 v17, v225, v229\n\t"
      "v_mfma_f32_16x16x32_bf16 v[252:255], v[44:47], v[120:123], v[252:255]\n\t"
      "v_cvt_pk_bf16_f32 v18, v226, v230\n\t"
      "v_mfma_f32_16x16x32_bf16 v[252:255], v[52:55], v[116:119], v[252:255]\n\t"
      "ds_write_b64 v12, v[16:17] offset:20480\n\t"
      "v_mfma_f32_16x16x32_bf16 v[248:251], v[48:51], v[116:119], v[248:251]\n\t"
      "ds_read_b128 v[116:119], v7 offset:63488\n\t"
      "v_cvt_pk_bf16_f32 v19, v227, v231\n\t"
      "v_mfma_f32_16x16x32_bf16 v[248:251], v[40:43], v[120:123], v[248:251]\n\t"
      "ds_write_b64 v12, v[18:19] offset:20488\n\t"
      "v_mfma_f32_16x16x32_bf16 v[240:243], v[56:59], v[112:115], v[240:243]\n\t"
      "v_cvt_pk_bf16_f32 v20, v232, v236\n\t"
      "s_waitcnt lgkmcnt(4)\n\t"
      "v_mfma_f32_16x16x32_bf16 v[240:243], v[64:67], v[140:143], v[240:243]\n\t"
      "v_cvt_pk_bf16_f32 v21, v233, v237\n\t"
      "v_cvt_pk_bf16_f32 v22, v234, v238\n\t"
      "v_mfma_f32_16x16x32_bf16 v[244:247], v[68:71], v[140:143], v[244:247]\n\t"
      "ds_write_b64 v12, v[20:21] offset:24576\n\t"
      "v_mfma_f32_16x16x32_bf16 v[244:247], v[60:63], v[112:115], v[244:247]\n\t"
      "v_cvt_pk_bf16_f32 v23, v235, v239\n\t"
      "v_mfma_f32_16x16x32_bf16 v[252:255], v[60:63], v[108:111], v[252:255]\n\t"
      "ds_write_b64 v12, v[22:23] offset:24584\n\t"
      "v_mfma_f32_16x16x32_bf16 v[252:255], v[68:71], v[132:135], v[252:255]\n\t"
      "v_mfma_f32_16x16x32_bf16 v[248:251], v[64:67], v[132:135], v[248:251]\n\t"
      "v_mfma_f32_16x16x32_bf16 v[248:251], v[56:59], v[108:111], v[248:251]\n\t"
      "v_mfma_f32_16x16x32_bf16 v[240:243], v[72:75], v[136:139], v[240:243]\n\t"
      "v_mfma_f32_16x16x32_bf16 v[240:243], v[80:83], v[24:27], v[240:243]\n\t"
      "v_mfma_f32_16x16x32_bf16 v[244:247], v[84:87], v[24:27], v[244:247]\n\t"
      "s_waitcnt lgkmcnt(0)\n\t"
      "s_barrier\n\t"
      "v_mfma_f32_16x16x32_bf16 v[244:247], v[76:79], v[136:139], v[244:247]\n\t"
      "v_mfma_f32_16x16x32_bf16 v[252:255], v[76:79], v[28:31], v[252:255]\n\t"
      "v_mfma_f32_16x16x32_bf16 v[252:255], v[84:87], v[32:35], v[252:255]\n\t"
      "v_mfma_f32_16x16x32_bf16 v[248:251], v[80:83], v[32:35], v[248:251]\n\t"
      "v_mfma_f32_16x16x32_bf16 v[248:251], v[72:75], v[28:31], v[248:251]\n\t"
      "v_mfma_f32_16x16x32_bf16 v[240:243], v[88:91], v[36:39], v[240:243]\n\t"
      "v_mfma_f32_16x16x32_bf16 v[240:243], v[96:99], v[124:127], v[240:243]\n\t"
      "v_mfma_f32_16x16x32_bf16 v[244:247], v[100:103], v[124:127], v[244:247]\n\t"
      "v_mfma_f32_16x16x32_bf16 v[244:247], v[92:95], v[36:39], v[244:247]\n\t"
      "v_mfma_f32_16x16x32_bf16 v[252:255], v[92:95], v[128:131], v[252:255]\n\t"
      "v_mfma_f32_16x16x32_bf16 v[252:255], v[100:103], v[116:119], v[252:255]\n\t"
      "v_mfma_f32_16x16x32_bf16 v[248:251], v[96:99], v[116:119], v[248:251]\n\t"
      "v_mfma_f32_16x16x32_bf16 v[248:251], v[88:91], v[128:131], v[248:251]\n\t"
      "s_nop 3\n\t"
      "v_cvt_pk_bf16_f32 v16, v240, v244\n\t"
      "v_cvt_pk_bf16_f32 v17, v241, v245\n\t"
      "v_cvt_pk_bf16_f32 v18, v242, v246\n\t"
      "ds_write_b64 v12, v[16:17] offset:28672\n\t"
      "v_cvt_pk_bf16_f32 v19, v243, v247\n\t"
      "ds_write_b64 v12, v[18:19] offset:28680\n\t"
      "v_cvt_pk_bf16_f32 v40, v248, v252\n\t"
      "v_cvt_pk_bf16_f32 v41, v249, v253\n\t"
      "v_cvt_pk_bf16_f32 v42, v250, v254\n\t"
      "v_cvt_pk_bf16_f32 v43, v251, v255\n\t"
      "ds_write_b128 v12, v[40:43] offset:32768\n\t"
      "s_waitcnt lgkmcnt(0)\n\t"
      "s_barrier\n\t"
      "s_endpgm\n\t"
      ".p2align 12\n\t"
      "LC15_%=:\n\t"
      "LH15x0_%=:\n\t"
      "s_cbranch_execz LH15x4_%=\n\t"
      "s_barrier\n\t"
      "ds_read_b128 v[132:135], v6 offset:0\n\t"
      "ds_read_b128 v[128:131], v7 offset:0\n\t"
      "ds_read_b128 v[124:127], v6 offset:2048\n\t"
      "ds_read_b128 v[120:123], v7 offset:2048\n\t"
      "ds_read_b128 v[116:119], v6 offset:4096\n\t"
      "ds_read_b128 v[112:115], v7 offset:4096\n\t"
      "s_waitcnt lgkmcnt(5)\n\t"
      "v_mfma_f32_16x16x32_bf16 v[136:139], v[40:43], v[132:135], 0\n\t"
      "ds_read_b128 v[108:111], v6 offset:6144\n\t"
      "s_waitcnt lgkmcnt(5)\n\t"
      "v_mfma_f32_16x16x32_bf16 v[136:139], v[48:51], v[128:131], v[136:139]\n\t"
      "ds_read_b128 v[104:107], v7 offset:6144\n\t"
      "v_lshlrev_b32 v28, 16, v76\n\t"
      "v_and_b32 v29, 0xffff0000, v76\n\t"
      "v_mfma_f32_16x16x32_bf16 v[140:143], v[52:55], v[128:131], 0\n\t"
      "v_lshlrev_b32 v30, 16, v77\n\t"
      "v_and_b32 v31, 0xffff0000, v77\n\t"
      "v_mfma_f32_16x16x32_bf16 v[140:143], v[44:47], v[132:135], v[140:143]\n\t"
      "v_mul_f32 v20, 0x4b800000, v28\n\t"
      "v_mul_f32 v24, 0x49800000, v28\n\t"
      "s_waitcnt lgkmcnt(5)\n\t"
      "v_mfma_f32_16x16x32_bf16 v[148:151], v[44:47], v[124:127], 0\n\t"
      "v_mul_f32 v22, v28, v30\n\t"
      "s_waitcnt lgkmcnt(4)\n\t"
      "v_mfma_f32_16x16x32_bf16 v[148:151], v[52:55], v[120:123], v[148:151]\n\t"
      "s_barrier\n\t"
      "v_mfma_f32_16x16x32_bf16 v[144:147], v[48:51], v[120:123], 0\n\t"
      "ds_read_b128 v[100:103], v6 offset:8192\n\t"
      "v_mul_f32 v21, 0x4b800000, v29\n\t"
      "v_mfma_f32_16x16x32_bf16 v[144:147], v[40:43], v[124:127], v[144:147]\n\t"
      "ds_read_b128 v[96:99], v7 offset:8192\n\t"
      "v_mul_f32 v25, 0x49800000, v29\n\t"
      "s_waitcnt lgkmcnt(2)\n\t"
      "v_mfma_f32_16x16x32_bf16 v[152:155], v[40:43], v[116:119], 0\n\t"
      "ds_read_b128 v[92:95], v6 offset:10240\n\t"
      "v_mul_f32 v23, v29, v31\n\t"
      "v_mfma_f32_16x16x32_bf16 v[152:155], v[48:51], v[112:115], v[152:155]\n\t"
      "ds_read_b128 v[88:91], v7 offset:10240\n\t"
      "v_lshrrev_b32 v31, 8, v72\n\t"
      "v_and_b32 v28, v13, v72\n\t"
      "v_mfma_f32_16x16x32_bf16 v[156:159], v[52:55], v[112:115], 0\n\t"
      "ds_read_b128 v[84:87], v6 offset:12288\n\t"
      "v_and_b32 v29, v14, v72\n\t"
      "v_mfma_f32_16x16x32_bf16 v[156:159], v[44:47], v[116:119], v[156:159]\n\t"
      "v_and_b32 v30, v13, v31\n\t"
      "v_and_b32 v31, v14, v31\n\t"
      "v_fma_mix_f32 v32, v28, v20, -v22 op_sel:[0,0,0] op_sel_hi:[1,0,0]\n\t"
      "v_mfma_f32_16x16x32_bf16 v[164:167], v[44:47], v[108:111], 0\n\t"
      "ds_read_b128 v[80:83], v7 offset:12288\n\t"
      "v_fma_mix_f32 v33, v29, v24, -v22 op_sel:[0,0,0] op_sel_hi:[1,0,0]\n\t"
      "v_mfma_f32_16x16x32_bf16 v[164:167], v[52:55], v[104:107], v[164:167]\n\t"
      "v_fma_mix_f32 v34, v30, v20, -v22 op_sel:[0,0,0] op_sel_hi:[1,0,0]\n\t"
      "v_fma_mix_f32 v35, v31, v24, -v22 op_sel:[0,0,0] op_sel_hi:[1,0,0]\n\t"
      "v_fma_mix_f32 v36, v28, v20, -v22 op_sel:[1,0,0] op_sel_hi:[1,0,0]\n\t"
      "v_mfma_f32_16x16x32_bf16 v[160:163], v[48:51], v[104:107], 0\n\t"
      "ds_read_b128 v[128:131], v6 offset:14336\n\t"
      "v_fma_mix_f32 v37, v29, v24, -v22 op_sel:[1,0,0] op_sel_hi:[1,0,0]\n\t"
      "v_mfma_f32_16x16x32_bf16 v[160:163], v[40:43], v[108:111], v[160:163]\n\t"
      "v_cvt_pk_bf16_f32 v56, v32, v33\n\t"
      "v_cvt_pk_bf16_f32 v57, v34, v35\n\t"
      "s_waitcnt lgkmcnt(3)\n\t"
      "v_mfma_f32_16x16x32_bf16 v[168:171], v[40:43], v[100:103], 0\n\t"
      "ds_read_b128 v[132:135], v7 offset:14336\n\t"
      "v_fma_mix_f32 v38, v30, v20, -v22 op_sel:[1,0,0] op_sel_hi:[1,0,0]\n\t"
      "v_mfma_f32_16x16x32_bf16 v[168:171], v[48:51], v[96:99], v[168:171]\n\t"
      "s_barrier\n\t"
      "v_mfma_f32_16x16x32_bf16 v[172:175], v[52:55], v[96:99], 0\n\t"
      "ds_read_b128 v[120:123], v6 offset:16384\n\t"
      "v_fma_mix_f32 v39, v31, v24, -v22 op_sel:[1,0,0] op_sel_hi:[1,0,0]\n\t"
      "v_mfma_f32_16x16x32_bf16 v[172:175], v[44:47], v[100:103], v[172:175]\n\t"
      "ds_read_b128 v[124:127], v7 offset:16384\n\t"
      "v_cvt_pk_bf16_f32 v58, v36, v37\n\t"
      "v_cvt_pk_bf16_f32 v59, v38, v39\n\t"
      "v_mfma_f32_16x16x32_bf16 v[180:183], v[44:47], v[92:95], 0\n\t"
      "ds_read_b128 v[112:115], v6 offset:18432\n\t"
      "v_lshrrev_b32 v31, 8, v73\n\t"
      "v_mfma_f32_16x16x32_bf16 v[180:183], v[52:55], v[88:91], v[180:183]\n\t"
      "v_and_b32 v28, v13, v73\n\t"
      "v_and_b32 v29, v14, v73\n\t"
      "v_and_b32 v30, v13, v31\n\t"
      "v_mfma_f32_16x16x32_bf16 v[176:179], v[48:51], v[88:91], 0\n\t"
      "ds_read_b128 v[116:119], v7 offset:18432\n\t"
      "v_and_b32 v31, v14, v31\n\t"
      "v_mfma_f32_16x16x32_bf16 v[176:179], v[40:43], v[92:95], v[176:179]\n\t"
      "v_fma_mix_f32 v32, v28, v21, -v23 op_sel:[0,0,0] op_sel_hi:[1,0,0]\n\t"
      "v_fma_mix_f32 v33, v29, v25, -v23 op_sel:[0,0,0] op_sel_hi:[1,0,0]\n\t"
      "s_waitcnt lgkmcnt(4)\n\t"
      "v_mfma_f32_16x16x32_bf16 v[184:187], v[40:43], v[84:87], 0\n\t"
      "ds_read_b128 v[104:107], v6 offset:20480\n\t"
      "v_fma_mix_f32 v34, v30, v21, -v23 op_sel:[0,0,0] op_sel_hi:[1,0,0]\n\t"
      "v_mfma_f32_16x16x32_bf16 v[184:187], v[48:51], v[80:83], v[184:187]\n\t"
      "v_fma_mix_f32 v35, v31, v25, -v23 op_sel:[0,0,0] op_sel_hi:[1,0,0]\n\t"
      "v_fma_mix_f32 v36, v28, v21, -v23 op_sel:[1,0,0] op_sel_hi:[1,0,0]\n\t"
      "v_fma_mix_f32 v37, v29, v25, -v23 op_sel:[1,0,0] op_sel_hi:[1,0,0]\n\t"
      "v_mfma_f32_16x16x32_bf16 v[188:191], v[52:55], v[80:83], 0\n\t"
      "ds_read_b128 v[108:111], v7 offset:20480\n\t"
      "v_cvt_pk_bf16_f32 v60, v32, v33\n\t"
      "v_mfma_f32_16x16x32_bf16 v[188:191], v[44:47], v[84:87], v[188:191]\n\t"
      "v_cvt_pk_bf16_f32 v61, v34, v35\n\t"
      "v_fma_mix_f32 v38, v30, v21, -v23 op_sel:[1,0,0] op_sel_hi:[1,0,0]\n\t"
      "v_mfma_f32_16x16x32_bf16 v[196:199], v[44:47], v[128:131], 0\n\t"
      "ds_read_b128 v[96:99], v6 offset:22528\n\t"
      "v_fma_mix_f32 v39, v31, v25, -v23 op_sel:[1,0,0] op_sel_hi:[1,0,0]\n\t"
      "v_mfma_f32_16x16x32_bf16 v[196:199], v[52:55], v[132:135], v[196:199]\n\t"
      "v_cvt_pk_bf16_f32 v62, v36, v37\n\t"
      "v_cvt_pk_bf16_f32 v63, v38, v39\n\t"
      "v_mfma_f32_16x16x32_bf16 v[192:195], v[48:51], v[132:135], 0\n\t"
      "ds_read_b128 v[100:103], v7 offset:22528\n\t"
      "v_lshrrev_b32 v31, 8, v74\n\t"
      "v_mfma_f32_16x16x32_bf16 v[192:195], v[40:43], v[128:131], v[192:195]\n\t"
      "v_and_b32 v28, v13, v74\n\t"
      "s_waitcnt lgkmcnt(3)\n\t"
      "v_mfma_f32_16x16x32_bf16 v[200:203], v[40:43], v[120:123], 0\n\t"
      "v_and_b32 v29, v14, v74\n\t"
      "v_and_b32 v30, v13, v31\n\t"
      "v_mfma_f32_16x16x32_bf16 v[200:203], v[48:51], v[124:127], v[200:203]\n\t"
      "s_barrier\n\t"
      "v_mfma_f32_16x16x32_bf16 v[204:207], v[52:55], v[124:127], 0\n\t"
      "ds_read_b128 v[88:91], v6 offset:24576\n\t"
      "v_and_b32 v31, v14, v31\n\t"
      "v_mfma_f32_16x16x32_bf16 v[204:207], v[44:47], v[120:123], v[204:207]\n\t"
      "ds_read_b128 v[92:95], v7 offset:24576\n\t"
      "v_fma_mix_f32 v32, v28, v20, -v22 op_sel:[0,0,0] op_sel_hi:[1,0,0]\n\t"
      "v_mfma_f32_16x16x32_bf16 v[212:215], v[44:47], v[112:115], 0\n\t"
      "ds_read_b128 v[80:83], v6 offset:26624\n\t"
      "ds_read_b64 v[16:17], v10 offset:8192\n\t"
      "v_mfma_f32_16x16x32_bf16 v[212:215], v[52:55], v[116:119], v[212:215]\n\t"
      "ds_read_b128 v[84:87], v7 offset:26624\n\t"
      "ds_read_b64 v[18:19], v10 offset:10240\n\t"
      "v_mfma_f32_16x16x32_bf16 v[208:211], v[48:51], v[116:119], 0\n\t"
      "ds_read_b128 v[132:135], v6 offset:28672\n\t"
      "ds_read2st64_b32 v[26:27], v11 offset0:2 offset1:18\n\t"
      "v_mfma_f32_16x16x32_bf16 v[208:211], v[40:43], v[112:115], v[208:211]\n\t"
      "v_fma_mix_f32 v33, v29, v24, -v22 op_sel:[0,0,0] op_sel_hi:[1,0,0]\n\t"
      "v_fma_mix_f32 v34, v30, v20, -v22 op_sel:[0,0,0] op_sel_hi:[1,0,0]\n\t"
      "v_mfma_f32_16x16x32_bf16 v[216:219], v[40:43], v[104:107], 0\n\t"
      "ds_read_b128 v[128:131], v7 offset:28672\n\t"
      "s_waitcnt lgkmcnt(8)\n\t"
      "v_mfma_f32_16x16x32_bf16 v[216:219], v[48:51], v[108:111], v[216:219]\n\t"
      "v_fma_mix_f32 v35, v31, v24, -v22 op_sel:[0,0,0] op_sel_hi:[1,0,0]\n\t"
      "v_fma_mix_f32 v36, v28, v20, -v22 op_sel:[1,0,0] op_sel_hi:[1,0,0]\n\t"
      "v_mfma_f32_16x16x32_bf16 v[220:223], v[52:55], v[108:111], 0\n\t"
      "ds_read_b128 v[124:127], v6 offset:32768\n\t"
      "v_fma_mix_f32 v37, v29, v24, -v22 op_sel:[1,0,0] op_sel_hi:[1,0,0]\n\t"
      "v_mfma_f32_16x16x32_bf16 v[220:223], v[44:47], v[104:107], v[220:223]\n\t"
      "v_cvt_pk_bf16_f32 v64, v32, v33\n\t"
      "v_cvt_pk_bf16_f32 v65, v34, v35\n\t"
      "v_mfma_f32_16x16x32_bf16 v[228:231], v[44:47], v[96:99], 0\n\t"
      "ds_read_b128 v[120:123], v7 offset:32768\n\t"
      "v_fma_mix_f32 v38, v30, v20, -v22 op_sel:[1,0,0] op_sel_hi:[1,0,0]\n\t"
      "v_mfma_f32_16x16x32_bf16 v[228:231], v[52:55], v[100:103], v[228:231]\n\t"
      "s_barrier\n\t"
      "v_mfma_f32_16x16x32_bf16 v[224:227], v[48:51], v[100:103], 0\n\t"
      "ds_read_b128 v[116:119], v6 offset:34816\n\t"
      "v_fma_mix_f32 v39, v31, v24, -v22 op_sel:[1,0,0] op_sel_hi:[1,0,0]\n\t"
      "v_mfma_f32_16x16x32_bf16 v[224:227], v[40:43], v[96:99], v[224:227]\n\t"
      "v_cvt_pk_bf16_f32 v66, v36, v37\n\t"
      "v_cvt_pk_bf16_f32 v67, v38, v39\n\t"
      "v_mfma_f32_16x16x32_bf16 v[232:235], v[40:43], v[88:91], 0\n\t"
      "ds_read_b128 v[112:115], v7 offset:34816\n\t"
      "s_waitcnt lgkmcnt(3)\n\t"
      "v_mfma_f32_16x16x32_bf16 v[232:235], v[48:51], v[92:95], v[232:235]\n\t"
      "ds_read_b128 v[108:111], v6 offset:36864\n\t"
      "v_lshrrev_b32 v31, 8, v75\n\t"
      "v_mfma_f32_16x16x32_bf16 v[236:239], v[52:55], v[92:95], 0\n\t"
      "ds_read_b128 v[104:107], v7 offset:36864\n\t"
      "v_and_b32 v28, v13, v75\n\t"
      "v_mfma_f32_16x16x32_bf16 v[236:239], v[44:47], v[88:91], v[236:239]\n\t"
      "v_and_b32 v29, v14, v75\n\t"
      "v_and_b32 v30, v13, v31\n\t"
      "v_and_b32 v31, v14, v31\n\t"
      "v_mfma_f32_16x16x32_bf16 v[244:247], v[44:47], v[80:83], 0\n\t"
      "ds_read_b128 v[100:103], v6 offset:38912\n\t"
      "v_fma_mix_f32 v32, v28, v21, -v23 op_sel:[0,0,0] op_sel_hi:[1,0,0]\n\t"
      "v_mfma_f32_16x16x32_bf16 v[244:247], v[52:55], v[84:87], v[244:247]\n\t"
      "v_fma_mix_f32 v33, v29, v25, -v23 op_sel:[0,0,0] op_sel_hi:[1,0,0]\n\t"
      "v_fma_mix_f32 v34, v30, v21, -v23 op_sel:[0,0,0] op_sel_hi:[1,0,0]\n\t"
      "v_mfma_f32_16x16x32_bf16 v[240:243], v[48:51], v[84:87], 0\n\t"
      "ds_read_b128 v[96:99], v7 offset:38912\n\t"
      "v_fma_mix_f32 v35, v31, v25, -v23 op_sel:[0,0,0] op_sel_hi:[1,0,0]\n\t"
      "v_mfma_f32_16x16x32_bf16 v[240:243], v[40:43], v[80:83], v[240:243]\n\t"
      "v_fma_mix_f32 v36, v28, v21, -v23 op_sel:[1,0,0] op_sel_hi:[1,0,0]\n\t"
      "v_fma_mix_f32 v37, v29, v25, -v23 op_sel:[1,0,0] op_sel_hi:[1,0,0]\n\t"
      "v_mfma_f32_16x16x32_bf16 v[248:251], v[40:43], v[132:135], 0\n\t"
      "ds_read_b128 v[92:95], v6 offset:40960\n\t"
      "v_cvt_pk_bf16_f32 v68, v32, v33\n\t"
      "v_mfma_f32_16x16x32_bf16 v[248:251], v[48:51], v[128:131], v[248:251]\n\t"
      "v_cvt_pk_bf16_f32 v69, v34, v35\n\t"
      "v_fma_mix_f32 v38, v30, v21, -v23 op_sel:[1,0,0] op_sel_hi:[1,0,0]\n\t"
      "v_mfma_f32_16x16x32_bf16 v[252:255], v[52:55], v[128:131], 0\n\t"
      "ds_read_b128 v[88:91], v7 offset:40960\n\t"
      "v_fma_mix_f32 v39, v31, v25, -v23 op_sel:[1,0,0] op_sel_hi:[1,0,0]\n\t"
      "v_mfma_f32_16x16x32_bf16 v[252:255], v[44:47], v[132:135], v[252:255]\n\t"
      "v_cvt_pk_bf16_f32 v70, v36, v37\n\t"
      "v_cvt_pk_bf16_f32 v71, v38, v39\n\t"
      "v_mfma_f32_16x16x32_bf16 v[140:143], v[60:63], v[124:127], v[140:143]\n\t"
      "ds_read_b128 v[84:87], v6 offset:43008\n\t"
      "s_waitcnt lgkmcnt(3)\n\t"
      "v_mfma_f32_16x16x32_bf16 v[140:143], v[68:71], v[120:123], v[140:143]\n\t"
      "v_lshlrev_b32 v28, 16, v26\n\t"
      "v_and_b32 v29, 0xffff0000, v26\n\t"
      "v_mfma_f32_16x16x32_bf16 v[136:139], v[64:67], v[120:123], v[136:139]\n\t"
      "ds_read_b128 v[80:83], v7 offset:43008\n\t"
      "v_lshlrev_b32 v30, 16, v27\n\t"
      "v_mfma_f32_16x16x32_bf16 v[136:139], v[56:59], v[124:127], v[136:139]\n\t"
      "v_and_b32 v31, 0xffff0000, v27\n\t"
      "v_mul_f32 v20, 0x4b800000, v28\n\t"
      "v_mfma_f32_16x16x32_bf16 v[144:147], v[56:59], v[116:119], v[144:147]\n\t"
      "ds_read_b128 v[128:131], v6 offset:45056\n\t"
      "v_mul_f32 v24, 0x49800000, v28\n\t"
      "v_mfma_f32_16x16x32_bf16 v[144:147], v[64:67], v[112:115], v[144:147]\n\t"
      "ds_read_b128 v[132:135], v7 offset:45056\n\t"
      "v_mul_f32 v22, v28, v30\n\t"
      "v_mfma_f32_16x16x32_bf16 v[148:151], v[68:71], v[112:115], v[148:151]\n\t"
      "v_mul_f32 v21, 0x4b800000, v29\n\t"
      "v_mul_f32 v25, 0x49800000, v29\n\t"
      "v_mfma_f32_16x16x32_bf16 v[148:151], v[60:63], v[116:119], v[148:151]\n\t"
      "v_mul_f32 v23, v29, v31\n\t"
      "ds_read2st64_b32 v[26:27], v11 offset0:3 offset1:19\n\t"
      "v_mfma_f32_16x16x32_bf16 v[156:159], v[60:63], v[108:111], v[156:159]\n\t"
      "v_lshrrev_b32 v31, 8, v16\n\t"
      "v_and_b32 v28, v13, v16\n\t"
      "v_mfma_f32_16x16x32_bf16 v[156:159], v[68:71], v[104:107], v[156:159]\n\t"
      "s_barrier\n\t"
      "v_mfma_f32_16x16x32_bf16 v[152:155], v[64:67], v[104:107], v[152:155]\n\t"
      "ds_read_b128 v[76:79], v6 offset:47104\n\t"
      "v_and_b32 v29, v14, v16\n\t"
      "v_mfma_f32_16x16x32_bf16 v[152:155], v[56:59], v[108:111], v[152:155]\n\t"
      "ds_read_b128 v[72:75], v7 offset:47104\n\t"
      "v_and_b32 v30, v13, v31\n\t"
      "v_mfma_f32_16x16x32_bf16 v[160:163], v[56:59], v[100:103], v[160:163]\n\t"
      "ds_read_b128 v[120:123], v6 offset:49152\n\t"
      "v_and_b32 v31, v14, v31\n\t"
      "v_mfma_f32_16x16x32_bf16 v[160:163], v[64:67], v[96:99], v[160:163]\n\t"
      "ds_read_b128 v[124:127], v7 offset:49152\n\t"
      "v_fma_mix_f32 v32, v28, v20, -v22 op_sel:[0,0,0] op_sel_hi:[1,0,0]\n\t"
      "v_mfma_f32_16x16x32_bf16 v[164:167], v[68:71], v[96:99], v[164:167]\n\t"
      "ds_read_b128 v[112:115], v6 offset:51200\n\t"
      "v_fma_mix_f32 v33, v29, v24, -v22 op_sel:[0,0,0] op_sel_hi:[1,0,0]\n\t"
      "v_mfma_f32_16x16x32_bf16 v[164:167], v[60:63], v[100:103], v[164:167]\n\t"
      "v_fma_mix_f32 v34, v30, v20, -v22 op_sel:[0,0,0] op_sel_hi:[1,0,0]\n\t"
      "s_waitcnt lgkmcnt(5)\n\t"
      "v_mfma_f32_16x16x32_bf16 v[172:175], v[60:63], v[92:95], v[172:175]\n\t"
      "ds_read_b128 v[116:119], v7 offset:51200\n\t"
      "v_fma_mix_f32 v35, v31, v24, -v22 op_sel:[0,0,0] op_sel_hi:[1,0,0]\n\t"
      "v_mfma_f32_16x16x32_bf16 v[172:175], v[68:71], v[88:91], v[172:175]\n\t"
      "v_fma_mix_f32 v36, v28, v20, -v22 op_sel:[1,0,0] op_sel_hi:[1,0,0]\n\t"
      "v_fma_mix_f32 v37, v29, v24, -v22 op_sel:[1,0,0] op_sel_hi:[1,0,0]\n\t"
      "v_cvt_pk_bf16_f32 v40, v32, v33\n\t"
      "v_mfma_f32_16x16x32_bf16 v[168:171], v[64:67], v[88:91], v[168:171]\n\t"
      "ds_read_b128 v[104:107], v6 offset:53248\n\t"
      "v_cvt_pk_bf16_f32 v41, v34, v35\n\t"
      "v_mfma_f32_16x16x32_bf16 v[168:171], v[56:59], v[92:95], v[168:171]\n\t"
      "v_fma_mix_f32 v38, v30, v20, -v22 op_sel:[1,0,0] op_sel_hi:[1,0,0]\n\t"
      "v_fma_mix_f32 v39, v31, v24, -v22 op_sel:[1,0,0] op_sel_hi:[1,0,0]\n\t"
      "v_mfma_f32_16x16x32_bf16 v[176:179], v[56:59], v[84:87], v[176:179]\n\t"
      "ds_read_b128 v[108:111], v7 offset:53248\n\t"
      "v_cvt_pk_bf16_f32 v42, v36, v37\n\t"
      "v_mfma_f32_16x16x32_bf16 v[176:179], v[64:67], v[80:83], v[176:179]\n\t"
      "ds_read_b128 v[96:99], v6 offset:55296\n\t"
      "v_cvt_pk_bf16_f32 v43, v38, v39\n\t"
      "v_mfma_f32_16x16x32_bf16 v[180:183], v[68:71], v[80:83], v[180:183]\n\t"
      "ds_read_b128 v[100:103], v7 offset:55296\n\t"
      "v_lshrrev_b32 v31, 8, v17\n\t"
      "v_mfma_f32_16x16x32_bf16 v[180:183], v[60:63], v[84:87], v[180:183]\n\t"
      "v_and_b32 v28, v13, v17\n\t"
      "v_and_b32 v29, v14, v17\n\t"
      "v_mfma_f32_16x16x32_bf16 v[188:191], v[60:63], v[128:131], v[188:191]\n\t"
      "ds_read_b128 v[88:91], v6 offset:57344\n\t"
      "v_and_b32 v30, v13, v31\n\t"
      "v_mfma_f32_16x16x32_bf16 v[188:191], v[68:71], v[132:135], v[188:191]\n\t"
      "v_and_b32 v31, v14, v31\n\t"
      "ds_read_b64 v[16:17], v10 offset:12288\n\t"
      "v_mfma_f32_16x16x32_bf16 v[184:187], v[64:67], v[132:135], v[184:187]\n\t"
      "ds_read_b128 v[92:95], v7 offset:57344\n\t"
      "v_fma_mix_f32 v32, v28, v21, -v23 op_sel:[0,0,0] op_sel_hi:[1,0,0]\n\t"
      "v_mfma_f32_16x16x32_bf16 v[184:187], v[56:59], v[128:131], v[184:187]\n\t"
      "v_fma_mix_f32 v33, v29, v25, -v23 op_sel:[0,0,0] op_sel_hi:[1,0,0]\n\t"
      "s_waitcnt lgkmcnt(4)\n\t"
      "v_mfma_f32_16x16x32_bf16 v[192:195], v[56:59], v[76:79], v[192:195]\n\t"
      "v_fma_mix_f32 v34, v30, v21, -v23 op_sel:[0,0,0] op_sel_hi:[1,0,0]\n\t"
      "v_fma_mix_f32 v35, v31, v25, -v23 op_sel:[0,0,0] op_sel_hi:[1,0,0]\n\t"
      "v_mfma_f32_16x16x32_bf16 v[192:195], v[64:67], v[72:75], v[192:195]\n\t"
      "s_barrier\n\t"
      "v_mfma_f32_16x16x32_bf16 v[196:199], v[68:71], v[72:75], v[196:199]\n\t"
      "ds_read_b128 v[80:83], v6 offset:59392\n\t"
      "v_fma_mix_f32 v36, v28, v21, -v23 op_sel:[1,0,0] op_sel_hi:[1,0,0]\n\t"
      "v_mfma_f32_16x16x32_bf16 v[196:199], v[60:63], v[76:79], v[196:199]\n\t"
      "ds_read_b128 v[84:87], v7 offset:59392\n\t"
      "v_fma_mix_f32 v37, v29, v25, -v23 op_sel:[1,0,0] op_sel_hi:[1,0,0]\n\t"
      "v_mfma_f32_16x16x32_bf16 v[204:207], v[60:63], v[120:123], v[204:207]\n\t"
      "ds_read_b128 v[132:135], v6 offset:61440\n\t"
      "v_cvt_pk_bf16_f32 v44, v32, v33\n\t"
      "v_mfma_f32_16x16x32_bf16 v[204:207], v[68:71], v[124:127], v[204:207]\n\t"
      "v_cvt_pk_bf16_f32 v45, v34, v35\n\t"
      "v_fma_mix_f32 v38, v30, v21, -v23 op_sel:[1,0,0] op_sel_hi:[1,0,0]\n\t"
      "v_mfma_f32_16x16x32_bf16 v[200:203], v[64:67], v[124:127], v[200:203]\n\t"
      "ds_read_b128 v[128:131], v7 offset:61440\n\t"
      "v_fma_mix_f32 v39, v31, v25, -v23 op_sel:[1,0,0] op_sel_hi:[1,0,0]\n\t"
      "v_mfma_f32_16x16x32_bf16 v[200:203], v[56:59], v[120:123], v[200:203]\n\t"
      "v_cvt_pk_bf16_f32 v46, v36, v37\n\t"
      "v_cvt_pk_bf16_f32 v47, v38, v39\n\t"
      "v_mfma_f32_16x16x32_bf16 v[208:211], v[56:59], v[112:115], v[208:211]\n\t"
      "ds_read_b128 v[72:75], v8 offset:0\n\t"
      "v_lshrrev_b32 v31, 8, v18\n\t"
      "v_mfma_f32_16x16x32_bf16 v[208:211], v[64:67], v[116:119], v[208:211]\n\t"
      "ds_read_b128 v[76:79], v9 offset:0\n\t"
      "v_and_b32 v28, v13, v18\n\t"
      "v_mfma_f32_16x16x32_bf16 v[212:215], v[68:71], v[116:119], v[212:215]\n\t"
      "ds_read_b128 v[124:127], v8 offset:2048\n\t"
      "v_and_b32 v29, v14, v18\n\t"
      "v_mfma_f32_16x16x32_bf16 v[212:215], v[60:63], v[112:115], v[212:215]\n\t"
      "v_and_b32 v30, v13, v31\n\t"
      "v_and_b32 v31, v14, v31\n\t"
      "v_fma_mix_f32 v32, v28, v20, -v22 op_sel:[0,0,0] op_sel_hi:[1,0,0]\n\t"
      "v_mfma_f32_16x16x32_bf16 v[220:223], v[60:63], v[104:107], v[220:223]\n\t"
      "ds_read_b128 v[120:123], v9 offset:2048\n\t"
      "v_fma_mix_f32 v33, v29, v24, -v22 op_sel:[0,0,0] op_sel_hi:[1,0,0]\n\t"
      "v_mfma_f32_16x16x32_bf16 v[220:223], v[68:71], v[108:111], v[220:223]\n\t"
      "v_fma_mix_f32 v34, v30, v20, -v22 op_sel:[0,0,0] op_sel_hi:[1,0,0]\n\t"
      "v_fma_mix_f32 v35, v31, v24, -v22 op_sel:[0,0,0] op_sel_hi:[1,0,0]\n\t"
      "v_fma_mix_f32 v36, v28, v20, -v22 op_sel:[1,0,0] op_sel_hi:[1,0,0]\n\t"
      "v_mfma_f32_16x16x32_bf16 v[216:219], v[64:67], v[108:111], v[216:219]\n\t"
      "ds_read_b128 v[116:119], v8 offset:4096\n\t"
      "v_fma_mix_f32 v37, v29, v24, -v22 op_sel:[1,0,0] op_sel_hi:[1,0,0]\n\t"
      "v_mfma_f32_16x16x32_bf16 v[216:219], v[56:59], v[104:107], v[216:219]\n\t"
      "v_cvt_pk_bf16_f32 v48, v32, v33\n\t"
      "v_cvt_pk_bf16_f32 v49, v34, v35\n\t"
      "v_mfma_f32_16x16x32_bf16 v[224:227], v[56:59], v[96:99], v[224:227]\n\t"
      "ds_read_b128 v[112:115], v9 offset:4096\n\t"
      "s_waitcnt lgkmcnt(3)\n\t"
      "v_mfma_f32_16x16x32_bf16 v[224:227], v[64:67], v[100:103], v[224:227]\n\t"
      "v_fma_mix_f32 v38, v30, v20, -v22 op_sel:[1,0,0] op_sel_hi:[1,0,0]\n\t"
      "v_fma_mix_f32 v39, v31, v24, -v22 op_sel:[1,0,0] op_sel_hi:[1,0,0]\n\t"
      "v_cvt_pk_bf16_f32 v50, v36, v37\n\t"
      "v_mfma_f32_16x16x32_bf16 v[228:231], v[68:71], v[100:103], v[228:231]\n\t"
      "ds_read_b128 v[108:111], v8 offset:6144\n\t"
      "v_cvt_pk_bf16_f32 v51, v38, v39\n\t"
      "v_mfma_f32_16x16x32_bf16 v[228:231], v[60:63], v[96:99], v[228:231]\n\t"
      "v_lshrrev_b32 v31, 8, v19\n\t"
      "v_and_b32 v28, v13, v19\n\t"
      "v_mfma_f32_16x16x32_bf16 v[236:239], v[60:63], v[88:91], v[236:239]\n\t"
      "ds_read_b128 v[104:107], v9 offset:6144\n\t"
      "v_and_b32 v29, v14, v19\n\t"
      "v_mfma_f32_16x16x32_bf16 v[236:239], v[68:71], v[92:95], v[236:239]\n\t"
      "v_and_b32 v30, v13, v31\n\t"
      "v_and_b32 v31, v14, v31\n\t"
      "v_mfma_f32_16x16x32_bf16 v[232:235], v[64:67], v[92:95], v[232:235]\n\t"
      "ds_read_b128 v[100:103], v8 offset:8192\n\t"
      "ds_read_b64 v[18:19], v10 offset:14336\n\t"
      "v_mfma_f32_16x16x32_bf16 v[232:235], v[56:59], v[88:91], v[232:235]\n\t"
      "v_fma_mix_f32 v32, v28, v21, -v23 op_sel:[0,0,0] op_sel_hi:[1,0,0]\n\t"
      "v_fma_mix_f32 v33, v29, v25, -v23 op_sel:[0,0,0] op_sel_hi:[1,0,0]\n\t"
      "v_mfma_f32_16x16x32_bf16 v[240:243], v[56:59], v[80:83], v[240:243]\n\t"
      "ds_read_b128 v[96:99], v9 offset:8192\n\t"
      "v_fma_mix_f32 v34, v30, v21, -v23 op_sel:[0,0,0] op_sel_hi:[1,0,0]\n\t"
      "v_mfma_f32_16x16x32_bf16 v[240:243], v[64:67], v[84:87], v[240:243]\n\t"
      "ds_read_b128 v[92:95], v8 offset:10240\n\t"
      "v_fma_mix_f32 v35, v31, v25, -v23 op_sel:[0,0,0] op_sel_hi:[1,0,0]\n\t"
      "v_mfma_f32_16x16x32_bf16 v[244:247], v[68:71], v[84:87], v[244:247]\n\t"
      "ds_read_b128 v[88:91], v9 offset:10240\n\t"
      "v_fma_mix_f32 v36, v28, v21, -v23 op_sel:[1,0,0] op_sel_hi:[1,0,0]\n\t"
      "v_mfma_f32_16x16x32_bf16 v[244:247], v[60:63], v[80:83], v[244:247]\n\t"
      "v_fma_mix_f32 v37, v29, v25, -v23 op_sel:[1,0,0] op_sel_hi:[1,0,0]\n\t"
      "v_cvt_pk_bf16_f32 v52, v32, v33\n\t"
      "v_cvt_pk_bf16_f32 v53, v34, v35\n\t"
      "v_mfma_f32_16x16x32_bf16 v[252:255], v[60:63], v[132:135], v[252:255]\n\t"
      "v_fma_mix_f32 v38, v30, v21, -v23 op_sel:[1,0,0] op_sel_hi:[1,0,0]\n\t"
      "v_fma_mix_f32 v39, v31, v25, -v23 op_sel:[1,0,0] op_sel_hi:[1,0,0]\n\t"
      "v_mfma_f32_16x16x32_bf16 v[252:255], v[68:71], v[128:131], v[252:255]\n\t"
      "s_barrier\n\t"
      "v_mfma_f32_16x16x32_bf16 v[248:251], v[64:67], v[128:131], v[248:251]\n\t"
      "ds_read_b128 v[84:87], v8 offset:12288\n\t"
      "v_cvt_pk_bf16_f32 v54, v36, v37\n\t"
      "v_mfma_f32_16x16x32_bf16 v[248:251], v[56:59], v[132:135], v[248:251]\n\t"
      "ds_read_b128 v[80:83], v9 offset:12288\n\t"
      "v_cvt_pk_bf16_f32 v55, v38, v39\n\t"
      "v_mfma_f32_16x16x32_bf16 v[136:139], v[40:43], v[72:75], v[136:139]\n\t"
      "ds_read_b128 v[128:131], v8 offset:14336\n\t"
      "v_lshlrev_b32 v28, 16, v26\n\t"
      "v_mfma_f32_16x16x32_bf16 v[136:139], v[48:51], v[76:79], v[136:139]\n\t"
      "v_and_b32 v29, 0xffff0000, v26\n\t"
      "v_lshlrev_b32 v30, 16, v27\n\t"
      "v_mfma_f32_16x16x32_bf16 v[140:143], v[52:55], v[76:79], v[140:143]\n\t"
      "ds_read_b128 v[132:135], v9 offset:14336\n\t"
      "v_and_b32 v31, 0xffff0000, v27\n\t"
      "v_mfma_f32_16x16x32_bf16 v[140:143], v[44:47], v[72:75], v[140:143]\n\t"
      "v_mul_f32 v20, 0x4b800000, v28\n\t"
      "v_mul_f32 v24, 0x49800000, v28\n\t"
      "v_mfma_f32_16x16x32_bf16 v[148:151], v[44:47], v[124:127], v[148:151]\n\t"
      "ds_read_b128 v[76:79], v8 offset:16384\n\t"
      "s_waitcnt lgkmcnt(4)\n\t"
      "v_mfma_f32_16x16x32_bf16 v[148:151], v[52:55], v[120:123], v[148:151]\n\t"
      "v_mul_f32 v22, v28, v30\n\t"
      "v_mul_f32 v21, 0x4b800000, v29\n\t"
      "v_mfma_f32_16x16x32_bf16 v[144:147], v[48:51], v[120:123], v[144:147]\n\t"
      "ds_read_b128 v[72:75], v9 offset:16384\n\t"
      "v_mul_f32 v25, 0x49800000, v29\n\t"
      "v_mfma_f32_16x16x32_bf16 v[144:147], v[40:43], v[124:127], v[144:147]\n\t"
      "v_mul_f32 v23, v29, v31\n\t"
      "ds_read2st64_b32 v[26:27], v11 offset0:4 offset1:20\n\t"
      "v_mfma_f32_16x16x32_bf16 v[152:155], v[40:43], v[116:119], v[152:155]\n\t"
      "ds_read_b128 v[120:123], v8 offset:18432\n\t"
      "v_lshrrev_b32 v31, 8, v16\n\t"
      "v_mfma_f32_16x16x32_bf16 v[152:155], v[48:51], v[112:115], v[152:155]\n\t"
      "ds_read_b128 v[124:127], v9 offset:18432\n\t"
      "v_and_b32 v28, v13, v16\n\t"
      "v_mfma_f32_16x16x32_bf16 v[156:159], v[52:55], v[112:115], v[156:159]\n\t"
      "ds_read_b128 v[112:115], v8 offset:20480\n\t"
      "v_and_b32 v29, v14, v16\n\t"
      "v_mfma_f32_16x16x32_bf16 v[156:159], v[44:47], v[116:119], v[156:159]\n\t"
      "v_and_b32 v30, v13, v31\n\t"
      "v_and_b32 v31, v14, v31\n\t"
      "v_mfma_f32_16x16x32_bf16 v[164:167], v[44:47], v[108:111], v[164:167]\n\t"
      "ds_read_b128 v[116:119], v9 offset:20480\n\t"
      "v_fma_mix_f32 v32, v28, v20, -v22 op_sel:[0,0,0] op_sel_hi:[1,0,0]\n\t"
      "v_mfma_f32_16x16x32_bf16 v[164:167], v[52:55], v[104:107], v[164:167]\n\t"
      "v_fma_mix_f32 v33, v29, v24, -v22 op_sel:[0,0,0] op_sel_hi:[1,0,0]\n\t"
      "v_fma_mix_f32 v34, v30, v20, -v22 op_sel:[0,0,0] op_sel_hi:[1,0,0]\n\t"
      "v_mfma_f32_16x16x32_bf16 v[160:163], v[48:51], v[104:107], v[160:163]\n\t"
      "ds_read_b128 v[104:107], v8 offset:22528\n\t"
      "v_fma_mix_f32 v35, v31, v24, -v22 op_sel:[0,0,0] op_sel_hi:[1,0,0]\n\t"
      "v_mfma_f32_16x16x32_bf16 v[160:163], v[40:43], v[108:111], v[160:163]\n\t"
      "v_fma_mix_f32 v36, v28, v20, -v22 op_sel:[1,0,0] op_sel_hi:[1,0,0]\n\t"
      "v_fma_mix_f32 v37, v29, v24, -v22 op_sel:[1,0,0] op_sel_hi:[1,0,0]\n\t"
      "v_mfma_f32_16x16x32_bf16 v[168:171], v[40:43], v[100:103], v[168:171]\n\t"
      "ds_read_b128 v[108:111], v9 offset:22528\n\t"
      "v_cvt_pk_bf16_f32 v56, v32, v33\n\t"
      "v_mfma_f32_16x16x32_bf16 v[168:171], v[48:51], v[96:99], v[168:171]\n\t"
      "v_cvt_pk_bf16_f32 v57, v34, v35\n\t"
      "v_fma_mix_f32 v38, v30, v20, -v22 op_sel:[1,0,0] op_sel_hi:[1,0,0]\n\t"
      "v_mfma_f32_16x16x32_bf16 v[172:175], v[52:55], v[96:99], v[172:175]\n\t"
      "ds_read_b128 v[96:99], v8 offset:24576\n\t"
      "v_fma_mix_f32 v39, v31, v24, -v22 op_sel:[1,0,0] op_sel_hi:[1,0,0]\n\t"
      "v_mfma_f32_16x16x32_bf16 v[172:175], v[44:47], v[100:103], v[172:175]\n\t"
      "v_cvt_pk_bf16_f32 v58, v36, v37\n\t"
      "v_cvt_pk_bf16_f32 v59, v38, v39\n\t"
      "v_mfma_f32_16x16x32_bf16 v[180:183], v[44:47], v[92:95], v[180:183]\n\t"
      "ds_read_b128 v[100:103], v9 offset:24576\n\t"
      "v_lshrrev_b32 v31, 8, v17\n\t"
      "v_mfma_f32_16x16x32_bf16 v[180:183], v[52:55], v[88:91], v[180:183]\n\t"
      "v_and_b32 v28, v13, v17\n\t"
      "v_and_b32 v29, v14, v17\n\t"
      "v_mfma_f32_16x16x32_bf16 v[176:179], v[48:51], v[88:91], v[176:179]\n\t"
      "ds_read_b128 v[88:91], v8 offset:26624\n\t"
      "v_and_b32 v30, v13, v31\n\t"
      "v_mfma_f32_16x16x32_bf16 v[176:179], v[40:43], v[92:95], v[176:179]\n\t"
      "v_and_b32 v31, v14, v31\n\t"
      "ds_read_b64 v[16:17], v10 offset:16384\n\t"
      "v_mfma_f32_16x16x32_bf16 v[184:187], v[40:43], v[84:87], v[184:187]\n\t"
      "ds_read_b128 v[92:95], v9 offset:26624\n\t"
      "s_waitcnt lgkmcnt(4)\n\t"
      "v_mfma_f32_16x16x32_bf16 v[184:187], v[48:51], v[80:83], v[184:187]\n\t"
      "v_fma_mix_f32 v32, v28, v21, -v23 op_sel:[0,0,0] op_sel_hi:[1,0,0]\n\t"
      "v_fma_mix_f32 v33, v29, v25, -v23 op_sel:[0,0,0] op_sel_hi:[1,0,0]\n\t"
      "v_mfma_f32_16x16x32_bf16 v[188:191], v[52:55], v[80:83], v[188:191]\n\t"
      "ds_read_b128 v[80:83], v8 offset:28672\n\t"
      "v_fma_mix_f32 v34, v30, v21, -v23 op_sel:[0,0,0] op_sel_hi:[1,0,0]\n\t"
      "v_mfma_f32_16x16x32_bf16 v[188:191], v[44:47], v[84:87], v[188:191]\n\t"
      "ds_read_b128 v[84:87], v9 offset:28672\n\t"
      "v_fma_mix_f32 v35, v31, v25, -v23 op_sel:[0,0,0] op_sel_hi:[1,0,0]\n\t"
      "v_mfma_f32_16x16x32_bf16 v[196:199], v[44:47], v[128:131], v[196:199]\n\t"
      "v_fma_mix_f32 v36, v28, v21, -v23 op_sel:[1,0,0] op_sel_hi:[1,0,0]\n\t"
      "v_fma_mix_f32 v37, v29, v25, -v23 op_sel:[1,0,0] op_sel_hi:[1,0,0]\n\t"
      "v_mfma_f32_16x16x32_bf16 v[196:199], v[52:55], v[132:135], v[196:199]\n\t"
      "s_barrier\n\t"
      "v_mfma_f32_16x16x32_bf16 v[192:195], v[48:51], v[132:135], v[192:195]\n\t"
      "ds_read_b128 v[132:135], v8 offset:32768\n\t"
      "v_cvt_pk_bf16_f32 v60, v32, v33\n\t"
      "v_mfma_f32_16x16x32_bf16 v[192:195], v[40:43], v[128:131], v[192:195]\n\t"
      "ds_read_b128 v[128:131], v9 offset:32768\n\t"
      "v_cvt_pk_bf16_f32 v61, v34, v35\n\t"
      "v_mfma_f32_16x16x32_bf16 v[200:203], v[40:43], v[76:79], v[200:203]\n\t"
      "v_fma_mix_f32 v38, v30, v21, -v23 op_sel:[1,0,0] op_sel_hi:[1,0,0]\n\t"
      "v_fma_mix_f32 v39, v31, v25, -v23 op_sel:[1,0,0] op_sel_hi:[1,0,0]\n\t"
      "v_mfma_f32_16x16x32_bf16 v[200:203], v[48:51], v[72:75], v[200:203]\n\t"
      "v_cvt_pk_bf16_f32 v62, v36, v37\n\t"
      "v_cvt_pk_bf16_f32 v63, v38, v39\n\t"
      "v_mfma_f32_16x16x32_bf16 v[204:207], v[52:55], v[72:75], v[204:207]\n\t"
      "ds_read_b128 v[72:75], v8 offset:34816\n\t"
      "v_lshrrev_b32 v31, 8, v18\n\t"
      "v_mfma_f32_16x16x32_bf16 v[204:207], v[44:47], v[76:79], v[204:207]\n\t"
      "ds_read_b128 v[76:79], v9 offset:34816\n\t"
      "v_and_b32 v28, v13, v18\n\t"
      "v_mfma_f32_16x16x32_bf16 v[212:215], v[44:47], v[120:123], v[212:215]\n\t"
      "v_and_b32 v29, v14, v18\n\t"
      "v_and_b32 v30, v13, v31\n\t"
      "v_mfma_f32_16x16x32_bf16 v[212:215], v[52:55], v[124:127], v[212:215]\n\t"
      "v_and_b32 v31, v14, v31\n\t"
      "v_fma_mix_f32 v32, v28, v20, -v22 op_sel:[0,0,0] op_sel_hi:[1,0,0]\n\t"
      "v_mfma_f32_16x16x32_bf16 v[208:211], v[48:51], v[124:127], v[208:211]\n\t"
      "ds_read_b128 v[124:127], v8 offset:36864\n\t"
      "v_fma_mix_f32 v33, v29, v24, -v22 op_sel:[0,0,0] op_sel_hi:[1,0,0]\n\t"
      "v_mfma_f32_16x16x32_bf16 v[208:211], v[40:43], v[120:123], v[208:211]\n\t"
      "ds_read_b128 v[120:123], v9 offset:36864\n\t"
      "v_fma_mix_f32 v34, v30, v20, -v22 op_sel:[0,0,0] op_sel_hi:[1,0,0]\n\t"
      "v_mfma_f32_16x16x32_bf16 v[216:219], v[40:43], v[112:115], v[216:219]\n\t"
      "v_fma_mix_f32 v35, v31, v24, -v22 op_sel:[0,0,0] op_sel_hi:[1,0,0]\n\t"
      "v_fma_mix_f32 v36, v28, v20, -v22 op_sel:[1,0,0] op_sel_hi:[1,0,0]\n\t"
      "v_mfma_f32_16x16x32_bf16 v[216:219], v[48:51], v[116:119], v[216:219]\n\t"
      "v_fma_mix_f32 v37, v29, v24, -v22 op_sel:[1,0,0] op_sel_hi:[1,0,0]\n\t"
      "v_cvt_pk_bf16_f32 v64, v32, v33\n\t"
      "v_mfma_f32_16x16x32_bf16 v[220:223], v[52:55], v[116:119], v[220:223]\n\t"
      "ds_read_b128 v[116:119], v8 offset:38912\n\t"
      "v_cvt_pk_bf16_f32 v65, v34, v35\n\t"
      "v_mfma_f32_16x16x32_bf16 v[220:223], v[44:47], v[112:115], v[220:223]\n\t"
      "ds_read_b128 v[112:115], v9 offset:38912\n\t"
      "v_fma_mix_f32 v38, v30, v20, -v22 op_sel:[1,0,0] op_sel_hi:[1,0,0]\n\t"
      "v_mfma_f32_16x16x32_bf16 v[228:231], v[44:47], v[104:107], v[228:231]\n\t"
      "v_fma_mix_f32 v39, v31, v24, -v22 op_sel:[1,0,0] op_sel_hi:[1,0,0]\n\t"
      "v_cvt_pk_bf16_f32 v66, v36, v37\n\t"
      "v_mfma_f32_16x16x32_bf16 v[228:231], v[52:55], v[108:111], v[228:231]\n\t"
      "v_cvt_pk_bf16_f32 v67, v38, v39\n\t"
      "v_lshrrev_b32 v31, 8, v19\n\t"
      "v_mfma_f32_16x16x32_bf16 v[224:227], v[48:51], v[108:111], v[224:227]\n\t"
      "ds_read_b128 v[108:111], v8 offset:40960\n\t"
      "v_and_b32 v28, v13, v19\n\t"
      "v_mfma_f32_16x16x32_bf16 v[224:227], v[40:43], v[104:107], v[224:227]\n\t"
      "ds_read_b128 v[104:107], v9 offset:40960\n\t"
      "v_and_b32 v29, v14, v19\n\t"
      "v_mfma_f32_16x16x32_bf16 v[232:235], v[40:43], v[96:99], v[232:235]\n\t"
      "v_and_b32 v30, v13, v31\n\t"
      "s_waitcnt lgkmcnt(3)\n\t"
      "v_mfma_f32_16x16x32_bf16 v[232:235], v[48:51], v[100:103], v[232:235]\n\t"
      "v_and_b32 v31, v14, v31\n\t"
      "ds_read_b64 v[18:19], v10 offset:18432\n\t"
      "v_mfma_f32_16x16x32_bf16 v[236:239], v[52:55], v[100:103], v[236:239]\n\t"
      "ds_read_b128 v[100:103], v8 offset:43008\n\t"
      "v_fma_mix_f32 v32, v28, v21, -v23 op_sel:[0,0,0] op_sel_hi:[1,0,0]\n\t"
      "v_mfma_f32_16x16x32_bf16 v[236:239], v[44:47], v[96:99], v[236:239]\n\t"
      "ds_read_b128 v[96:99], v9 offset:43008\n\t"
      "v_fma_mix_f32 v33, v29, v25, -v23 op_sel:[0,0,0] op_sel_hi:[1,0,0]\n\t"
      "v_mfma_f32_16x16x32_bf16 v[244:247], v[44:47], v[88:91], v[244:247]\n\t"
      "v_fma_mix_f32 v34, v30, v21, -v23 op_sel:[0,0,0] op_sel_hi:[1,0,0]\n\t"
      "v_fma_mix_f32 v35, v31, v25, -v23 op_sel:[0,0,0] op_sel_hi:[1,0,0]\n\t"
      "v_mfma_f32_16x16x32_bf16 v[244:247], v[52:55], v[92:95], v[244:247]\n\t"
      "v_fma_mix_f32 v36, v28, v21, -v23 op_sel:[1,0,0] op_sel_hi:[1,0,0]\n\t"
      "v_fma_mix_f32 v37, v29, v25, -v23 op_sel:[1,0,0] op_sel_hi:[1,0,0]\n\t"
      "v_mfma_f32_16x16x32_bf16 v[240:243], v[48:51], v[92:95], v[240:243]\n\t"
      "ds_read_b128 v[92:95], v8 offset:45056\n\t"
      "v_cvt_pk_bf16_f32 v68, v32, v33\n\t"
      "v_mfma_f32_16x16x32_bf16 v[240:243], v[40:43], v[88:91], v[240:243]\n\t"
      "ds_read_b128 v[88:91], v9 offset:45056\n\t"
      "v_cvt_pk_bf16_f32 v69, v34, v35\n\t"
      "v_mfma_f32_16x16x32_bf16 v[248:251], v[40:43], v[80:83], v[248:251]\n\t"
      "v_fma_mix_f32 v38, v30, v21, -v23 op_sel:[1,0,0] op_sel_hi:[1,0,0]\n\t"
      "v_fma_mix_f32 v39, v31, v25, -v23 op_sel:[1,0,0] op_sel_hi:[1,0,0]\n\t"
      "v_mfma_f32_16x16x32_bf16 v[248:251], v[48:51], v[84:87], v[248:251]\n\t"
      "v_cvt_pk_bf16_f32 v70, v36, v37\n\t"
      "v_cvt_pk_bf16_f32 v71, v38, v39\n\t"
      "v_mfma_f32_16x16x32_bf16 v[252:255], v[52:55], v[84:87], v[252:255]\n\t"
      "ds_read_b128 v[84:87], v8 offset:47104\n\t"
      "v_mfma_f32_16x16x32_bf16 v[252:255], v[44:47], v[80:83], v[252:255]\n\t"
      "ds_read_b128 v[80:83], v9 offset:47104\n\t"
      "v_mfma_f32_16x16x32_bf16 v[140:143], v[60:63], v[132:135], v[140:143]\n\t"
      "v_lshlrev_b32 v28, 16, v26\n\t"
      "v_and_b32 v29, 0xffff0000, v26\n\t"
      "v_mfma_f32_16x16x32_bf16 v[140:143], v[68:71], v[128:131], v[140:143]\n\t"
      "v_lshlrev_b32 v30, 16, v27\n\t"
      "v_and_b32 v31, 0xffff0000, v27\n\t"
      "s_nop 0\n\t"
      "LH15x1_%=:\n\t"
      "s_cbranch_execz LH15x5_%=\n\t"
      "v_mfma_f32_16x16x32_bf16 v[136:139], v[64:67], v[128:131], v[136:139]\n\t"
      "ds_read_b128 v[128:131], v8 offset:49152\n\t"
      "v_mul_f32 v20, 0x4b800000, v28\n\t"
      "v_mfma_f32_16x16x32_bf16 v[136:139], v[56:59], v[132:135], v[136:139]\n\t"
      "ds_read_b128 v[132:135], v9 offset:49152\n\t"
      "v_mul_f32 v24, 0x49800000, v28\n\t"
      "v_mfma_f32_16x16x32_bf16 v[144:147], v[56:59], v[72:75], v[144:147]\n\t"
      "v_mul_f32 v22, v28, v30\n\t"
      "v_mul_f32 v21, 0x4b800000, v29\n\t"
      "v_mfma_f32_16x16x32_bf16 v[144:147], v[64:67], v[76:79], v[144:147]\n\t"
      "v_mul_f32 v25, 0x49800000, v29\n\t"
      "v_mul_f32 v23, v29, v31\n\t"
      "v_mfma_f32_16x16x32_bf16 v[148:151], v[68:71], v[76:79], v[148:151]\n\t"
      "ds_read_b128 v[76:79], v8 offset:51200\n\t"
      "ds_read2st64_b32 v[26:27], v11 offset0:5 offset1:21\n\t"
      "v_mfma_f32_16x16x32_bf16 v[148:151], v[60:63], v[72:75], v[148:151]\n\t"
      "ds_read_b128 v[72:75], v9 offset:51200\n\t"
      "v_lshrrev_b32 v31, 8, v16\n\t"
      "v_mfma_f32_16x16x32_bf16 v[156:159], v[60:63], v[124:127], v[156:159]\n\t"
      "v_and_b32 v28, v13, v16\n\t"
      "v_and_b32 v29, v14, v16\n\t"
      "v_mfma_f32_16x16x32_bf16 v[156:159], v[68:71], v[120:123], v[156:159]\n\t"
      "v_and_b32 v30, v13, v31\n\t"
      "v_and_b32 v31, v14, v31\n\t"
      "v_mfma_f32_16x16x32_bf16 v[152:155], v[64:67], v[120:123], v[152:155]\n\t"
      "ds_read_b128 v[120:123], v8 offset:53248\n\t"
      "v_fma_mix_f32 v32, v28, v20, -v22 op_sel:[0,0,0] op_sel_hi:[1,0,0]\n\t"
      "v_mfma_f32_16x16x32_bf16 v[152:155], v[56:59], v[124:127], v[152:155]\n\t"
      "ds_read_b128 v[124:127], v9 offset:53248\n\t"
      "v_fma_mix_f32 v33, v29, v24, -v22 op_sel:[0,0,0] op_sel_hi:[1,0,0]\n\t"
      "v_mfma_f32_16x16x32_bf16 v[160:163], v[56:59], v[116:119], v[160:163]\n\t"
      "v_fma_mix_f32 v34, v30, v20, -v22 op_sel:[0,0,0] op_sel_hi:[1,0,0]\n\t"
      "s_waitcnt lgkmcnt(3)\n\t"
      "v_mfma_f32_16x16x32_bf16 v[160:163], v[64:67], v[112:115], v[160:163]\n\t"
      "v_fma_mix_f32 v35, v31, v24, -v22 op_sel:[0,0,0] op_sel_hi:[1,0,0]\n\t"
      "v_fma_mix_f32 v36, v28, v20, -v22 op_sel:[1,0,0] op_sel_hi:[1,0,0]\n\t"
      "v_mfma_f32_16x16x32_bf16 v[164:167], v[68:71], v[112:115], v[164:167]\n\t"
      "ds_read_b128 v[112:115], v8 offset:55296\n\t"
      "v_fma_mix_f32 v37, v29, v24, -v22 op_sel:[1,0,0] op_sel_hi:[1,0,0]\n\t"
      "v_mfma_f32_16x16x32_bf16 v[164:167], v[60:63], v[116:119], v[164:167]\n\t"
      "ds_read_b128 v[116:119], v9 offset:55296\n\t"
      "v_cvt_pk_bf16_f32 v40, v32, v33\n\t"
      "v_mfma_f32_16x16x32_bf16 v[172:175], v[60:63], v[108:111], v[172:175]\n\t"
      "v_cvt_pk_bf16_f32 v41, v34, v35\n\t"
      "v_fma_mix_f32 v38, v30, v20, -v22 op_sel:[1,0,0] op_sel_hi:[1,0,0]\n\t"
      "v_mfma_f32_16x16x32_bf16 v[172:175], v[68:71], v[104:107], v[172:175]\n\t"
      "v_fma_mix_f32 v39, v31, v24, -v22 op_sel:[1,0,0] op_sel_hi:[1,0,0]\n\t"
      "v_cvt_pk_bf16_f32 v42, v36, v37\n\t"
      "v_mfma_f32_16x16x32_bf16 v[168:171], v[64:67], v[104:107], v[168:171]\n\t"
      "ds_read_b128 v[104:107], v8 offset:57344\n\t"
      "v_cvt_pk_bf16_f32 v43, v38, v39\n\t"
      "v_mfma_f32_16x16x32_bf16 v[168:171], v[56:59], v[108:111], v[168:171]\n\t"
      "ds_read_b128 v[108:111], v9 offset:57344\n\t"
      "v_lshrrev_b32 v31, 8, v17\n\t"
      "v_mfma_f32_16x16x32_bf16 v[176:179], v[56:59], v[100:103], v[176:179]\n\t"
      "v_and_b32 v28, v13, v17\n\t"
      "v_and_b32 v29, v14, v17\n\t"
      "v_mfma_f32_16x16x32_bf16 v[176:179], v[64:67], v[96:99], v[176:179]\n\t"
      "v_and_b32 v30, v13, v31\n\t"
      "v_and_b32 v31, v14, v31\n\t"
      "v_mfma_f32_16x16x32_bf16 v[180:183], v[68:71], v[96:99], v[180:183]\n\t"
      "ds_read_b128 v[96:99], v8 offset:59392\n\t"
      "ds_read_b64 v[16:17], v10 offset:20480\n\t"
      "v_mfma_f32_16x16x32_bf16 v[180:183], v[60:63], v[100:103], v[180:183]\n\t"
      "ds_read_b128 v[100:103], v9 offset:59392\n\t"
      "v_fma_mix_f32 v32, v28, v21, -v23 op_sel:[0,0,0] op_sel_hi:[1,0,0]\n\t"
      "v_mfma_f32_16x16x32_bf16 v[188:191], v[60:63], v[92:95], v[188:191]\n\t"
      "v_fma_mix_f32 v33, v29, v25, -v23 op_sel:[0,0,0] op_sel_hi:[1,0,0]\n\t"
      "v_fma_mix_f32 v34, v30, v21, -v23 op_sel:[0,0,0] op_sel_hi:[1,0,0]\n\t"
      "v_mfma_f32_16x16x32_bf16 v[188:191], v[68:71], v[88:91], v[188:191]\n\t"
      "v_fma_mix_f32 v35, v31, v25, -v23 op_sel:[0,0,0] op_sel_hi:[1,0,0]\n\t"
      "v_fma_mix_f32 v36, v28, v21, -v23 op_sel:[1,0,0] op_sel_hi:[1,0,0]\n\t"
      "v_mfma_f32_16x16x32_bf16 v[184:187], v[64:67], v[88:91], v[184:187]\n\t"
      "ds_read_b128 v[88:91], v8 offset:61440\n\t"
      "v_fma_mix_f32 v37, v29, v25, -v23 op_sel:[1,0,0] op_sel_hi:[1,0,0]\n\t"
      "v_mfma_f32_16x16x32_bf16 v[184:187], v[56:59], v[92:95], v[184:187]\n\t"
      "ds_read_b128 v[92:95], v9 offset:61440\n\t"
      "v_cvt_pk_bf16_f32 v44, v32, v33\n\t"
      "v_mfma_f32_16x16x32_bf16 v[192:195], v[56:59], v[84:87], v[192:195]\n\t"
      "v_cvt_pk_bf16_f32 v45, v34, v35\n\t"
      "v_fma_mix_f32 v38, v30, v21, -v23 op_sel:[1,0,0] op_sel_hi:[1,0,0]\n\t"
      "v_mfma_f32_16x16x32_bf16 v[192:195], v[64:67], v[80:83], v[192:195]\n\t"
      "s_barrier\n\t"
      "v_mfma_f32_16x16x32_bf16 v[196:199], v[68:71], v[80:83], v[196:199]\n\t"
      "ds_read_b128 v[80:83], v6 offset:0\n\t"
      "v_fma_mix_f32 v39, v31, v25, -v23 op_sel:[1,0,0] op_sel_hi:[1,0,0]\n\t"
      "v_mfma_f32_16x16x32_bf16 v[196:199], v[60:63], v[84:87], v[196:199]\n\t"
      "ds_read_b128 v[84:87], v7 offset:0\n\t"
      "v_cvt_pk_bf16_f32 v46, v36, v37\n\t"
      "v_mfma_f32_16x16x32_bf16 v[204:207], v[60:63], v[128:131], v[204:207]\n\t"
      "v_cvt_pk_bf16_f32 v47, v38, v39\n\t"
      "v_lshrrev_b32 v31, 8, v18\n\t"
      "v_mfma_f32_16x16x32_bf16 v[204:207], v[68:71], v[132:135], v[204:207]\n\t"
      "v_and_b32 v28, v13, v18\n\t"
      "v_and_b32 v29, v14, v18\n\t"
      "v_mfma_f32_16x16x32_bf16 v[200:203], v[64:67], v[132:135], v[200:203]\n\t"
      "ds_read_b128 v[132:135], v6 offset:2048\n\t"
      "v_and_b32 v30, v13, v31\n\t"
      "v_mfma_f32_16x16x32_bf16 v[200:203], v[56:59], v[128:131], v[200:203]\n\t"
      "ds_read_b128 v[128:131], v7 offset:2048\n\t"
      "v_and_b32 v31, v14, v31\n\t"
      "v_mfma_f32_16x16x32_bf16 v[208:211], v[56:59], v[76:79], v[208:211]\n\t"
      "v_fma_mix_f32 v32, v28, v20, -v22 op_sel:[0,0,0] op_sel_hi:[1,0,0]\n\t"
      "s_waitcnt lgkmcnt(3)\n\t"
      "v_mfma_f32_16x16x32_bf16 v[208:211], v[64:67], v[72:75], v[208:211]\n\t"
      "v_fma_mix_f32 v33, v29, v24, -v22 op_sel:[0,0,0] op_sel_hi:[1,0,0]\n\t"
      "v_fma_mix_f32 v34, v30, v20, -v22 op_sel:[0,0,0] op_sel_hi:[1,0,0]\n\t"
      "v_mfma_f32_16x16x32_bf16 v[212:215], v[68:71], v[72:75], v[212:215]\n\t"
      "ds_read_b128 v[72:75], v6 offset:4096\n\t"
      "v_fma_mix_f32 v35, v31, v24, -v22 op_sel:[0,0,0] op_sel_hi:[1,0,0]\n\t"
      "v_mfma_f32_16x16x32_bf16 v[212:215], v[60:63], v[76:79], v[212:215]\n\t"
      "ds_read_b128 v[76:79], v7 offset:4096\n\t"
      "v_fma_mix_f32 v36, v28, v20, -v22 op_sel:[1,0,0] op_sel_hi:[1,0,0]\n\t"
      "v_mfma_f32_16x16x32_bf16 v[220:223], v[60:63], v[120:123], v[220:223]\n\t"
      "v_fma_mix_f32 v37, v29, v24, -v22 op_sel:[1,0,0] op_sel_hi:[1,0,0]\n\t"
      "v_cvt_pk_bf16_f32 v48, v32, v33\n\t"
      "v_mfma_f32_16x16x32_bf16 v[220:223], v[68:71], v[124:127], v[220:223]\n\t"
      "v_cvt_pk_bf16_f32 v49, v34, v35\n\t"
      "v_fma_mix_f32 v38, v30, v20, -v22 op_sel:[1,0,0] op_sel_hi:[1,0,0]\n\t"
      "v_mfma_f32_16x16x32_bf16 v[216:219], v[64:67], v[124:127], v[216:219]\n\t"
      "ds_read_b128 v[124:127], v6 offset:6144\n\t"
      "v_fma_mix_f32 v39, v31, v24, -v22 op_sel:[1,0,0] op_sel_hi:[1,0,0]\n\t"
      "v_mfma_f32_16x16x32_bf16 v[216:219], v[56:59], v[120:123], v[216:219]\n\t"
      "ds_read_b128 v[120:123], v7 offset:6144\n\t"
      "v_cvt_pk_bf16_f32 v50, v36, v37\n\t"
      "v_mfma_f32_16x16x32_bf16 v[224:227], v[56:59], v[112:115], v[224:227]\n\t"
      "v_cvt_pk_bf16_f32 v51, v38, v39\n\t"
      "v_lshrrev_b32 v31, 8, v19\n\t"
      "v_mfma_f32_16x16x32_bf16 v[224:227], v[64:67], v[116:119], v[224:227]\n\t"
      "v_and_b32 v28, v13, v19\n\t"
      "v_and_b32 v29, v14, v19\n\t"
      "v_mfma_f32_16x16x32_bf16 v[228:231], v[68:71], v[116:119], v[228:231]\n\t"
      "ds_read_b128 v[116:119], v6 offset:8192\n\t"
      "v_and_b32 v30, v13, v31\n\t"
      "v_mfma_f32_16x16x32_bf16 v[228:231], v[60:63], v[112:115], v[228:231]\n\t"
      "ds_read_b128 v[112:115], v7 offset:8192\n\t"
      "v_and_b32 v31, v14, v31\n\t"
      "v_mfma_f32_16x16x32_bf16 v[236:239], v[60:63], v[104:107], v[236:239]\n\t"
      "ds_read_b64 v[18:19], v10 offset:22528\n\t"
      "v_fma_mix_f32 v32, v28, v21, -v23 op_sel:[0,0,0] op_sel_hi:[1,0,0]\n\t"
      "v_mfma_f32_16x16x32_bf16 v[236:239], v[68:71], v[108:111], v[236:239]\n\t"
      "v_fma_mix_f32 v33, v29, v25, -v23 op_sel:[0,0,0] op_sel_hi:[1,0,0]\n\t"
      "v_fma_mix_f32 v34, v30, v21, -v23 op_sel:[0,0,0] op_sel_hi:[1,0,0]\n\t"
      "v_mfma_f32_16x16x32_bf16 v[232:235], v[64:67], v[108:111], v[232:235]\n\t"
      "ds_read_b128 v[108:111], v6 offset:10240\n\t"
      "v_fma_mix_f32 v35, v31, v25, -v23 op_sel:[0,0,0] op_sel_hi:[1,0,0]\n\t"
      "v_mfma_f32_16x16x32_bf16 v[232:235], v[56:59], v[104:107], v[232:235]\n\t"
      "ds_read_b128 v[104:107], v7 offset:10240\n\t"
      "v_fma_mix_f32 v36, v28, v21, -v23 op_sel:[1,0,0] op_sel_hi:[1,0,0]\n\t"
      "v_mfma_f32_16x16x32_bf16 v[240:243], v[56:59], v[96:99], v[240:243]\n\t"
      "v_fma_mix_f32 v37, v29, v25, -v23 op_sel:[1,0,0] op_sel_hi:[1,0,0]\n\t"
      "v_cvt_pk_bf16_f32 v52, v32, v33\n\t"
      "v_mfma_f32_16x16x32_bf16 v[240:243], v[64:67], v[100:103], v[240:243]\n\t"
      "v_cvt_pk_bf16_f32 v53, v34, v35\n\t"
      "v_fma_mix_f32 v38, v30, v21, -v23 op_sel:[1,0,0] op_sel_hi:[1,0,0]\n\t"
      "v_mfma_f32_16x16x32_bf16 v[244:247], v[68:71], v[100:103], v[244:247]\n\t"
      "ds_read_b128 v[100:103], v6 offset:12288\n\t"
      "v_fma_mix_f32 v39, v31, v25, -v23 op_sel:[1,0,0] op_sel_hi:[1,0,0]\n\t"
      "v_mfma_f32_16x16x32_bf16 v[244:247], v[60:63], v[96:99], v[244:247]\n\t"
      "ds_read_b128 v[96:99], v7 offset:12288\n\t"
      "v_cvt_pk_bf16_f32 v54, v36, v37\n\t"
      "v_mfma_f32_16x16x32_bf16 v[252:255], v[60:63], v[88:91], v[252:255]\n\t"
      "v_cvt_pk_bf16_f32 v55, v38, v39\n\t"
      "v_mfma_f32_16x16x32_bf16 v[252:255], v[68:71], v[92:95], v[252:255]\n\t"
      "v_mfma_f32_16x16x32_bf16 v[248:251], v[64:67], v[92:95], v[248:251]\n\t"
      "ds_read_b128 v[92:95], v6 offset:14336\n\t"
      "v_mfma_f32_16x16x32_bf16 v[248:251], v[56:59], v[88:91], v[248:251]\n\t"
      "ds_read_b128 v[88:91], v7 offset:14336\n\t"
      "v_mfma_f32_16x16x32_bf16 v[136:139], v[40:43], v[80:83], v[136:139]\n\t"
      "v_lshlrev_b32 v28, 16, v26\n\t"
      "s_waitcnt lgkmcnt(3)\n\t"
      "v_mfma_f32_16x16x32_bf16 v[136:139], v[48:51], v[84:87], v[136:139]\n\t"
      "v_and_b32 v29, 0xffff0000, v26\n\t"
      "v_lshlrev_b32 v30, 16, v27\n\t"
      "v_mfma_f32_16x16x32_bf16 v[140:143], v[52:55], v[84:87], v[140:143]\n\t"
      "ds_read_b128 v[84:87], v6 offset:16384\n\t"
      "v_and_b32 v31, 0xffff0000, v27\n\t"
      "v_mfma_f32_16x16x32_bf16 v[140:143], v[44:47], v[80:83], v[140:143]\n\t"
      "ds_read_b128 v[80:83], v7 offset:16384\n\t"
      "v_mul_f32 v20, 0x4b800000, v28\n\t"
      "v_mfma_f32_16x16x32_bf16 v[148:151], v[44:47], v[132:135], v[148:151]\n\t"
      "v_mul_f32 v24, 0x49800000, v28\n\t"
      "v_mul_f32 v22, v28, v30\n\t"
      "v_mfma_f32_16x16x32_bf16 v[148:151], v[52:55], v[128:131], v[148:151]\n\t"
      "v_mul_f32 v21, 0x4b800000, v29\n\t"
      "v_mul_f32 v25, 0x49800000, v29\n\t"
      "v_mfma_f32_16x16x32_bf16 v[144:147], v[48:51], v[128:131], v[144:147]\n\t"
      "ds_read_b128 v[128:131], v6 offset:18432\n\t"
      "v_mul_f32 v23, v29, v31\n\t"
      "v_mfma_f32_16x16x32_bf16 v[144:147], v[40:43], v[132:135], v[144:147]\n\t"
      "ds_read_b128 v[132:135], v7 offset:18432\n\t"
      "ds_read2st64_b32 v[26:27], v11 offset0:6 offset1:22\n\t"
      "v_mfma_f32_16x16x32_bf16 v[152:155], v[40:43], v[72:75], v[152:155]\n\t"
      "v_lshrrev_b32 v31, 8, v16\n\t"
      "v_and_b32 v28, v13, v16\n\t"
      "v_mfma_f32_16x16x32_bf16 v[152:155], v[48:51], v[76:79], v[152:155]\n\t"
      "v_and_b32 v29, v14, v16\n\t"
      "v_and_b32 v30, v13, v31\n\t"
      "v_mfma_f32_16x16x32_bf16 v[156:159], v[52:55], v[76:79], v[156:159]\n\t"
      "ds_read_b128 v[76:79], v6 offset:20480\n\t"
      "v_and_b32 v31, v14, v31\n\t"
      "v_mfma_f32_16x16x32_bf16 v[156:159], v[44:47], v[72:75], v[156:159]\n\t"
      "ds_read_b128 v[72:75], v7 offset:20480\n\t"
      "v_fma_mix_f32 v32, v28, v20, -v22 op_sel:[0,0,0] op_sel_hi:[1,0,0]\n\t"
      "v_mfma_f32_16x16x32_bf16 v[164:167], v[44:47], v[124:127], v[164:167]\n\t"
      "v_fma_mix_f32 v33, v29, v24, -v22 op_sel:[0,0,0] op_sel_hi:[1,0,0]\n\t"
      "v_fma_mix_f32 v34, v30, v20, -v22 op_sel:[0,0,0] op_sel_hi:[1,0,0]\n\t"
      "v_mfma_f32_16x16x32_bf16 v[164:167], v[52:55], v[120:123], v[164:167]\n\t"
      "v_fma_mix_f32 v35, v31, v24, -v22 op_sel:[0,0,0] op_sel_hi:[1,0,0]\n\t"
      "v_fma_mix_f32 v36, v28, v20, -v22 op_sel:[1,0,0] op_sel_hi:[1,0,0]\n\t"
      "v_mfma_f32_16x16x32_bf16 v[160:163], v[48:51], v[120:123], v[160:163]\n\t"
      "ds_read_b128 v[120:123], v6 offset:22528\n\t"
      "v_fma_mix_f32 v37, v29, v24, -v22 op_sel:[1,0,0] op_sel_hi:[1,0,0]\n\t"
      "v_mfma_f32_16x16x32_bf16 v[160:163], v[40:43], v[124:127], v[160:163]\n\t"
      "ds_read_b128 v[124:127], v7 offset:22528\n\t"
      "v_cvt_pk_bf16_f32 v56, v32, v33\n\t"
      "v_mfma_f32_16x16x32_bf16 v[168:171], v[40:43], v[116:119], v[168:171]\n\t"
      "v_cvt_pk_bf16_f32 v57, v34, v35\n\t"
      "v_fma_mix_f32 v38, v30, v20, -v22 op_sel:[1,0,0] op_sel_hi:[1,0,0]\n\t"
      "v_mfma_f32_16x16x32_bf16 v[168:171], v[48:51], v[112:115], v[168:171]\n\t"
      "v_fma_mix_f32 v39, v31, v24, -v22 op_sel:[1,0,0] op_sel_hi:[1,0,0]\n\t"
      "v_cvt_pk_bf16_f32 v58, v36, v37\n\t"
      "v_mfma_f32_16x16x32_bf16 v[172:175], v[52:55], v[112:115], v[172:175]\n\t"
      "ds_read_b128 v[112:115], v6 offset:24576\n\t"
      "v_cvt_pk_bf16_f32 v59, v38, v39\n\t"
      "v_mfma_f32_16x16x32_bf16 v[172:175], v[44:47], v[116:119], v[172:175]\n\t"
      "ds_read_b128 v[116:119], v7 offset:24576\n\t"
      "v_lshrrev_b32 v31, 8, v17\n\t"
      "v_mfma_f32_16x16x32_bf16 v[180:183], v[44:47], v[108:111], v[180:183]\n\t"
      "v_and_b32 v28, v13, v17\n\t"
      "v_and_b32 v29, v14, v17\n\t"
      "v_mfma_f32_16x16x32_bf16 v[180:183], v[52:55], v[104:107], v[180:183]\n\t"
      "v_and_b32 v30, v13, v31\n\t"
      "v_and_b32 v31, v14, v31\n\t"
      "v_mfma_f32_16x16x32_bf16 v[176:179], v[48:51], v[104:107], v[176:179]\n\t"
      "ds_read_b128 v[104:107], v6 offset:26624\n\t"
      "ds_read_b64 v[16:17], v10 offset:0\n\t"
      "v_mfma_f32_16x16x32_bf16 v[176:179], v[40:43], v[108:111], v[176:179]\n\t"
      "ds_read_b128 v[108:111], v7 offset:26624\n\t"
      "v_fma_mix_f32 v32, v28, v21, -v23 op_sel:[0,0,0] op_sel_hi:[1,0,0]\n\t"
      "v_mfma_f32_16x16x32_bf16 v[184:187], v[40:43], v[100:103], v[184:187]\n\t"
      "v_fma_mix_f32 v33, v29, v25, -v23 op_sel:[0,0,0] op_sel_hi:[1,0,0]\n\t"
      "s_waitcnt lgkmcnt(4)\n\t"
      "v_mfma_f32_16x16x32_bf16 v[184:187], v[48:51], v[96:99], v[184:187]\n\t"
      "v_fma_mix_f32 v34, v30, v21, -v23 op_sel:[0,0,0] op_sel_hi:[1,0,0]\n\t"
      "v_fma_mix_f32 v35, v31, v25, -v23 op_sel:[0,0,0] op_sel_hi:[1,0,0]\n\t"
      "v_mfma_f32_16x16x32_bf16 v[188:191], v[52:55], v[96:99], v[188:191]\n\t"
      "ds_read_b128 v[96:99], v6 offset:28672\n\t"
      "v_fma_mix_f32 v36, v28, v21, -v23 op_sel:[1,0,0] op_sel_hi:[1,0,0]\n\t"
      "v_mfma_f32_16x16x32_bf16 v[188:191], v[44:47], v[100:103], v[188:191]\n\t"
      "ds_read_b128 v[100:103], v7 offset:28672\n\t"
      "v_fma_mix_f32 v37, v29, v25, -v23 op_sel:[1,0,0] op_sel_hi:[1,0,0]\n\t"
      "v_mfma_f32_16x16x32_bf16 v[196:199], v[44:47], v[92:95], v[196:199]\n\t"
      "v_cvt_pk_bf16_f32 v60, v32, v33\n\t"
      "v_cvt_pk_bf16_f32 v61, v34, v35\n\t"
      "v_mfma_f32_16x16x32_bf16 v[196:199], v[52:55], v[88:91], v[196:199]\n\t"
      "s_barrier\n\t"
      "v_mfma_f32_16x16x32_bf16 v[192:195], v[48:51], v[88:91], v[192:195]\n\t"
      "ds_read_b128 v[88:91], v6 offset:32768\n\t"
      "v_fma_mix_f32 v38, v30, v21, -v23 op_sel:[1,0,0] op_sel_hi:[1,0,0]\n\t"
      "v_mfma_f32_16x16x32_bf16 v[192:195], v[40:43], v[92:95], v[192:195]\n\t"
      "ds_read_b128 v[92:95], v7 offset:32768\n\t"
      "v_fma_mix_f32 v39, v31, v25, -v23 op_sel:[1,0,0] op_sel_hi:[1,0,0]\n\t"
      "v_mfma_f32_16x16x32_bf16 v[200:203], v[40:43], v[84:87], v[200:203]\n\t"
      "v_cvt_pk_bf16_f32 v62, v36, v37\n\t"
      "v_cvt_pk_bf16_f32 v63, v38, v39\n\t"
      "v_mfma_f32_16x16x32_bf16 v[200:203], v[48:51], v[80:83], v[200:203]\n\t"
      "v_lshrrev_b32 v31, 8, v18\n\t"
      "v_and_b32 v28, v13, v18\n\t"
      "v_mfma_f32_16x16x32_bf16 v[204:207], v[52:55], v[80:83], v[204:207]\n\t"
      "ds_read_b128 v[80:83], v6 offset:34816\n\t"
      "v_and_b32 v29, v14, v18\n\t"
      "v_mfma_f32_16x16x32_bf16 v[204:207], v[44:47], v[84:87], v[204:207]\n\t"
      "ds_read_b128 v[84:87], v7 offset:34816\n\t"
      "v_and_b32 v30, v13, v31\n\t"
      "v_mfma_f32_16x16x32_bf16 v[212:215], v[44:47], v[128:131], v[212:215]\n\t"
      "v_and_b32 v31, v14, v31\n\t"
      "v_fma_mix_f32 v32, v28, v20, -v22 op_sel:[0,0,0] op_sel_hi:[1,0,0]\n\t"
      "v_mfma_f32_16x16x32_bf16 v[212:215], v[52:55], v[132:135], v[212:215]\n\t"
      "v_fma_mix_f32 v33, v29, v24, -v22 op_sel:[0,0,0] op_sel_hi:[1,0,0]\n\t"
      "v_fma_mix_f32 v34, v30, v20, -v22 op_sel:[0,0,0] op_sel_hi:[1,0,0]\n\t"
      "v_mfma_f32_16x16x32_bf16 v[208:211], v[48:51], v[132:135], v[208:211]\n\t"
      "ds_read_b128 v[132:135], v6 offset:36864\n\t"
      "v_fma_mix_f32 v35, v31, v24, -v22 op_sel:[0,0,0] op_sel_hi:[1,0,0]\n\t"
      "v_mfma_f32_16x16x32_bf16 v[208:211], v[40:43], v[128:131], v[208:211]\n\t"
      "ds_read_b128 v[128:131], v7 offset:36864\n\t"
      "v_fma_mix_f32 v36, v28, v20, -v22 op_sel:[1,0,0] op_sel_hi:[1,0,0]\n\t"
      "v_mfma_f32_16x16x32_bf16 v[216:219], v[40:43], v[76:79], v[216:219]\n\t"
      "v_fma_mix_f32 v37, v29, v24, -v22 op_sel:[1,0,0] op_sel_hi:[1,0,0]\n\t"
      "v_cvt_pk_bf16_f32 v64, v32, v33\n\t"
      "v_mfma_f32_16x16x32_bf16 v[216:219], v[48:51], v[72:75], v[216:219]\n\t"
      "v_cvt_pk_bf16_f32 v65, v34, v35\n\t"
      "v_fma_mix_f32 v38, v30, v20, -v22 op_sel:[1,0,0] op_sel_hi:[1,0,0]\n\t"
      "v_mfma_f32_16x16x32_bf16 v[220:223], v[52:55], v[72:75], v[220:223]\n\t"
      "ds_read_b128 v[72:75], v6 offset:38912\n\t"
      "v_fma_mix_f32 v39, v31, v24, -v22 op_sel:[1,0,0] op_sel_hi:[1,0,0]\n\t"
      "v_mfma_f32_16x16x32_bf16 v[220:223], v[44:47], v[76:79], v[220:223]\n\t"
      "ds_read_b128 v[76:79], v7 offset:38912\n\t"
      "v_cvt_pk_bf16_f32 v66, v36, v37\n\t"
      "v_mfma_f32_16x16x32_bf16 v[228:231], v[44:47], v[120:123], v[228:231]\n\t"
      "v_cvt_pk_bf16_f32 v67, v38, v39\n\t"
      "v_lshrrev_b32 v31, 8, v19\n\t"
      "v_mfma_f32_16x16x32_bf16 v[228:231], v[52:55], v[124:127], v[228:231]\n\t"
      "v_and_b32 v28, v13, v19\n\t"
      "v_and_b32 v29, v14, v19\n\t"
      "v_mfma_f32_16x16x32_bf16 v[224:227], v[48:51], v[124:127], v[224:227]\n\t"
      "ds_read_b128 v[124:127], v6 offset:40960\n\t"
      "v_and_b32 v30, v13, v31\n\t"
      "v_mfma_f32_16x16x32_bf16 v[224:227], v[40:43], v[120:123], v[224:227]\n\t"
      "ds_read_b128 v[120:123], v7 offset:40960\n\t"
      "v_and_b32 v31, v14, v31\n\t"
      "v_mfma_f32_16x16x32_bf16 v[232:235], v[40:43], v[112:115], v[232:235]\n\t"
      "ds_read_b64 v[18:19], v10 offset:2048\n\t"
      "s_waitcnt lgkmcnt(4)\n\t"
      "v_mfma_f32_16x16x32_bf16 v[232:235], v[48:51], v[116:119], v[232:235]\n\t"
      "v_fma_mix_f32 v32, v28, v21, -v23 op_sel:[0,0,0] op_sel_hi:[1,0,0]\n\t"
      "v_fma_mix_f32 v33, v29, v25, -v23 op_sel:[0,0,0] op_sel_hi:[1,0,0]\n\t"
      "v_mfma_f32_16x16x32_bf16 v[236:239], v[52:55], v[116:119], v[236:239]\n\t"
      "ds_read_b128 v[116:119], v6 offset:43008\n\t"
      "v_fma_mix_f32 v34, v30, v21, -v23 op_sel:[0,0,0] op_sel_hi:[1,0,0]\n\t"
      "v_mfma_f32_16x16x32_bf16 v[236:239], v[44:47], v[112:115], v[236:239]\n\t"
      "ds_read_b128 v[112:115], v7 offset:43008\n\t"
      "v_fma_mix_f32 v35, v31, v25, -v23 op_sel:[0,0,0] op_sel_hi:[1,0,0]\n\t"
      "v_mfma_f32_16x16x32_bf16 v[244:247], v[44:47], v[104:107], v[244:247]\n\t"
      "v_fma_mix_f32 v36, v28, v21, -v23 op_sel:[1,0,0] op_sel_hi:[1,0,0]\n\t"
      "v_fma_mix_f32 v37, v29, v25, -v23 op_sel:[1,0,0] op_sel_hi:[1,0,0]\n\t"
      "v_mfma_f32_16x16x32_bf16 v[244:247], v[52:55], v[108:111], v[244:247]\n\t"
      "v_cvt_pk_bf16_f32 v68, v32, v33\n\t"
      "v_cvt_pk_bf16_f32 v69, v34, v35\n\t"
      "v_mfma_f32_16x16x32_bf16 v[240:243], v[48:51], v[108:111], v[240:243]\n\t"
      "ds_read_b128 v[108:111], v6 offset:45056\n\t"
      "v_fma_mix_f32 v38, v30, v21, -v23 op_sel:[1,0,0] op_sel_hi:[1,0,0]\n\t"
      "v_mfma_f32_16x16x32_bf16 v[240:243], v[40:43], v[104:107], v[240:243]\n\t"
      "ds_read_b128 v[104:107], v7 offset:45056\n\t"
      "v_fma_mix_f32 v39, v31, v25, -v23 op_sel:[1,0,0] op_sel_hi:[1,0,0]\n\t"
      "v_mfma_f32_16x16x32_bf16 v[248:251], v[40:43], v[96:99], v[248:251]\n\t"
      "v_cvt_pk_bf16_f32 v70, v36, v37\n\t"
      "v_cvt_pk_bf16_f32 v71, v38, v39\n\t"
      "v_mfma_f32_16x16x32_bf16 v[248:251], v[48:51], v[100:103], v[248:251]\n\t"
      "v_mfma_f32_16x16x32_bf16 v[252:255], v[52:55], v[100:103], v[252:255]\n\t"
      "ds_read_b128 v[100:103], v6 offset:47104\n\t"
      "v_mfma_f32_16x16x32_bf16 v[252:255], v[44:47], v[96:99], v[252:255]\n\t"
      "ds_read_b128 v[96:99], v7 offset:47104\n\t"
      "v_mfma_f32_16x16x32_bf16 v[140:143], v[60:63], v[88:91], v[140:143]\n\t"
      "v_lshlrev_b32 v28, 16, v26\n\t"
      "v_and_b32 v29, 0xffff0000, v26\n\t"
      "v_mfma_f32_16x16x32_bf16 v[140:143], v[68:71], v[92:95], v[140:143]\n\t"
      "v_lshlrev_b32 v30, 16, v27\n\t"
      "v_and_b32 v31, 0xffff0000, v27\n\t"
      "v_mfma_f32_16x16x32_bf16 v[136:139], v[64:67], v[92:95], v[136:139]\n\t"
      "ds_read_b128 v[92:95], v6 offset:49152\n\t"
      "v_mul_f32 v20, 0x4b800000, v28\n\t"
      "v_mfma_f32_16x16x32_bf16 v[136:139], v[56:59], v[88:91], v[136:139]\n\t"
      "ds_read_b128 v[88:91], v7 offset:49152\n\t"
      "v_mul_f32 v24, 0x49800000, v28\n\t"
      "v_mfma_f32_16x16x32_bf16 v[144:147], v[56:59], v[80:83], v[144:147]\n\t"
      "v_mul_f32 v22, v28, v30\n\t"
      "v_mul_f32 v21, 0x4b800000, v29\n\t"
      "v_mfma_f32_16x16x32_bf16 v[144:147], v[64:67], v[84:87], v[144:147]\n\t"
      "v_mul_f32 v25, 0x49800000, v29\n\t"
      "v_mul_f32 v23, v29, v31\n\t"
      "v_mfma_f32_16x16x32_bf16 v[148:151], v[68:71], v[84:87], v[148:151]\n\t"
      "ds_read_b128 v[84:87], v6 offset:51200\n\t"
      "ds_read2st64_b32 v[26:27], v11 offset0:7 offset1:23\n\t"
      "v_mfma_f32_16x16x32_bf16 v[148:151], v[60:63], v[80:83], v[148:151]\n\t"
      "ds_read_b128 v[80:83], v7 offset:51200\n\t"
      "v_lshrrev_b32 v31, 8, v16\n\t"
      "v_mfma_f32_16x16x32_bf16 v[156:159], v[60:63], v[132:135], v[156:159]\n\t"
      "v_and_b32 v28, v13, v16\n\t"
      "v_and_b32 v29, v14, v16\n\t"
      "v_mfma_f32_16x16x32_bf16 v[156:159], v[68:71], v[128:131], v[156:159]\n\t"
      "v_and_b32 v30, v13, v31\n\t"
      "v_and_b32 v31, v14, v31\n\t"
      "v_mfma_f32_16x16x32_bf16 v[152:155], v[64:67], v[128:131], v[152:155]\n\t"
      "ds_read_b128 v[128:131], v6 offset:53248\n\t"
      "v_fma_mix_f32 v32, v28, v20, -v22 op_sel:[0,0,0] op_sel_hi:[1,0,0]\n\t"
      "v_mfma_f32_16x16x32_bf16 v[152:155], v[56:59], v[132:135], v[152:155]\n\t"
      "ds_read_b128 v[132:135], v7 offset:53248\n\t"
      "v_fma_mix_f32 v33, v29, v24, -v22 op_sel:[0,0,0] op_sel_hi:[1,0,0]\n\t"
      "v_mfma_f32_16x16x32_bf16 v[160:163], v[56:59], v[72:75], v[160:163]\n\t"
      "v_fma_mix_f32 v34, v30, v20, -v22 op_sel:[0,0,0] op_sel_hi:[1,0,0]\n\t"
      "s_waitcnt lgkmcnt(3)\n\t"
      "v_mfma_f32_16x16x32_bf16 v[160:163], v[64:67], v[76:79], v[160:163]\n\t"
      "v_fma_mix_f32 v35, v31, v24, -v22 op_sel:[0,0,0] op_sel_hi:[1,0,0]\n\t"
      "v_fma_mix_f32 v36, v28, v20, -v22 op_sel:[1,0,0] op_sel_hi:[1,0,0]\n\t"
      "v_mfma_f32_16x16x32_bf16 v[164:167], v[68:71], v[76:79], v[164:167]\n\t"
      "ds_read_b128 v[76:79], v6 offset:55296\n\t"
      "v_fma_mix_f32 v37, v29, v24, -v22 op_sel:[1,0,0] op_sel_hi:[1,0,0]\n\t"
      "v_mfma_f32_16x16x32_bf16 v[164:167], v[60:63], v[72:75], v[164:167]\n\t"
      "ds_read_b128 v[72:75], v7 offset:55296\n\t"
      "v_cvt_pk_bf16_f32 v40, v32, v33\n\t"
      "v_mfma_f32_16x16x32_bf16 v[172:175], v[60:63], v[124:127], v[172:175]\n\t"
      "v_cvt_pk_bf16_f32 v41, v34, v35\n\t"
      "v_fma_mix_f32 v38, v30, v20, -v22 op_sel:[1,0,0] op_sel_hi:[1,0,0]\n\t"
      "v_mfma_f32_16x16x32_bf16 v[172:175], v[68:71], v[120:123], v[172:175]\n\t"
      "v_fma_mix_f32 v39, v31, v24, -v22 op_sel:[1,0,0] op_sel_hi:[1,0,0]\n\t"
      "v_cvt_pk_bf16_f32 v42, v36, v37\n\t"
      "v_mfma_f32_16x16x32_bf16 v[168:171], v[64:67], v[120:123], v[168:171]\n\t"
      "ds_read_b128 v[120:123], v6 offset:57344\n\t"
      "v_cvt_pk_bf16_f32 v43, v38, v39\n\t"
      "v_mfma_f32_16x16x32_bf16 v[168:171], v[56:59], v[124:127], v[168:171]\n\t"
      "ds_read_b128 v[124:127], v7 offset:57344\n\t"
      "v_lshrrev_b32 v31, 8, v17\n\t"
      "v_mfma_f32_16x16x32_bf16 v[176:179], v[56:59], v[116:119], v[176:179]\n\t"
      "v_and_b32 v28, v13, v17\n\t"
      "v_and_b32 v29, v14, v17\n\t"
      "v_mfma_f32_16x16x32_bf16 v[176:179], v[64:67], v[112:115], v[176:179]\n\t"
      "v_and_b32 v30, v13, v31\n\t"
      "v_and_b32 v31, v14, v31\n\t"
      "v_mfma_f32_16x16x32_bf16 v[180:183], v[68:71], v[112:115], v[180:183]\n\t"
      "ds_read_b128 v[112:115], v6 offset:59392\n\t"
      "ds_read_b64 v[16:17], v10 offset:4096\n\t"
      "v_mfma_f32_16x16x32_bf16 v[180:183], v[60:63], v[116:119], v[180:183]\n\t"
      "ds_read_b128 v[116:119], v7 offset:59392\n\t"
      "v_fma_mix_f32 v32, v28, v21, -v23 op_sel:[0,0,0] op_sel_hi:[1,0,0]\n\t"
      "v_mfma_f32_16x16x32_bf16 v[188:191], v[60:63], v[108:111], v[188:191]\n\t"
      "v_fma_mix_f32 v33, v29, v25, -v23 op_sel:[0,0,0] op_sel_hi:[1,0,0]\n\t"
      "v_fma_mix_f32 v34, v30, v21, -v23 op_sel:[0,0,0] op_sel_hi:[1,0,0]\n\t"
      "v_mfma_f32_16x16x32_bf16 v[188:191], v[68:71], v[104:107], v[188:191]\n\t"
      "v_fma_mix_f32 v35, v31, v25, -v23 op_sel:[0,0,0] op_sel_hi:[1,0,0]\n\t"
      "v_fma_mix_f32 v36, v28, v21, -v23 op_sel:[1,0,0] op_sel_hi:[1,0,0]\n\t"
      "v_mfma_f32_16x16x32_bf16 v[184:187], v[64:67], v[104:107], v[184:187]\n\t"
      "ds_read_b128 v[104:107], v6 offset:61440\n\t"
      "v_fma_mix_f32 v37, v29, v25, -v23 op_sel:[1,0,0] op_sel_hi:[1,0,0]\n\t"
      "v_mfma_f32_16x16x32_bf16 v[184:187], v[56:59], v[108:111], v[184:187]\n\t"
      "ds_read_b128 v[108:111], v7 offset:61440\n\t"
      "v_cvt_pk_bf16_f32 v44, v32, v33\n\t"
      "v_mfma_f32_16x16x32_bf16 v[192:195], v[56:59], v[100:103], v[192:195]\n\t"
      "v_cvt_pk_bf16_f32 v45, v34, v35\n\t"
      "v_fma_mix_f32 v38, v30, v21, -v23 op_sel:[1,0,0] op_sel_hi:[1,0,0]\n\t"
      "v_mfma_f32_16x16x32_bf16 v[192:195], v[64:67], v[96:99], v[192:195]\n\t"
      "s_barrier\n\t"
      "v_mfma_f32_16x16x32_bf16 v[196:199], v[68:71], v[96:99], v[196:199]\n\t"
      "ds_read_b128 v[96:99], v8 offset:0\n\t"
      "v_fma_mix_f32 v39, v31, v25, -v23 op_sel:[1,0,0] op_sel_hi:[1,0,0]\n\t"
      "v_mfma_f32_16x16x32_bf16 v[196:199], v[60:63], v[100:103], v[196:199]\n\t"
      "ds_read_b128 v[100:103], v9 offset:0\n\t"
      "v_cvt_pk_bf16_f32 v46, v36, v37\n\t"
      "v_mfma_f32_16x16x32_bf16 v[204:207], v[60:63], v[92:95], v[204:207]\n\t"
      "v_cvt_pk_bf16_f32 v47, v38, v39\n\t"
      "v_lshrrev_b32 v31, 8, v18\n\t"
      "v_mfma_f32_16x16x32_bf16 v[204:207], v[68:71], v[88:91], v[204:207]\n\t"
      "v_and_b32 v28, v13, v18\n\t"
      "v_and_b32 v29, v14, v18\n\t"
      "v_mfma_f32_16x16x32_bf16 v[200:203], v[64:67], v[88:91], v[200:203]\n\t"
      "ds_read_b128 v[88:91], v8 offset:2048\n\t"
      "v_and_b32 v30, v13, v31\n\t"
      "v_mfma_f32_16x16x32_bf16 v[200:203], v[56:59], v[92:95], v[200:203]\n\t"
      "ds_read_b128 v[92:95], v9 offset:2048\n\t"
      "v_and_b32 v31, v14, v31\n\t"
      "v_mfma_f32_16x16x32_bf16 v[208:211], v[56:59], v[84:87], v[208:211]\n\t"
      "v_fma_mix_f32 v32, v28, v20, -v22 op_sel:[0,0,0] op_sel_hi:[1,0,0]\n\t"
      "s_waitcnt lgkmcnt(3)\n\t"
      "v_mfma_f32_16x16x32_bf16 v[208:211], v[64:67], v[80:83], v[208:211]\n\t"
      "v_fma_mix_f32 v33, v29, v24, -v22 op_sel:[0,0,0] op_sel_hi:[1,0,0]\n\t"
      "v_fma_mix_f32 v34, v30, v20, -v22 op_sel:[0,0,0] op_sel_hi:[1,0,0]\n\t"
      "v_mfma_f32_16x16x32_bf16 v[212:215], v[68:71], v[80:83], v[212:215]\n\t"
      "ds_read_b128 v[80:83], v8 offset:4096\n\t"
      "v_fma_mix_f32 v35, v31, v24, -v22 op_sel:[0,0,0] op_sel_hi:[1,0,0]\n\t"
      "v_mfma_f32_16x16x32_bf16 v[212:215], v[60:63], v[84:87], v[212:215]\n\t"
      "ds_read_b128 v[84:87], v9 offset:4096\n\t"
      "v_fma_mix_f32 v36, v28, v20, -v22 op_sel:[1,0,0] op_sel_hi:[1,0,0]\n\t"
      "v_mfma_f32_16x16x32_bf16 v[220:223], v[60:63], v[128:131], v[220:223]\n\t"
      "v_fma_mix_f32 v37, v29, v24, -v22 op_sel:[1,0,0] op_sel_hi:[1,0,0]\n\t"
      "v_cvt_pk_bf16_f32 v48, v32, v33\n\t"
      "v_mfma_f32_16x16x32_bf16 v[220:223], v[68:71], v[132:135], v[220:223]\n\t"
      "v_cvt_pk_bf16_f32 v49, v34, v35\n\t"
      "v_fma_mix_f32 v38, v30, v20, -v22 op_sel:[1,0,0] op_sel_hi:[1,0,0]\n\t"
      "v_mfma_f32_16x16x32_bf16 v[216:219], v[64:67], v[132:135], v[216:219]\n\t"
      "ds_read_b128 v[132:135], v8 offset:6144\n\t"
      "v_fma_mix_f32 v39, v31, v24, -v22 op_sel:[1,0,0] op_sel_hi:[1,0,0]\n\t"
      "v_mfma_f32_16x16x32_bf16 v[216:219], v[56:59], v[128:131], v[216:219]\n\t"
      "ds_read_b128 v[128:131], v9 offset:6144\n\t"
      "v_cvt_pk_bf16_f32 v50, v36, v37\n\t"
      "v_mfma_f32_16x16x32_bf16 v[224:227], v[56:59], v[76:79], v[224:227]\n\t"
      "v_cvt_pk_bf16_f32 v51, v38, v39\n\t"
      "v_lshrrev_b32 v31, 8, v19\n\t"
      "v_mfma_f32_16x16x32_bf16 v[224:227], v[64:67], v[72:75], v[224:227]\n\t"
      "v_and_b32 v28, v13, v19\n\t"
      "v_and_b32 v29, v14, v19\n\t"
      "v_mfma_f32_16x16x32_bf16 v[228:231], v[68:71], v[72:75], v[228:231]\n\t"
      "ds_read_b128 v[72:75], v8 offset:8192\n\t"
      "v_and_b32 v30, v13, v31\n\t"
      "v_mfma_f32_16x16x32_bf16 v[228:231], v[60:63], v[76:79], v[228:231]\n\t"
      "ds_read_b128 v[76:79], v9 offset:8192\n\t"
      "v_and_b32 v31, v14, v31\n\t"
      "v_mfma_f32_16x16x32_bf16 v[236:239], v[60:63], v[120:123], v[236:239]\n\t"
      "ds_read_b64 v[18:19], v10 offset:6144\n\t"
      "v_fma_mix_f32 v32, v28, v21, -v23 op_sel:[0,0,0] op_sel_hi:[1,0,0]\n\t"
      "v_mfma_f32_16x16x32_bf16 v[236:239], v[68:71], v[124:127], v[236:239]\n\t"
      "v_fma_mix_f32 v33, v29, v25, -v23 op_sel:[0,0,0] op_sel_hi:[1,0,0]\n\t"
      "v_fma_mix_f32 v34, v30, v21, -v23 op_sel:[0,0,0] op_sel_hi:[1,0,0]\n\t"
      "v_mfma_f32_16x16x32_bf16 v[232:235], v[64:67], v[124:127], v[232:235]\n\t"
      "ds_read_b128 v[124:127], v8 offset:10240\n\t"
      "v_fma_mix_f32 v35, v31, v25, -v23 op_sel:[0,0,0] op_sel_hi:[1,0,0]\n\t"
      "v_mfma_f32_16x16x32_bf16 v[232:235], v[56:59], v[120:123], v[232:235]\n\t"
      "ds_read_b128 v[120:123], v9 offset:10240\n\t"
      "v_fma_mix_f32 v36, v28, v21, -v23 op_sel:[1,0,0] op_sel_hi:[1,0,0]\n\t"
      "v_mfma_f32_16x16x32_bf16 v[240:243], v[56:59], v[112:115], v[240:243]\n\t"
      "v_fma_mix_f32 v37, v29, v25, -v23 op_sel:[1,0,0] op_sel_hi:[1,0,0]\n\t"
      "v_cvt_pk_bf16_f32 v52, v32, v33\n\t"
      "v_mfma_f32_16x16x32_bf16 v[240:243], v[64:67], v[116:119], v[240:243]\n\t"
      "v_cvt_pk_bf16_f32 v53, v34, v35\n\t"
      "v_fma_mix_f32 v38, v30, v21, -v23 op_sel:[1,0,0] op_sel_hi:[1,0,0]\n\t"
      "v_mfma_f32_16x16x32_bf16 v[244:247], v[68:71], v[116:119], v[244:247]\n\t"
      "ds_read_b128 v[116:119], v8 offset:12288\n\t"
      "v_fma_mix_f32 v39, v31, v25, -v23 op_sel:[1,0,0] op_sel_hi:[1,0,0]\n\t"
      "v_mfma_f32_16x16x32_bf16 v[244:247], v[60:63], v[112:115], v[244:247]\n\t"
      "ds_read_b128 v[112:115], v9 offset:12288\n\t"
      "v_cvt_pk_bf16_f32 v54, v36, v37\n\t"
      "v_mfma_f32_16x16x32_bf16 v[252:255], v[60:63], v[104:107], v[252:255]\n\t"
      "v_cvt_pk_bf16_f32 v55, v38, v39\n\t"
      "v_mfma_f32_16x16x32_bf16 v[252:255], v[68:71], v[108:111], v[252:255]\n\t"
      "v_mfma_f32_16x16x32_bf16 v[248:251], v[64:67], v[108:111], v[248:251]\n\t"
      "ds_read_b128 v[108:111], v8 offset:14336\n\t"
      "v_mfma_f32_16x16x32_bf16 v[248:251], v[56:59], v[104:107], v[248:251]\n\t"
      "ds_read_b128 v[104:107], v9 offset:14336\n\t"
      "v_mfma_f32_16x16x32_bf16 v[136:139], v[40:43], v[96:99], v[136:139]\n\t"
      "v_lshlrev_b32 v28, 16, v26\n\t"
      "s_waitcnt lgkmcnt(3)\n\t"
      "v_mfma_f32_16x16x32_bf16 v[136:139], v[48:51], v[100:103], v[136:139]\n\t"
      "v_and_b32 v29, 0xffff0000, v26\n\t"
      "v_lshlrev_b32 v30, 16, v27\n\t"
      "v_mfma_f32_16x16x32_bf16 v[140:143], v[52:55], v[100:103], v[140:143]\n\t"
      "ds_read_b128 v[100:103], v8 offset:16384\n\t"
      "v_and_b32 v31, 0xffff0000, v27\n\t"
      "v_mfma_f32_16x16x32_bf16 v[140:143], v[44:47], v[96:99], v[140:143]\n\t"
      "ds_read_b128 v[96:99], v9 offset:16384\n\t"
      "v_mul_f32 v20, 0x4b800000, v28\n\t"
      "v_mfma_f32_16x16x32_bf16 v[148:151], v[44:47], v[88:91], v[148:151]\n\t"
      "v_mul_f32 v24, 0x49800000, v28\n\t"
      "v_mul_f32 v22, v28, v30\n\t"
      "v_mfma_f32_16x16x32_bf16 v[148:151], v[52:55], v[92:95], v[148:151]\n\t"
      "v_mul_f32 v21, 0x4b800000, v29\n\t"
      "v_mul_f32 v25, 0x49800000, v29\n\t"
      "v_mfma_f32_16x16x32_bf16 v[144:147], v[48:51], v[92:95], v[144:147]\n\t"
      "ds_read_b128 v[92:95], v8 offset:18432\n\t"
      "v_mul_f32 v23, v29, v31\n\t"
      "v_mfma_f32_16x16x32_bf16 v[144:147], v[40:43], v[88:91], v[144:147]\n\t"
      "ds_read_b128 v[88:91], v9 offset:18432\n\t"
      "ds_read2st64_b32 v[26:27], v11 offset0:8 offset1:24\n\t"
      "v_mfma_f32_16x16x32_bf16 v[152:155], v[40:43], v[80:83], v[152:155]\n\t"
      "v_lshrrev_b32 v31, 8, v16\n\t"
      "v_and_b32 v28, v13, v16\n\t"
      "v_mfma_f32_16x16x32_bf16 v[152:155], v[48:51], v[84:87], v[152:155]\n\t"
      "v_and_b32 v29, v14, v16\n\t"
      "v_and_b32 v30, v13, v31\n\t"
      "v_mfma_f32_16x16x32_bf16 v[156:159], v[52:55], v[84:87], v[156:159]\n\t"
      "ds_read_b128 v[84:87], v8 offset:20480\n\t"
      "v_and_b32 v31, v14, v31\n\t"
      "v_mfma_f32_16x16x32_bf16 v[156:159], v[44:47], v[80:83], v[156:159]\n\t"
      "ds_read_b128 v[80:83], v9 offset:20480\n\t"
      "v_fma_mix_f32 v32, v28, v20, -v22 op_sel:[0,0,0] op_sel_hi:[1,0,0]\n\t"
      "v_mfma_f32_16x16x32_bf16 v[164:167], v[44:47], v[132:135], v[164:167]\n\t"
      "v_fma_mix_f32 v33, v29, v24, -v22 op_sel:[0,0,0] op_sel_hi:[1,0,0]\n\t"
      "v_fma_mix_f32 v34, v30, v20, -v22 op_sel:[0,0,0] op_sel_hi:[1,0,0]\n\t"
      "s_nop 0\n\t"
      "LH15x2_%=:\n\t"
      "s_cbranch_execz LHRET_%=\n\t"
      "v_mfma_f32_16x16x32_bf16 v[164:167], v[52:55], v[128:131], v[164:167]\n\t"
      "v_fma_mix_f32 v35, v31, v24, -v22 op_sel:[0,0,0] op_sel_hi:[1,0,0]\n\t"
      "v_fma_mix_f32 v36, v28, v20, -v22 op_sel:[1,0,0] op_sel_hi:[1,0,0]\n\t"
      "v_mfma_f32_16x16x32_bf16 v[160:163], v[48:51], v[128:131], v[160:163]\n\t"
      "ds_read_b128 v[128:131], v8 offset:22528\n\t"
      "v_fma_mix_f32 v37, v29, v24, -v22 op_sel:[1,0,0] op_sel_hi:[1,0,0]\n\t"
      "v_mfma_f32_16x16x32_bf16 v[160:163], v[40:43], v[132:135], v[160:163]\n\t"
      "ds_read_b128 v[132:135], v9 offset:22528\n\t"
      "v_cvt_pk_bf16_f32 v56, v32, v33\n\t"
      "v_mfma_f32_16x16x32_bf16 v[168:171], v[40:43], v[72:75], v[168:171]\n\t"
      "v_cvt_pk_bf16_f32 v57, v34, v35\n\t"
      "v_fma_mix_f32 v38, v30, v20, -v22 op_sel:[1,0,0] op_sel_hi:[1,0,0]\n\t"
      "v_mfma_f32_16x16x32_bf16 v[168:171], v[48:51], v[76:79], v[168:171]\n\t"
      "v_fma_mix_f32 v39, v31, v24, -v22 op_sel:[1,0,0] op_sel_hi:[1,0,0]\n\t"
      "v_cvt_pk_bf16_f32 v58, v36, v37\n\t"
      "v_mfma_f32_16x16x32_bf16 v[172:175], v[52:55], v[76:79], v[172:175]\n\t"
      "ds_read_b128 v[76:79], v8 offset:24576\n\t"
      "v_cvt_pk_bf16_f32 v59, v38, v39\n\t"
      "v_mfma_f32_16x16x32_bf16 v[172:175], v[44:47], v[72:75], v[172:175]\n\t"
      "ds_read_b128 v[72:75], v9 offset:24576\n\t"
      "v_lshrrev_b32 v31, 8, v17\n\t"
      "v_mfma_f32_16x16x32_bf16 v[180:183], v[44:47], v[124:127], v[180:183]\n\t"
      "v_and_b32 v28, v13, v17\n\t"
      "v_and_b32 v29, v14, v17\n\t"
      "v_mfma_f32_16x16x32_bf16 v[180:183], v[52:55], v[120:123], v[180:183]\n\t"
      "v_and_b32 v30, v13, v31\n\t"
      "v_and_b32 v31, v14, v31\n\t"
      "v_mfma_f32_16x16x32_bf16 v[176:179], v[48:51], v[120:123], v[176:179]\n\t"
      "ds_read_b128 v[120:123], v8 offset:26624\n\t"
      "ds_read_b64 v[16:17], v10 offset:8192\n\t"
      "v_mfma_f32_16x16x32_bf16 v[176:179], v[40:43], v[124:127], v[176:179]\n\t"
      "ds_read_b128 v[124:127], v9 offset:26624\n\t"
      "v_fma_mix_f32 v32, v28, v21, -v23 op_sel:[0,0,0] op_sel_hi:[1,0,0]\n\t"
      "v_mfma_f32_16x16x32_bf16 v[184:187], v[40:43], v[116:119], v[184:187]\n\t"
      "v_fma_mix_f32 v33, v29, v25, -v23 op_sel:[0,0,0] op_sel_hi:[1,0,0]\n\t"
      "s_waitcnt lgkmcnt(4)\n\t"
      "v_mfma_f32_16x16x32_bf16 v[184:187], v[48:51], v[112:115], v[184:187]\n\t"
      "v_fma_mix_f32 v34, v30, v21, -v23 op_sel:[0,0,0] op_sel_hi:[1,0,0]\n\t"
      "v_fma_mix_f32 v35, v31, v25, -v23 op_sel:[0,0,0] op_sel_hi:[1,0,0]\n\t"
      "v_mfma_f32_16x16x32_bf16 v[188:191], v[52:55], v[112:115], v[188:191]\n\t"
      "ds_read_b128 v[112:115], v8 offset:28672\n\t"
      "v_fma_mix_f32 v36, v28, v21, -v23 op_sel:[1,0,0] op_sel_hi:[1,0,0]\n\t"
      "v_mfma_f32_16x16x32_bf16 v[188:191], v[44:47], v[116:119], v[188:191]\n\t"
      "ds_read_b128 v[116:119], v9 offset:28672\n\t"
      "v_fma_mix_f32 v37, v29, v25, -v23 op_sel:[1,0,0] op_sel_hi:[1,0,0]\n\t"
      "v_mfma_f32_16x16x32_bf16 v[196:199], v[44:47], v[108:111], v[196:199]\n\t"
      "v_cvt_pk_bf16_f32 v60, v32, v33\n\t"
      "v_cvt_pk_bf16_f32 v61, v34, v35\n\t"
      "v_mfma_f32_16x16x32_bf16 v[196:199], v[52:55], v[104:107], v[196:199]\n\t"
      "s_barrier\n\t"
      "v_mfma_f32_16x16x32_bf16 v[192:195], v[48:51], v[104:107], v[192:195]\n\t"
      "ds_read_b128 v[104:107], v8 offset:32768\n\t"
      "v_fma_mix_f32 v38, v30, v21, -v23 op_sel:[1,0,0] op_sel_hi:[1,0,0]\n\t"
      "v_mfma_f32_16x16x32_bf16 v[192:195], v[40:43], v[108:111], v[192:195]\n\t"
      "ds_read_b128 v[108:111], v9 offset:32768\n\t"
      "v_fma_mix_f32 v39, v31, v25, -v23 op_sel:[1,0,0] op_sel_hi:[1,0,0]\n\t"
      "v_mfma_f32_16x16x32_bf16 v[200:203], v[40:43], v[100:103], v[200:203]\n\t"
      "v_cvt_pk_bf16_f32 v62, v36, v37\n\t"
      "v_cvt_pk_bf16_f32 v63, v38, v39\n\t"
      "v_mfma_f32_16x16x32_bf16 v[200:203], v[48:51], v[96:99], v[200:203]\n\t"
      "v_lshrrev_b32 v31, 8, v18\n\t"
      "v_and_b32 v28, v13, v18\n\t"
      "v_mfma_f32_16x16x32_bf16 v[204:207], v[52:55], v[96:99], v[204:207]\n\t"
      "ds_read_b128 v[96:99], v8 offset:34816\n\t"
      "v_and_b32 v29, v14, v18\n\t"
      "v_mfma_f32_16x16x32_bf16 v[204:207], v[44:47], v[100:103], v[204:207]\n\t"
      "ds_read_b128 v[100:103], v9 offset:34816\n\t"
      "v_and_b32 v30, v13, v31\n\t"
      "v_mfma_f32_16x16x32_bf16 v[212:215], v[44:47], v[92:95], v[212:215]\n\t"
      "v_and_b32 v31, v14, v31\n\t"
      "v_fma_mix_f32 v32, v28, v20, -v22 op_sel:[0,0,0] op_sel_hi:[1,0,0]\n\t"
      "v_mfma_f32_16x16x32_bf16 v[212:215], v[52:55], v[88:91], v[212:215]\n\t"
      "v_fma_mix_f32 v33, v29, v24, -v22 op_sel:[0,0,0] op_sel_hi:[1,0,0]\n\t"
      "v_fma_mix_f32 v34, v30, v20, -v22 op_sel:[0,0,0] op_sel_hi:[1,0,0]\n\t"
      "v_mfma_f32_16x16x32_bf16 v[208:211], v[48:51], v[88:91], v[208:211]\n\t"
      "ds_read_b128 v[88:91], v8 offset:36864\n\t"
      "v_fma_mix_f32 v35, v31, v24, -v22 op_sel:[0,0,0] op_sel_hi:[1,0,0]\n\t"
      "v_mfma_f32_16x16x32_bf16 v[208:211], v[40:43], v[92:95], v[208:211]\n\t"
      "ds_read_b128 v[92:95], v9 offset:36864\n\t"
      "v_fma_mix_f32 v36, v28, v20, -v22 op_sel:[1,0,0] op_sel_hi:[1,0,0]\n\t"
      "v_mfma_f32_16x16x32_bf16 v[216:219], v[40:43], v[84:87], v[216:219]\n\t"
      "v_fma_mix_f32 v37, v29, v24, -v22 op_sel:[1,0,0] op_sel_hi:[1,0,0]\n\t"
      "v_cvt_pk_bf16_f32 v64, v32, v33\n\t"
      "v_mfma_f32_16x16x32_bf16 v[216:219], v[48:51], v[80:83], v[216:219]\n\t"
      "v_cvt_pk_bf16_f32 v65, v34, v35\n\t"
      "v_fma_mix_f32 v38, v30, v20, -v22 op_sel:[1,0,0] op_sel_hi:[1,0,0]\n\t"
      "v_mfma_f32_16x16x32_bf16 v[220:223], v[52:55], v[80:83], v[220:223]\n\t"
      "ds_read_b128 v[80:83], v8 offset:38912\n\t"
      "v_fma_mix_f32 v39, v31, v24, -v22 op_sel:[1,0,0] op_sel_hi:[1,0,0]\n\t"
      "v_mfma_f32_16x16x32_bf16 v[220:223], v[44:47], v[84:87], v[220:223]\n\t"
      "ds_read_b128 v[84:87], v9 offset:38912\n\t"
      "v_cvt_pk_bf16_f32 v66, v36, v37\n\t"
      "v_mfma_f32_16x16x32_bf16 v[228:231], v[44:47], v[128:131], v[228:231]\n\t"
      "v_cvt_pk_bf16_f32 v67, v38, v39\n\t"
      "v_lshrrev_b32 v31, 8, v19\n\t"
      "v_mfma_f32_16x16x32_bf16 v[228:231], v[52:55], v[132:135], v[228:231]\n\t"
      "v_and_b32 v28, v13, v19\n\t"
      "v_and_b32 v29, v14, v19\n\t"
      "v_mfma_f32_16x16x32_bf16 v[224:227], v[48:51], v[132:135], v[224:227]\n\t"
      "ds_read_b128 v[132:135], v8 offset:40960\n\t"
      "v_and_b32 v30, v13, v31\n\t"
      "v_mfma_f32_16x16x32_bf16 v[224:227], v[40:43], v[128:131], v[224:227]\n\t"
      "ds_read_b128 v[128:131], v9 offset:40960\n\t"
      "v_and_b32 v31, v14, v31\n\t"
      "v_mfma_f32_16x16x32_bf16 v[232:235], v[40:43], v[76:79], v[232:235]\n\t"
      "ds_read_b64 v[18:19], v10 offset:10240\n\t"
      "s_waitcnt lgkmcnt(4)\n\t"
      "v_mfma_f32_16x16x32_bf16 v[232:235], v[48:51], v[72:75], v[232:235]\n\t"
      "v_fma_mix_f32 v32, v28, v21, -v23 op_sel:[0,0,0] op_sel_hi:[1,0,0]\n\t"
      "v_fma_mix_f32 v33, v29, v25, -v23 op_sel:[0,0,0] op_sel_hi:[1,0,0]\n\t"
      "v_mfma_f32_16x16x32_bf16 v[236:239], v[52:55], v[72:75], v[236:239]\n\t"
      "ds_read_b128 v[72:75], v8 offset:43008\n\t"
      "v_fma_mix_f32 v34, v30, v21, -v23 op_sel:[0,0,0] op_sel_hi:[1,0,0]\n\t"
      "v_mfma_f32_16x16x32_bf16 v[236:239], v[44:47], v[76:79], v[236:239]\n\t"
      "ds_read_b128 v[76:79], v9 offset:43008\n\t"
      "v_fma_mix_f32 v35, v31, v25, -v23 op_sel:[0,0,0] op_sel_hi:[1,0,0]\n\t"
      "v_mfma_f32_16x16x32_bf16 v[244:247], v[44:47], v[120:123], v[244:247]\n\t"
      "v_fma_mix_f32 v36, v28, v21, -v23 op_sel:[1,0,0] op_sel_hi:[1,0,0]\n\t"
      "v_fma_mix_f32 v37, v29, v25, -v23 op_sel:[1,0,0] op_sel_hi:[1,0,0]\n\t"
      "v_mfma_f32_16x16x32_bf16 v[244:247], v[52:55], v[124:127], v[244:247]\n\t"
      "v_cvt_pk_bf16_f32 v68, v32, v33\n\t"
      "v_cvt_pk_bf16_f32 v69, v34, v35\n\t"
      "v_mfma_f32_16x16x32_bf16 v[240:243], v[48:51], v[124:127], v[240:243]\n\t"
      "ds_read_b128 v[124:127], v8 offset:45056\n\t"
      "v_fma_mix_f32 v38, v30, v21, -v23 op_sel:[1,0,0] op_sel_hi:[1,0,0]\n\t"
      "v_mfma_f32_16x16x32_bf16 v[240:243], v[40:43], v[120:123], v[240:243]\n\t"
      "ds_read_b128 v[120:123], v9 offset:45056\n\t"
      "v_fma_mix_f32 v39, v31, v25, -v23 op_sel:[1,0,0] op_sel_hi:[1,0,0]\n\t"
      "v_mfma_f32_16x16x32_bf16 v[248:251], v[40:43], v[112:115], v[248:251]\n\t"
      "v_cvt_pk_bf16_f32 v70, v36, v37\n\t"
      "v_cvt_pk_bf16_f32 v71, v38, v39\n\t"
      "v_mfma_f32_16x16x32_bf16 v[248:251], v[48:51], v[116:119], v[248:251]\n\t"
      "v_mfma_f32_16x16x32_bf16 v[252:255], v[52:55], v[116:119], v[252:255]\n\t"
      "ds_read_b128 v[116:119], v8 offset:47104\n\t"
      "v_mfma_f32_16x16x32_bf16 v[252:255], v[44:47], v[112:115], v[252:255]\n\t"
      "ds_read_b128 v[112:115], v9 offset:47104\n\t"
      "v_mfma_f32_16x16x32_bf16 v[140:143], v[60:63], v[104:107], v[140:143]\n\t"
      "v_lshlrev_b32 v28, 16, v26\n\t"
      "v_and_b32 v29, 0xffff0000, v26\n\t"
      "v_mfma_f32_16x16x32_bf16 v[140:143], v[68:71], v[108:111], v[140:143]\n\t"
      "v_lshlrev_b32 v30, 16, v27\n\t"
      "v_and_b32 v31, 0xffff0000, v27\n\t"
      "v_mfma_f32_16x16x32_bf16 v[136:139], v[64:67], v[108:111], v[136:139]\n\t"
      "ds_read_b128 v[108:111], v8 offset:49152\n\t"
      "v_mul_f32 v20, 0x4b800000, v28\n\t"
      "v_mfma_f32_16x16x32_bf16 v[136:139], v[56:59], v[104:107], v[136:139]\n\t"
      "ds_read_b128 v[104:107], v9 offset:49152\n\t"
      "v_mul_f32 v24, 0x49800000, v28\n\t"
      "v_mfma_f32_16x16x32_bf16 v[144:147], v[56:59], v[96:99], v[144:147]\n\t"
      "v_mul_f32 v22, v28, v30\n\t"
      "v_mul_f32 v21, 0x4b800000, v29\n\t"
      "v_mfma_f32_16x16x32_bf16 v[144:147], v[64:67], v[100:103], v[144:147]\n\t"
      "v_mul_f32 v25, 0x49800000, v29\n\t"
      "v_mul_f32 v23, v29, v31\n\t"
      "v_mfma_f32_16x16x32_bf16 v[148:151], v[68:71], v[100:103], v[148:151]\n\t"
      "ds_read_b128 v[100:103], v8 offset:51200\n\t"
      "ds_read2st64_b32 v[26:27], v11 offset0:9 offset1:25\n\t"
      "v_mfma_f32_16x16x32_bf16 v[148:151], v[60:63], v[96:99], v[148:151]\n\t"
      "ds_read_b128 v[96:99], v9 offset:51200\n\t"
      "v_lshrrev_b32 v31, 8, v16\n\t"
      "v_mfma_f32_16x16x32_bf16 v[156:159], v[60:63], v[88:91], v[156:159]\n\t"
      "v_and_b32 v28, v13, v16\n\t"
      "v_and_b32 v29, v14, v16\n\t"
      "v_mfma_f32_16x16x32_bf16 v[156:159], v[68:71], v[92:95], v[156:159]\n\t"
      "v_and_b32 v30, v13, v31\n\t"
      "v_and_b32 v31, v14, v31\n\t"
      "v_mfma_f32_16x16x32_bf16 v[152:155], v[64:67], v[92:95], v[152:155]\n\t"
      "ds_read_b128 v[92:95], v8 offset:53248\n\t"
      "v_fma_mix_f32 v32, v28, v20, -v22 op_sel:[0,0,0] op_sel_hi:[1,0,0]\n\t"
      "v_mfma_f32_16x16x32_bf16 v[152:155], v[56:59], v[88:91], v[152:155]\n\t"
      "ds_read_b128 v[88:91], v9 offset:53248\n\t"
      "v_fma_mix_f32 v33, v29, v24, -v22 op_sel:[0,0,0] op_sel_hi:[1,0,0]\n\t"
      "v_mfma_f32_16x16x32_bf16 v[160:163], v[56:59], v[80:83], v[160:163]\n\t"
      "v_fma_mix_f32 v34, v30, v20, -v22 op_sel:[0,0,0] op_sel_hi:[1,0,0]\n\t"
      "s_waitcnt lgkmcnt(3)\n\t"
      "v_mfma_f32_16x16x32_bf16 v[160:163], v[64:67], v[84:87], v[160:163]\n\t"
      "v_fma_mix_f32 v35, v31, v24, -v22 op_sel:[0,0,0] op_sel_hi:[1,0,0]\n\t"
      "v_fma_mix_f32 v36, v28, v20, -v22 op_sel:[1,0,0] op_sel_hi:[1,0,0]\n\t"
      "v_mfma_f32_16x16x32_bf16 v[164:167], v[68:71], v[84:87], v[164:167]\n\t"
      "ds_read_b128 v[84:87], v8 offset:55296\n\t"
      "v_fma_mix_f32 v37, v29, v24, -v22 op_sel:[1,0,0] op_sel_hi:[1,0,0]\n\t"
      "v_mfma_f32_16x16x32_bf16 v[164:167], v[60:63], v[80:83], v[164:167]\n\t"
      "ds_read_b128 v[80:83], v9 offset:55296\n\t"
      "v_cvt_pk_bf16_f32 v40, v32, v33\n\t"
      "v_mfma_f32_16x16x32_bf16 v[172:175], v[60:63], v[132:135], v[172:175]\n\t"
      "v_cvt_pk_bf16_f32 v41, v34, v35\n\t"
      "v_fma_mix_f32 v38, v30, v20, -v22 op_sel:[1,0,0] op_sel_hi:[1,0,0]\n\t"
      "v_mfma_f32_16x16x32_bf16 v[172:175], v[68:71], v[128:131], v[172:175]\n\t"
      "v_fma_mix_f32 v39, v31, v24, -v22 op_sel:[1,0,0] op_sel_hi:[1,0,0]\n\t"
      "v_cvt_pk_bf16_f32 v42, v36, v37\n\t"
      "v_mfma_f32_16x16x32_bf16 v[168:171], v[64:67], v[128:131], v[168:171]\n\t"
      "ds_read_b128 v[128:131], v8 offset:57344\n\t"
      "v_cvt_pk_bf16_f32 v43, v38, v39\n\t"
      "v_mfma_f32_16x16x32_bf16 v[168:171], v[56:59], v[132:135], v[168:171]\n\t"
      "ds_read_b128 v[132:135], v9 offset:57344\n\t"
      "v_lshrrev_b32 v31, 8, v17\n\t"
      "v_mfma_f32_16x16x32_bf16 v[176:179], v[56:59], v[72:75], v[176:179]\n\t"
      "v_and_b32 v28, v13, v17\n\t"
      "v_and_b32 v29, v14, v17\n\t"
      "v_mfma_f32_16x16x32_bf16 v[176:179], v[64:67], v[76:79], v[176:179]\n\t"
      "v_and_b32 v30, v13, v31\n\t"
      "v_and_b32 v31, v14, v31\n\t"
      "v_mfma_f32_16x16x32_bf16 v[180:183], v[68:71], v[76:79], v[180:183]\n\t"
      "ds_read_b128 v[76:79], v8 offset:59392\n\t"
      "ds_read_b64 v[16:17], v10 offset:12288\n\t"
      "v_mfma_f32_16x16x32_bf16 v[180:183], v[60:63], v[72:75], v[180:183]\n\t"
      "ds_read_b128 v[72:75], v9 offset:59392\n\t"
      "v_fma_mix_f32 v32, v28, v21, -v23 op_sel:[0,0,0] op_sel_hi:[1,0,0]\n\t"
      "v_mfma_f32_16x16x32_bf16 v[188:191], v[60:63], v[124:127], v[188:191]\n\t"
      "v_fma_mix_f32 v33, v29, v25, -v23 op_sel:[0,0,0] op_sel_hi:[1,0,0]\n\t"
      "v_fma_mix_f32 v34, v30, v21, -v23 op_sel:[0,0,0] op_sel_hi:[1,0,0]\n\t"
      "v_mfma_f32_16x16x32_bf16 v[188:191], v[68:71], v[120:123], v[188:191]\n\t"
      "v_fma_mix_f32 v35, v31, v25, -v23 op_sel:[0,0,0] op_sel_hi:[1,0,0]\n\t"
      "v_fma_mix_f32 v36, v28, v21, -v23 op_sel:[1,0,0] op_sel_hi:[1,0,0]\n\t"
      "v_mfma_f32_16x16x32_bf16 v[184:187], v[64:67], v[120:123], v[184:187]\n\t"
      "ds_read_b128 v[120:123], v8 offset:61440\n\t"
      "v_fma_mix_f32 v37, v29, v25, -v23 op_sel:[1,0,0] op_sel_hi:[1,0,0]\n\t"
      "v_mfma_f32_16x16x32_bf16 v[184:187], v[56:59], v[124:127], v[184:187]\n\t"
      "ds_read_b128 v[124:127], v9 offset:61440\n\t"
      "v_cvt_pk_bf16_f32 v44, v32, v33\n\t"
      "v_mfma_f32_16x16x32_bf16 v[192:195], v[56:59], v[116:119], v[192:195]\n\t"
      "v_cvt_pk_bf16_f32 v45, v34, v35\n\t"
      "v_fma_mix_f32 v38, v30, v21, -v23 op_sel:[1,0,0] op_sel_hi:[1,0,0]\n\t"
      "v_mfma_f32_16x16x32_bf16 v[192:195], v[64:67], v[112:115], v[192:195]\n\t"
      "s_barrier\n\t"
      "v_mfma_f32_16x16x32_bf16 v[196:199], v[68:71], v[112:115], v[196:199]\n\t"
      "ds_read_b128 v[112:115], v6 offset:0\n\t"
      "v_fma_mix_f32 v39, v31, v25, -v23 op_sel:[1,0,0] op_sel_hi:[1,0,0]\n\t"
      "v_mfma_f32_16x16x32_bf16 v[196:199], v[60:63], v[116:119], v[196:199]\n\t"
      "ds_read_b128 v[116:119], v7 offset:0\n\t"
      "v_cvt_pk_bf16_f32 v46, v36, v37\n\t"
      "v_mfma_f32_16x16x32_bf16 v[204:207], v[60:63], v[108:111], v[204:207]\n\t"
      "v_cvt_pk_bf16_f32 v47, v38, v39\n\t"
      "v_lshrrev_b32 v31, 8, v18\n\t"
      "v_mfma_f32_16x16x32_bf16 v[204:207], v[68:71], v[104:107], v[204:207]\n\t"
      "v_and_b32 v28, v13, v18\n\t"
      "v_and_b32 v29, v14, v18\n\t"
      "v_mfma_f32_16x16x32_bf16 v[200:203], v[64:67], v[104:107], v[200:203]\n\t"
      "ds_read_b128 v[104:107], v6 offset:2048\n\t"
      "v_and_b32 v30, v13, v31\n\t"
      "v_mfma_f32_16x16x32_bf16 v[200:203], v[56:59], v[108:111], v[200:203]\n\t"
      "ds_read_b128 v[108:111], v7 offset:2048\n\t"
      "v_and_b32 v31, v14, v31\n\t"
      "v_mfma_f32_16x16x32_bf16 v[208:211], v[56:59], v[100:103], v[208:211]\n\t"
      "v_fma_mix_f32 v32, v28, v20, -v22 op_sel:[0,0,0] op_sel_hi:[1,0,0]\n\t"
      "s_waitcnt lgkmcnt(3)\n\t"
      "v_mfma_f32_16x16x32_bf16 v[208:211], v[64:67], v[96:99], v[208:211]\n\t"
      "v_fma_mix_f32 v33, v29, v24, -v22 op_sel:[0,0,0] op_sel_hi:[1,0,0]\n\t"
      "v_fma_mix_f32 v34, v30, v20, -v22 op_sel:[0,0,0] op_sel_hi:[1,0,0]\n\t"
      "v_mfma_f32_16x16x32_bf16 v[212:215], v[68:71], v[96:99], v[212:215]\n\t"
      "ds_read_b128 v[96:99], v6 offset:4096\n\t"
      "v_fma_mix_f32 v35, v31, v24, -v22 op_sel:[0,0,0] op_sel_hi:[1,0,0]\n\t"
      "v_mfma_f32_16x16x32_bf16 v[212:215], v[60:63], v[100:103], v[212:215]\n\t"
      "ds_read_b128 v[100:103], v7 offset:4096\n\t"
      "v_fma_mix_f32 v36, v28, v20, -v22 op_sel:[1,0,0] op_sel_hi:[1,0,0]\n\t"
      "v_mfma_f32_16x16x32_bf16 v[220:223], v[60:63], v[92:95], v[220:223]\n\t"
      "v_fma_mix_f32 v37, v29, v24, -v22 op_sel:[1,0,0] op_sel_hi:[1,0,0]\n\t"
      "v_cvt_pk_bf16_f32 v48, v32, v33\n\t"
      "v_mfma_f32_16x16x32_bf16 v[220:223], v[68:71], v[88:91], v[220:223]\n\t"
      "v_cvt_pk_bf16_f32 v49, v34, v35\n\t"
      "v_fma_mix_f32 v38, v30, v20, -v22 op_sel:[1,0,0] op_sel_hi:[1,0,0]\n\t"
      "v_mfma_f32_16x16x32_bf16 v[216:219], v[64:67], v[88:91], v[216:219]\n\t"
      "ds_read_b128 v[88:91], v6 offset:6144\n\t"
      "v_fma_mix_f32 v39, v31, v24, -v22 op_sel:[1,0,0] op_sel_hi:[1,0,0]\n\t"
      "v_mfma_f32_16x16x32_bf16 v[216:219], v[56:59], v[92:95], v[216:219]\n\t"
      "ds_read_b128 v[92:95], v7 offset:6144\n\t"
      "v_cvt_pk_bf16_f32 v50, v36, v37\n\t"
      "v_mfma_f32_16x16x32_bf16 v[224:227], v[56:59], v[84:87], v[224:227]\n\t"
      "v_cvt_pk_bf16_f32 v51, v38, v39\n\t"
      "v_lshrrev_b32 v31, 8, v19\n\t"
      "v_mfma_f32_16x16x32_bf16 v[224:227], v[64:67], v[80:83], v[224:227]\n\t"
      "v_and_b32 v28, v13, v19\n\t"
      "v_and_b32 v29, v14, v19\n\t"
      "v_mfma_f32_16x16x32_bf16 v[228:231], v[68:71], v[80:83], v[228:231]\n\t"
      "ds_read_b128 v[80:83], v6 offset:8192\n\t"
      "v_and_b32 v30, v13, v31\n\t"
      "v_mfma_f32_16x16x32_bf16 v[228:231], v[60:63], v[84:87], v[228:231]\n\t"
      "ds_read_b128 v[84:87], v7 offset:8192\n\t"
      "v_and_b32 v31, v14, v31\n\t"
      "v_mfma_f32_16x16x32_bf16 v[236:239], v[60:63], v[128:131], v[236:239]\n\t"
      "ds_read_b64 v[18:19], v10 offset:14336\n\t"
      "v_fma_mix_f32 v32, v28, v21, -v23 op_sel:[0,0,0] op_sel_hi:[1,0,0]\n\t"
      "v_mfma_f32_16x16x32_bf16 v[236:239], v[68:71], v[132:135], v[236:239]\n\t"
      "v_fma_mix_f32 v33, v29, v25, -v23 op_sel:[0,0,0] op_sel_hi:[1,0,0]\n\t"
      "v_fma_mix_f32 v34, v30, v21, -v23 op_sel:[0,0,0] op_sel_hi:[1,0,0]\n\t"
      "v_mfma_f32_16x16x32_bf16 v[232:235], v[64:67], v[132:135], v[232:235]\n\t"
      "ds_read_b128 v[132:135], v6 offset:10240\n\t"
      "v_fma_mix_f32 v35, v31, v25, -v23 op_sel:[0,0,0] op_sel_hi:[1,0,0]\n\t"
      "v_mfma_f32_16x16x32_bf16 v[232:235], v[56:59], v[128:131], v[232:235]\n\t"
      "ds_read_b128 v[128:131], v7 offset:10240\n\t"
      "v_fma_mix_f32 v36, v28, v21, -v23 op_sel:[1,0,0] op_sel_hi:[1,0,0]\n\t"
      "v_mfma_f32_16x16x32_bf16 v[240:243], v[56:59], v[76:79], v[240:243]\n\t"
      "v_fma_mix_f32 v37, v29, v25, -v23 op_sel:[1,0,0] op_sel_hi:[1,0,0]\n\t"
      "v_cvt_pk_bf16_f32 v52, v32, v33\n\t"
      "v_mfma_f32_16x16x32_bf16 v[240:243], v[64:67], v[72:75], v[240:243]\n\t"
      "v_cvt_pk_bf16_f32 v53, v34, v35\n\t"
      "v_fma_mix_f32 v38, v30, v21, -v23 op_sel:[1,0,0] op_sel_hi:[1,0,0]\n\t"
      "v_mfma_f32_16x16x32_bf16 v[244:247], v[68:71], v[72:75], v[244:247]\n\t"
      "ds_read_b128 v[72:75], v6 offset:12288\n\t"
      "v_fma_mix_f32 v39, v31, v25, -v23 op_sel:[1,0,0] op_sel_hi:[1,0,0]\n\t"
      "v_mfma_f32_16x16x32_bf16 v[244:247], v[60:63], v[76:79], v[244:247]\n\t"
      "ds_read_b128 v[76:79], v7 offset:12288\n\t"
      "v_cvt_pk_bf16_f32 v54, v36, v37\n\t"
      "v_mfma_f32_16x16x32_bf16 v[252:255], v[60:63], v[120:123], v[252:255]\n\t"
      "v_cvt_pk_bf16_f32 v55, v38, v39\n\t"
      "v_mfma_f32_16x16x32_bf16 v[252:255], v[68:71], v[124:127], v[252:255]\n\t"
      "v_mfma_f32_16x16x32_bf16 v[248:251], v[64:67], v[124:127], v[248:251]\n\t"
      "ds_read_b128 v[124:127], v6 offset:14336\n\t"
      "v_mfma_f32_16x16x32_bf16 v[248:251], v[56:59], v[120:123], v[248:251]\n\t"
      "ds_read_b128 v[120:123], v7 offset:14336\n\t"
      "v_mfma_f32_16x16x32_bf16 v[136:139], v[40:43], v[112:115], v[136:139]\n\t"
      "v_lshlrev_b32 v28, 16, v26\n\t"
      "s_waitcnt lgkmcnt(3)\n\t"
      "v_mfma_f32_16x16x32_bf16 v[136:139], v[48:51], v[116:119], v[136:139]\n\t"
      "v_and_b32 v29, 0xffff0000, v26\n\t"
      "v_lshlrev_b32 v30, 16, v27\n\t"
      "v_mfma_f32_16x16x32_bf16 v[140:143], v[52:55], v[116:119], v[140:143]\n\t"
      "ds_read_b128 v[116:119], v6 offset:16384\n\t"
      "v_and_b32 v31, 0xffff0000, v27\n\t"
      "v_mfma_f32_16x16x32_bf16 v[140:143], v[44:47], v[112:115], v[140:143]\n\t"
      "ds_read_b128 v[112:115], v7 offset:16384\n\t"
      "v_mul_f32 v20, 0x4b800000, v28\n\t"
      "v_mfma_f32_16x16x32_bf16 v[148:151], v[44:47], v[104:107], v[148:151]\n\t"
      "v_mul_f32 v24, 0x49800000, v28\n\t"
      "v_mul_f32 v22, v28, v30\n\t"
      "v_mfma_f32_16x16x32_bf16 v[148:151], v[52:55], v[108:111], v[148:151]\n\t"
      "v_mul_f32 v21, 0x4b800000, v29\n\t"
      "v_mul_f32 v25, 0x49800000, v29\n\t"
      "v_mfma_f32_16x16x32_bf16 v[144:147], v[48:51], v[108:111], v[144:147]\n\t"
      "ds_read_b128 v[108:111], v6 offset:18432\n\t"
      "v_mul_f32 v23, v29, v31\n\t"
      "v_mfma_f32_16x16x32_bf16 v[144:147], v[40:43], v[104:107], v[144:147]\n\t"
      "ds_read_b128 v[104:107], v7 offset:18432\n\t"
      "ds_read2st64_b32 v[26:27], v11 offset0:10 offset1:26\n\t"
      "v_mfma_f32_16x16x32_bf16 v[152:155], v[40:43], v[96:99], v[152:155]\n\t"
      "v_lshrrev_b32 v31, 8, v16\n\t"
      "v_and_b32 v28, v13, v16\n\t"
      "v_mfma_f32_16x16x32_bf16 v[152:155], v[48:51], v[100:103], v[152:155]\n\t"
      "v_and_b32 v29, v14, v16\n\t"
      "v_and_b32 v30, v13, v31\n\t"
      "v_mfma_f32_16x16x32_bf16 v[156:159], v[52:55], v[100:103], v[156:159]\n\t"
      "ds_read_b128 v[100:103], v6 offset:20480\n\t"
      "v_and_b32 v31, v14, v31\n\t"
      "v_mfma_f32_16x16x32_bf16 v[156:159], v[44:47], v[96:99], v[156:159]\n\t"
      "ds_read_b128 v[96:99], v7 offset:20480\n\t"
      "v_fma_mix_f32 v32, v28, v20, -v22 op_sel:[0,0,0] op_sel_hi:[1,0,0]\n\t"
      "v_mfma_f32_16x16x32_bf16 v[164:167], v[44:47], v[88:91], v[164:167]\n\t"
      "v_fma_mix_f32 v33, v29, v24, -v22 op_sel:[0,0,0] op_sel_hi:[1,0,0]\n\t"
      "v_fma_mix_f32 v34, v30, v20, -v22 op_sel:[0,0,0] op_sel_hi:[1,0,0]\n\t"
      "v_mfma_f32_16x16x32_bf16 v[164:167], v[52:55], v[92:95], v[164:167]\n\t"
      "v_fma_mix_f32 v35, v31, v24, -v22 op_sel:[0,0,0] op_sel_hi:[1,0,0]\n\t"
      "v_fma_mix_f32 v36, v28, v20, -v22 op_sel:[1,0,0] op_sel_hi:[1,0,0]\n\t"
      "v_mfma_f32_16x16x32_bf16 v[160:163], v[48:51], v[92:95], v[160:163]\n\t"
      "ds_read_b128 v[92:95], v6 offset:22528\n\t"
      "v_fma_mix_f32 v37, v29, v24, -v22 op_sel:[1,0,0] op_sel_hi:[1,0,0]\n\t"
      "v_mfma_f32_16x16x32_bf16 v[160:163], v[40:43], v[88:91], v[160:163]\n\t"
      "ds_read_b128 v[88:91], v7 offset:22528\n\t"
      "v_cvt_pk_bf16_f32 v56, v32, v33\n\t"
      "v_mfma_f32_16x16x32_bf16 v[168:171], v[40:43], v[80:83], v[168:171]\n\t"
      "v_cvt_pk_bf16_f32 v57, v34, v35\n\t"
      "v_fma_mix_f32 v38, v30, v20, -v22 op_sel:[1,0,0] op_sel_hi:[1,0,0]\n\t"
      "v_mfma_f32_16x16x32_bf16 v[168:171], v[48:51], v[84:87], v[168:171]\n\t"
      "v_fma_mix_f32 v39, v31, v24, -v22 op_sel:[1,0,0] op_sel_hi:[1,0,0]\n\t"
      "v_cvt_pk_bf16_f32 v58, v36, v37\n\t"
      "v_mfma_f32_16x16x32_bf16 v[172:175], v[52:55], v[84:87], v[172:175]\n\t"
      "ds_read_b128 v[84:87], v6 offset:24576\n\t"
      "v_cvt_pk_bf16_f32 v59, v38, v39\n\t"
      "v_mfma_f32_16x16x32_bf16 v[172:175], v[44:47], v[80:83], v[172:175]\n\t"
      "ds_read_b128 v[80:83], v7 offset:24576\n\t"
      "v_lshrrev_b32 v31, 8, v17\n\t"
      "v_mfma_f32_16x16x32_bf16 v[180:183], v[44:47], v[132:135], v[180:183]\n\t"
      "v_and_b32 v28, v13, v17\n\t"
      "v_and_b32 v29, v14, v17\n\t"
      "v_mfma_f32_16x16x32_bf16 v[180:183], v[52:55], v[128:131], v[180:183]\n\t"
      "v_and_b32 v30, v13, v31\n\t"
      "v_and_b32 v31, v14, v31\n\t"
      "v_mfma_f32_16x16x32_bf16 v[176:179], v[48:51], v[128:131], v[176:179]\n\t"
      "ds_read_b128 v[128:131], v6 offset:26624\n\t"
      "ds_read_b64 v[16:17], v10 offset:16384\n\t"
      "v_mfma_f32_16x16x32_bf16 v[176:179], v[40:43], v[132:135], v[176:179]\n\t"
      "ds_read_b128 v[132:135], v7 offset:26624\n\t"
      "v_fma_mix_f32 v32, v28, v21, -v23 op_sel:[0,0,0] op_sel_hi:[1,0,0]\n\t"
      "v_mfma_f32_16x16x32_bf16 v[184:187], v[40:43], v[72:75], v[184:187]\n\t"
      "v_fma_mix_f32 v33, v29, v25, -v23 op_sel:[0,0,0] op_sel_hi:[1,0,0]\n\t"
      "s_waitcnt lgkmcnt(4)\n\t"
      "v_mfma_f32_16x16x32_bf16 v[184:187], v[48:51], v[76:79], v[184:187]\n\t"
      "v_fma_mix_f32 v34, v30, v21, -v23 op_sel:[0,0,0] op_sel_hi:[1,0,0]\n\t"
      "v_fma_mix_f32 v35, v31, v25, -v23 op_sel:[0,0,0] op_sel_hi:[1,0,0]\n\t"
      "v_mfma_f32_16x16x32_bf16 v[188:191], v[52:55], v[76:79], v[188:191]\n\t"
      "ds_read_b128 v[76:79], v6 offset:28672\n\t"
      "v_fma_mix_f32 v36, v28, v21, -v23 op_sel:[1,0,0] op_sel_hi:[1,0,0]\n\t"
      "v_mfma_f32_16x16x32_bf16 v[188:191], v[44:47], v[72:75], v[188:191]\n\t"
      "ds_read_b128 v[72:75], v7 offset:28672\n\t"
      "v_fma_mix_f32 v37, v29, v25, -v23 op_sel:[1,0,0] op_sel_hi:[1,0,0]\n\t"
      "v_mfma_f32_16x16x32_bf16 v[196:199], v[44:47], v[124:127], v[196:199]\n\t"
      "v_cvt_pk_bf16_f32 v60, v32, v33\n\t"
      "v_cvt_pk_bf16_f32 v61, v34, v35\n\t"
      "v_mfma_f32_16x16x32_bf16 v[196:199], v[52:55], v[120:123], v[196:199]\n\t"
      "s_barrier\n\t"
      "v_mfma_f32_16x16x32_bf16 v[192:195], v[48:51], v[120:123], v[192:195]\n\t"
      "ds_read_b128 v[120:123], v6 offset:32768\n\t"
      "v_fma_mix_f32 v38, v30, v21, -v23 op_sel:[1,0,0] op_sel_hi:[1,0,0]\n\t"
      "v_mfma_f32_16x16x32_bf16 v[192:195], v[40:43], v[124:127], v[192:195]\n\t"
      "ds_read_b128 v[124:127], v7 offset:32768\n\t"
      "v_fma_mix_f32 v39, v31, v25, -v23 op_sel:[1,0,0] op_sel_hi:[1,0,0]\n\t"
      "v_mfma_f32_16x16x32_bf16 v[200:203], v[40:43], v[116:119], v[200:203]\n\t"
      "v_cvt_pk_bf16_f32 v62, v36, v37\n\t"
      "v_cvt_pk_bf16_f32 v63, v38, v39\n\t"
      "v_mfma_f32_16x16x32_bf16 v[200:203], v[48:51], v[112:115], v[200:203]\n\t"
      "v_lshrrev_b32 v31, 8, v18\n\t"
      "v_and_b32 v28, v13, v18\n\t"
      "v_mfma_f32_16x16x32_bf16 v[204:207], v[52:55], v[112:115], v[204:207]\n\t"
      "ds_read_b128 v[112:115], v6 offset:34816\n\t"
      "v_and_b32 v29, v14, v18\n\t"
      "v_mfma_f32_16x16x32_bf16 v[204:207], v[44:47], v[116:119], v[204:207]\n\t"
      "ds_read_b128 v[116:119], v7 offset:34816\n\t"
      "v_and_b32 v30, v13, v31\n\t"
      "v_mfma_f32_16x16x32_bf16 v[212:215], v[44:47], v[108:111], v[212:215]\n\t"
      "v_and_b32 v31, v14, v31\n\t"
      "v_fma_mix_f32 v32, v28, v20, -v22 op_sel:[0,0,0] op_sel_hi:[1,0,0]\n\t"
      "v_mfma_f32_16x16x32_bf16 v[212:215], v[52:55], v[104:107], v[212:215]\n\t"
      "v_fma_mix_f32 v33, v29, v24, -v22 op_sel:[0,0,0] op_sel_hi:[1,0,0]\n\t"
      "v_fma_mix_f32 v34, v30, v20, -v22 op_sel:[0,0,0] op_sel_hi:[1,0,0]\n\t"
      "v_mfma_f32_16x16x32_bf16 v[208:211], v[48:51], v[104:107], v[208:211]\n\t"
      "ds_read_b128 v[104:107], v6 offset:36864\n\t"
      "v_fma_mix_f32 v35, v31, v24, -v22 op_sel:[0,0,0] op_sel_hi:[1,0,0]\n\t"
      "v_mfma_f32_16x16x32_bf16 v[208:211], v[40:43], v[108:111], v[208:211]\n\t"
      "ds_read_b128 v[108:111], v7 offset:36864\n\t"
      "v_fma_mix_f32 v36, v28, v20, -v22 op_sel:[1,0,0] op_sel_hi:[1,0,0]\n\t"
      "v_mfma_f32_16x16x32_bf16 v[216:219], v[40:43], v[100:103], v[216:219]\n\t"
      "v_fma_mix_f32 v37, v29, v24, -v22 op_sel:[1,0,0] op_sel_hi:[1,0,0]\n\t"
      "v_cvt_pk_bf16_f32 v64, v32, v33\n\t"
      "v_mfma_f32_16x16x32_bf16 v[216:219], v[48:51], v[96:99], v[216:219]\n\t"
      "v_cvt_pk_bf16_f32 v65, v34, v35\n\t"
      "v_fma_mix_f32 v38, v30, v20, -v22 op_sel:[1,0,0] op_sel_hi:[1,0,0]\n\t"
      "v_mfma_f32_16x16x32_bf16 v[220:223], v[52:55], v[96:99], v[220:223]\n\t"
      "ds_read_b128 v[96:99], v6 offset:38912\n\t"
      "v_fma_mix_f32 v39, v31, v24, -v22 op_sel:[1,0,0] op_sel_hi:[1,0,0]\n\t"
      "v_mfma_f32_16x16x32_bf16 v[220:223], v[44:47], v[100:103], v[220:223]\n\t"
      "ds_read_b128 v[100:103], v7 offset:38912\n\t"
      "v_cvt_pk_bf16_f32 v66, v36, v37\n\t"
      "v_mfma_f32_16x16x32_bf16 v[228:231], v[44:47], v[92:95], v[228:231]\n\t"
      "v_cvt_pk_bf16_f32 v67, v38, v39\n\t"
      "v_lshrrev_b32 v31, 8, v19\n\t"
      "v_mfma_f32_16x16x32_bf16 v[228:231], v[52:55], v[88:91], v[228:231]\n\t"
      "v_and_b32 v28, v13, v19\n\t"
      "v_and_b32 v29, v14, v19\n\t"
      "v_mfma_f32_16x16x32_bf16 v[224:227], v[48:51], v[88:91], v[224:227]\n\t"
      "ds_read_b128 v[88:91], v6 offset:40960\n\t"
      "v_and_b32 v30, v13, v31\n\t"
      "v_mfma_f32_16x16x32_bf16 v[224:227], v[40:43], v[92:95], v[224:227]\n\t"
      "ds_read_b128 v[92:95], v7 offset:40960\n\t"
      "v_and_b32 v31, v14, v31\n\t"
      "v_mfma_f32_16x16x32_bf16 v[232:235], v[40:43], v[84:87], v[232:235]\n\t"
      "ds_read_b64 v[18:19], v10 offset:18432\n\t"
      "s_waitcnt lgkmcnt(4)\n\t"
      "v_mfma_f32_16x16x32_bf16 v[232:235], v[48:51], v[80:83], v[232:235]\n\t"
      "v_fma_mix_f32 v32, v28, v21, -v23 op_sel:[0,0,0] op_sel_hi:[1,0,0]\n\t"
      "v_fma_mix_f32 v33, v29, v25, -v23 op_sel:[0,0,0] op_sel_hi:[1,0,0]\n\t"
      "v_mfma_f32_16x16x32_bf16 v[236:239], v[52:55], v[80:83], v[236:239]\n\t"
      "ds_read_b128 v[80:83], v6 offset:43008\n\t"
      "v_fma_mix_f32 v34, v30, v21, -v23 op_sel:[0,0,0] op_sel_hi:[1,0,0]\n\t"
      "v_mfma_f32_16x16x32_bf16 v[236:239], v[44:47], v[84:87], v[236:239]\n\t"
      "ds_read_b128 v[84:87], v7 offset:43008\n\t"
      "v_fma_mix_f32 v35, v31, v25, -v23 op_sel:[0,0,0] op_sel_hi:[1,0,0]\n\t"
      "v_mfma_f32_16x16x32_bf16 v[244:247], v[44:47], v[128:131], v[244:247]\n\t"
      "v_fma_mix_f32 v36, v28, v21, -v23 op_sel:[1,0,0] op_sel_hi:[1,0,0]\n\t"
      "v_fma_mix_f32 v37, v29, v25, -v23 op_sel:[1,0,0] op_sel_hi:[1,0,0]\n\t"
      "v_mfma_f32_16x16x32_bf16 v[244:247], v[52:55], v[132:135], v[244:247]\n\t"
      "v_cvt_pk_bf16_f32 v68, v32, v33\n\t"
      "v_cvt_pk_bf16_f32 v69, v34, v35\n\t"
      "v_mfma_f32_16x16x32_bf16 v[240:243], v[48:51], v[132:135], v[240:243]\n\t"
      "ds_read_b128 v[132:135], v6 offset:45056\n\t"
      "v_fma_mix_f32 v38, v30, v21, -v23 op_sel:[1,0,0] op_sel_hi:[1,0,0]\n\t"
      "v_mfma_f32_16x16x32_bf16 v[240:243], v[40:43], v[128:131], v[240:243]\n\t"
      "ds_read_b128 v[128:131], v7 offset:45056\n\t"
      "v_fma_mix_f32 v39, v31, v25, -v23 op_sel:[1,0,0] op_sel_hi:[1,0,0]\n\t"
      "v_mfma_f32_16x16x32_bf16 v[248:251], v[40:43], v[76:79], v[248:251]\n\t"
      "v_cvt_pk_bf16_f32 v70, v36, v37\n\t"
      "v_cvt_pk_bf16_f32 v71, v38, v39\n\t"
      "v_mfma_f32_16x16x32_bf16 v[248:251], v[48:51], v[72:75], v[248:251]\n\t"
      "v_mfma_f32_16x16x32_bf16 v[252:255], v[52:55], v[72:75], v[252:255]\n\t"
      "ds_read_b128 v[72:75], v6 offset:47104\n\t"
      "v_mfma_f32_16x16x32_bf16 v[252:255], v[44:47], v[76:79], v[252:255]\n\t"
      "ds_read_b128 v[76:79], v7 offset:47104\n\t"
      "v_mfma_f32_16x16x32_bf16 v[140:143], v[60:63], v[120:123], v[140:143]\n\t"
      "v_lshlrev_b32 v28, 16, v26\n\t"
      "v_and_b32 v29, 0xffff0000, v26\n\t"
      "v_mfma_f32_16x16x32_bf16 v[140:143], v[68:71], v[124:127], v[140:143]\n\t"
      "v_lshlrev_b32 v30, 16, v27\n\t"
      "v_and_b32 v31, 0xffff0000, v27\n\t"
      "v_mfma_f32_16x16x32_bf16 v[136:139], v[64:67], v[124:127], v[136:139]\n\t"
      "ds_read_b128 v[124:127], v6 offset:49152\n\t"
      "v_mul_f32 v20, 0x4b800000, v28\n\t"
      "v_mfma_f32_16x16x32_bf16 v[136:139], v[56:59], v[120:123], v[136:139]\n\t"
      "ds_read_b128 v[120:123], v7 offset:49152\n\t"
      "v_mul_f32 v24, 0x49800000, v28\n\t"
      "v_mfma_f32_16x16x32_bf16 v[144:147], v[56:59], v[112:115], v[144:147]\n\t"
      "v_mul_f32 v22, v28, v30\n\t"
      "v_mul_f32 v21, 0x4b800000, v29\n\t"
      "v_mfma_f32_16x16x32_bf16 v[144:147], v[64:67], v[116:119], v[144:147]\n\t"
      "v_mul_f32 v25, 0x49800000, v29\n\t"
      "v_mul_f32 v23, v29, v31\n\t"
      "v_mfma_f32_16x16x32_bf16 v[148:151], v[68:71], v[116:119], v[148:151]\n\t"
      "ds_read_b128 v[116:119], v6 offset:51200\n\t"
      "ds_read2st64_b32 v[26:27], v11 offset0:11 offset1:27\n\t"
      "v_mfma_f32_16x16x32_bf16 v[148:151], v[60:63], v[112:115], v[148:151]\n\t"
      "ds_read_b128 v[112:115], v7 offset:51200\n\t"
      "v_lshrrev_b32 v31, 8, v16\n\t"
      "v_mfma_f32_16x16x32_bf16 v[156:159], v[60:63], v[104:107], v[156:159]\n\t"
      "v_and_b32 v28, v13, v16\n\t"
      "v_and_b32 v29, v14, v16\n\t"
      "v_mfma_f32_16x16x32_bf16 v[156:159], v[68:71], v[108:111], v[156:159]\n\t"
      "v_and_b32 v30, v13, v31\n\t"
      "v_and_b32 v31, v14, v31\n\t"
      "v_mfma_f32_16x16x32_bf16 v[152:155], v[64:67], v[108:111], v[152:155]\n\t"
      "ds_read_b128 v[108:111], v6 offset:53248\n\t"
      "v_fma_mix_f32 v32, v28, v20, -v22 op_sel:[0,0,0] op_sel_hi:[1,0,0]\n\t"
      "v_mfma_f32_16x16x32_bf16 v[152:155], v[56:59], v[104:107], v[152:155]\n\t"
      "ds_read_b128 v[104:107], v7 offset:53248\n\t"
      "v_fma_mix_f32 v33, v29, v24, -v22 op_sel:[0,0,0] op_sel_hi:[1,0,0]\n\t"
      "v_mfma_f32_16x16x32_bf16 v[160:163], v[56:59], v[96:99], v[160:163]\n\t"
      "v_fma_mix_f32 v34, v30, v20, -v22 op_sel:[0,0,0] op_sel_hi:[1,0,0]\n\t"
      "s_waitcnt lgkmcnt(3)\n\t"
      "v_mfma_f32_16x16x32_bf16 v[160:163], v[64:67], v[100:103], v[160:163]\n\t"
      "v_fma_mix_f32 v35, v31, v24, -v22 op_sel:[0,0,0] op_sel_hi:[1,0,0]\n\t"
      "v_fma_mix_f32 v36, v28, v20, -v22 op_sel:[1,0,0] op_sel_hi:[1,0,0]\n\t"
      "v_mfma_f32_16x16x32_bf16 v[164:167], v[68:71], v[100:103], v[164:167]\n\t"
      "ds_read_b128 v[100:103], v6 offset:55296\n\t"
      "v_fma_mix_f32 v37, v29, v24, -v22 op_sel:[1,0,0] op_sel_hi:[1,0,0]\n\t"
      "v_mfma_f32_16x16x32_bf16 v[164:167], v[60:63], v[96:99], v[164:167]\n\t"
      "ds_read_b128 v[96:99], v7 offset:55296\n\t"
      "v_cvt_pk_bf16_f32 v40, v32, v33\n\t"
      "v_mfma_f32_16x16x32_bf16 v[172:175], v[60:63], v[88:91], v[172:175]\n\t"
      "v_cvt_pk_bf16_f32 v41, v34, v35\n\t"
      "v_fma_mix_f32 v38, v30, v20, -v22 op_sel:[1,0,0] op_sel_hi:[1,0,0]\n\t"
      "v_mfma_f32_16x16x32_bf16 v[172:175], v[68:71], v[92:95], v[172:175]\n\t"
      "v_fma_mix_f32 v39, v31, v24, -v22 op_sel:[1,0,0] op_sel_hi:[1,0,0]\n\t"
      "v_cvt_pk_bf16_f32 v42, v36, v37\n\t"
      "v_mfma_f32_16x16x32_bf16 v[168:171], v[64:67], v[92:95], v[168:171]\n\t"
      "ds_read_b128 v[92:95], v6 offset:57344\n\t"
      "v_cvt_pk_bf16_f32 v43, v38, v39\n\t"
      "v_mfma_f32_16x16x32_bf16 v[168:171], v[56:59], v[88:91], v[168:171]\n\t"
      "ds_read_b128 v[88:91], v7 offset:57344\n\t"
      "v_lshrrev_b32 v31, 8, v17\n\t"
      "v_mfma_f32_16x16x32_bf16 v[176:179], v[56:59], v[80:83], v[176:179]\n\t"
      "v_and_b32 v28, v13, v17\n\t"
      "v_and_b32 v29, v14, v17\n\t"
      "v_mfma_f32_16x16x32_bf16 v[176:179], v[64:67], v[84:87], v[176:179]\n\t"
      "v_and_b32 v30, v13, v31\n\t"
      "v_and_b32 v31, v14, v31\n\t"
      "v_mfma_f32_16x16x32_bf16 v[180:183], v[68:71], v[84:87], v[180:183]\n\t"
      "ds_read_b128 v[84:87], v6 offset:59392\n\t"
      "ds_read_b64 v[16:17], v10 offset:20480\n\t"
      "v_mfma_f32_16x16x32_bf16 v[180:183], v[60:63], v[80:83], v[180:183]\n\t"
      "s_nop 0\n\t"
      "LH15x3_%=:\n\t"
      "s_cbranch_execz LHRET_%=\n\t"
      "ds_read_b128 v[80:83], v7 offset:59392\n\t"
      "v_fma_mix_f32 v32, v28, v21, -v23 op_sel:[0,0,0] op_sel_hi:[1,0,0]\n\t"
      "v_mfma_f32_16x16x32_bf16 v[188:191], v[60:63], v[132:135], v[188:191]\n\t"
      "v_fma_mix_f32 v33, v29, v25, -v23 op_sel:[0,0,0] op_sel_hi:[1,0,0]\n\t"
      "v_fma_mix_f32 v34, v30, v21, -v23 op_sel:[0,0,0] op_sel_hi:[1,0,0]\n\t"
      "v_mfma_f32_16x16x32_bf16 v[188:191], v[68:71], v[128:131], v[188:191]\n\t"
      "v_fma_mix_f32 v35, v31, v25, -v23 op_sel:[0,0,0] op_sel_hi:[1,0,0]\n\t"
      "v_fma_mix_f32 v36, v28, v21, -v23 op_sel:[1,0,0] op_sel_hi:[1,0,0]\n\t"
      "v_mfma_f32_16x16x32_bf16 v[184:187], v[64:67], v[128:131], v[184:187]\n\t"
      "ds_read_b128 v[128:131], v6 offset:61440\n\t"
      "v_fma_mix_f32 v37, v29, v25, -v23 op_sel:[1,0,0] op_sel_hi:[1,0,0]\n\t"
      "v_mfma_f32_16x16x32_bf16 v[184:187], v[56:59], v[132:135], v[184:187]\n\t"
      "ds_read_b128 v[132:135], v7 offset:61440\n\t"
      "v_cvt_pk_bf16_f32 v44, v32, v33\n\t"
      "v_mfma_f32_16x16x32_bf16 v[192:195], v[56:59], v[72:75], v[192:195]\n\t"
      "v_cvt_pk_bf16_f32 v45, v34, v35\n\t"
      "v_fma_mix_f32 v38, v30, v21, -v23 op_sel:[1,0,0] op_sel_hi:[1,0,0]\n\t"
      "v_mfma_f32_16x16x32_bf16 v[192:195], v[64:67], v[76:79], v[192:195]\n\t"
      "s_barrier\n\t"
      "v_mfma_f32_16x16x32_bf16 v[196:199], v[68:71], v[76:79], v[196:199]\n\t"
      "ds_read_b128 v[76:79], v8 offset:0\n\t"
      "v_fma_mix_f32 v39, v31, v25, -v23 op_sel:[1,0,0] op_sel_hi:[1,0,0]\n\t"
      "v_mfma_f32_16x16x32_bf16 v[196:199], v[60:63], v[72:75], v[196:199]\n\t"
      "ds_read_b128 v[72:75], v9 offset:0\n\t"
      "v_cvt_pk_bf16_f32 v46, v36, v37\n\t"
      "v_mfma_f32_16x16x32_bf16 v[204:207], v[60:63], v[124:127], v[204:207]\n\t"
      "v_cvt_pk_bf16_f32 v47, v38, v39\n\t"
      "v_lshrrev_b32 v31, 8, v18\n\t"
      "v_mfma_f32_16x16x32_bf16 v[204:207], v[68:71], v[120:123], v[204:207]\n\t"
      "v_and_b32 v28, v13, v18\n\t"
      "v_and_b32 v29, v14, v18\n\t"
      "v_mfma_f32_16x16x32_bf16 v[200:203], v[64:67], v[120:123], v[200:203]\n\t"
      "ds_read_b128 v[120:123], v8 offset:2048\n\t"
      "v_and_b32 v30, v13, v31\n\t"
      "v_mfma_f32_16x16x32_bf16 v[200:203], v[56:59], v[124:127], v[200:203]\n\t"
      "ds_read_b128 v[124:127], v9 offset:2048\n\t"
      "v_and_b32 v31, v14, v31\n\t"
      "v_mfma_f32_16x16x32_bf16 v[208:211], v[56:59], v[116:119], v[208:211]\n\t"
      "v_fma_mix_f32 v32, v28, v20, -v22 op_sel:[0,0,0] op_sel_hi:[1,0,0]\n\t"
      "s_waitcnt lgkmcnt(3)\n\t"
      "v_mfma_f32_16x16x32_bf16 v[208:211], v[64:67], v[112:115], v[208:211]\n\t"
      "v_fma_mix_f32 v33, v29, v24, -v22 op_sel:[0,0,0] op_sel_hi:[1,0,0]\n\t"
      "v_fma_mix_f32 v34, v30, v20, -v22 op_sel:[0,0,0] op_sel_hi:[1,0,0]\n\t"
      "v_mfma_f32_16x16x32_bf16 v[212:215], v[68:71], v[112:115], v[212:215]\n\t"
      "ds_read_b128 v[112:115], v8 offset:4096\n\t"
      "v_fma_mix_f32 v35, v31, v24, -v22 op_sel:[0,0,0] op_sel_hi:[1,0,0]\n\t"
      "v_mfma_f32_16x16x32_bf16 v[212:215], v[60:63], v[116:119], v[212:215]\n\t"
      "ds_read_b128 v[116:119], v9 offset:4096\n\t"
      "v_fma_mix_f32 v36, v28, v20, -v22 op_sel:[1,0,0] op_sel_hi:[1,0,0]\n\t"
      "v_mfma_f32_16x16x32_bf16 v[220:223], v[60:63], v[108:111], v[220:223]\n\t"
      "v_fma_mix_f32 v37, v29, v24, -v22 op_sel:[1,0,0] op_sel_hi:[1,0,0]\n\t"
      "v_cvt_pk_bf16_f32 v48, v32, v33\n\t"
      "v_mfma_f32_16x16x32_bf16 v[220:223], v[68:71], v[104:107], v[220:223]\n\t"
      "v_cvt_pk_bf16_f32 v49, v34, v35\n\t"
      "v_fma_mix_f32 v38, v30, v20, -v22 op_sel:[1,0,0] op_sel_hi:[1,0,0]\n\t"
      "v_mfma_f32_16x16x32_bf16 v[216:219], v[64:67], v[104:107], v[216:219]\n\t"
      "ds_read_b128 v[104:107], v8 offset:6144\n\t"
      "v_fma_mix_f32 v39, v31, v24, -v22 op_sel:[1,0,0] op_sel_hi:[1,0,0]\n\t"
      "v_mfma_f32_16x16x32_bf16 v[216:219], v[56:59], v[108:111], v[216:219]\n\t"
      "ds_read_b128 v[108:111], v9 offset:6144\n\t"
      "v_cvt_pk_bf16_f32 v50, v36, v37\n\t"
      "v_mfma_f32_16x16x32_bf16 v[224:227], v[56:59], v[100:103], v[224:227]\n\t"
      "v_cvt_pk_bf16_f32 v51, v38, v39\n\t"
      "v_lshrrev_b32 v31, 8, v19\n\t"
      "v_mfma_f32_16x16x32_bf16 v[224:227], v[64:67], v[96:99], v[224:227]\n\t"
      "v_and_b32 v28, v13, v19\n\t"
      "v_and_b32 v29, v14, v19\n\t"
      "v_mfma_f32_16x16x32_bf16 v[228:231], v[68:71], v[96:99], v[228:231]\n\t"
      "ds_read_b128 v[96:99], v8 offset:8192\n\t"
      "v_and_b32 v30, v13, v31\n\t"
      "v_mfma_f32_16x16x32_bf16 v[228:231], v[60:63], v[100:103], v[228:231]\n\t"
      "ds_read_b128 v[100:103], v9 offset:8192\n\t"
      "v_and_b32 v31, v14, v31\n\t"
      "v_mfma_f32_16x16x32_bf16 v[236:239], v[60:63], v[92:95], v[236:239]\n\t"
      "ds_read_b64 v[18:19], v10 offset:22528\n\t"
      "v_fma_mix_f32 v32, v28, v21, -v23 op_sel:[0,0,0] op_sel_hi:[1,0,0]\n\t"
      "v_mfma_f32_16x16x32_bf16 v[236:239], v[68:71], v[88:91], v[236:239]\n\t"
      "v_fma_mix_f32 v33, v29, v25, -v23 op_sel:[0,0,0] op_sel_hi:[1,0,0]\n\t"
      "v_fma_mix_f32 v34, v30, v21, -v23 op_sel:[0,0,0] op_sel_hi:[1,0,0]\n\t"
      "v_mfma_f32_16x16x32_bf16 v[232:235], v[64:67], v[88:91], v[232:235]\n\t"
      "ds_read_b128 v[88:91], v8 offset:10240\n\t"
      "v_fma_mix_f32 v35, v31, v25, -v23 op_sel:[0,0,0] op_sel_hi:[1,0,0]\n\t"
      "v_mfma_f32_16x16x32_bf16 v[232:235], v[56:59], v[92:95], v[232:235]\n\t"
      "ds_read_b128 v[92:95], v9 offset:10240\n\t"
      "v_fma_mix_f32 v36, v28, v21, -v23 op_sel:[1,0,0] op_sel_hi:[1,0,0]\n\t"
      "v_mfma_f32_16x16x32_bf16 v[240:243], v[56:59], v[84:87], v[240:243]\n\t"
      "v_fma_mix_f32 v37, v29, v25, -v23 op_sel:[1,0,0] op_sel_hi:[1,0,0]\n\t"
      "v_cvt_pk_bf16_f32 v52, v32, v33\n\t"
      "v_mfma_f32_16x16x32_bf16 v[240:243], v[64:67], v[80:83], v[240:243]\n\t"
      "v_cvt_pk_bf16_f32 v53, v34, v35\n\t"
      "v_fma_mix_f32 v38, v30, v21, -v23 op_sel:[1,0,0] op_sel_hi:[1,0,0]\n\t"
      "v_mfma_f32_16x16x32_bf16 v[244:247], v[68:71], v[80:83], v[244:247]\n\t"
      "ds_read_b128 v[80:83], v8 offset:12288\n\t"
      "v_fma_mix_f32 v39, v31, v25, -v23 op_sel:[1,0,0] op_sel_hi:[1,0,0]\n\t"
      "v_mfma_f32_16x16x32_bf16 v[244:247], v[60:63], v[84:87], v[244:247]\n\t"
      "ds_read_b128 v[84:87], v9 offset:12288\n\t"
      "v_cvt_pk_bf16_f32 v54, v36, v37\n\t"
      "v_mfma_f32_16x16x32_bf16 v[252:255], v[60:63], v[128:131], v[252:255]\n\t"
      "v_cvt_pk_bf16_f32 v55, v38, v39\n\t"
      "v_mfma_f32_16x16x32_bf16 v[252:255], v[68:71], v[132:135], v[252:255]\n\t"
      "v_mfma_f32_16x16x32_bf16 v[248:251], v[64:67], v[132:135], v[248:251]\n\t"
      "ds_read_b128 v[132:135], v8 offset:14336\n\t"
      "v_mfma_f32_16x16x32_bf16 v[248:251], v[56:59], v[128:131], v[248:251]\n\t"
      "ds_read_b128 v[128:131], v9 offset:14336\n\t"
      "v_mfma_f32_16x16x32_bf16 v[136:139], v[40:43], v[76:79], v[136:139]\n\t"
      "v_lshlrev_b32 v28, 16, v26\n\t"
      "s_waitcnt lgkmcnt(3)\n\t"
      "v_mfma_f32_16x16x32_bf16 v[136:139], v[48:51], v[72:75], v[136:139]\n\t"
      "v_and_b32 v29, 0xffff0000, v26\n\t"
      "v_lshlrev_b32 v30, 16, v27\n\t"
      "v_mfma_f32_16x16x32_bf16 v[140:143], v[52:55], v[72:75], v[140:143]\n\t"
      "ds_read_b128 v[72:75], v8 offset:16384\n\t"
      "v_and_b32 v31, 0xffff0000, v27\n\t"
      "v_mfma_f32_16x16x32_bf16 v[140:143], v[44:47], v[76:79], v[140:143]\n\t"
      "ds_read_b128 v[76:79], v9 offset:16384\n\t"
      "v_mul_f32 v20, 0x4b800000, v28\n\t"
      "v_mfma_f32_16x16x32_bf16 v[148:151], v[44:47], v[120:123], v[148:151]\n\t"
      "v_mul_f32 v24, 0x49800000, v28\n\t"
      "v_mul_f32 v22, v28, v30\n\t"
      "v_mfma_f32_16x16x32_bf16 v[148:151], v[52:55], v[124:127], v[148:151]\n\t"
      "v_mul_f32 v21, 0x4b800000, v29\n\t"
      "v_mul_f32 v25, 0x49800000, v29\n\t"
      "v_mfma_f32_16x16x32_bf16 v[144:147], v[48:51], v[124:127], v[144:147]\n\t"
      "ds_read_b128 v[124:127], v8 offset:18432\n\t"
      "v_mul_f32 v23, v29, v31\n\t"
      "v_mfma_f32_16x16x32_bf16 v[144:147], v[40:43], v[120:123], v[144:147]\n\t"
      "ds_read_b128 v[120:123], v9 offset:18432\n\t"
      "ds_read2st64_b32 v[26:27], v11 offset0:12 offset1:28\n\t"
      "v_mfma_f32_16x16x32_bf16 v[152:155], v[40:43], v[112:115], v[152:155]\n\t"
      "v_lshrrev_b32 v31, 8, v16\n\t"
      "v_and_b32 v28, v13, v16\n\t"
      "v_mfma_f32_16x16x32_bf16 v[152:155], v[48:51], v[116:119], v[152:155]\n\t"
      "v_and_b32 v29, v14, v16\n\t"
      "v_and_b32 v30, v13, v31\n\t"
      "v_mfma_f32_16x16x32_bf16 v[156:159], v[52:55], v[116:119], v[156:159]\n\t"
      "ds_read_b128 v[116:119], v8 offset:20480\n\t"
      "v_and_b32 v31, v14, v31\n\t"
      "v_mfma_f32_16x16x32_bf16 v[156:159], v[44:47], v[112:115], v[156:159]\n\t"
      "ds_read_b128 v[112:115], v9 offset:20480\n\t"
      "v_fma_mix_f32 v32, v28, v20, -v22 op_sel:[0,0,0] op_sel_hi:[1,0,0]\n\t"
      "v_mfma_f32_16x16x32_bf16 v[164:167], v[44:47], v[104:107], v[164:167]\n\t"
      "v_fma_mix_f32 v33, v29, v24, -v22 op_sel:[0,0,0] op_sel_hi:[1,0,0]\n\t"
      "v_fma_mix_f32 v34, v30, v20, -v22 op_sel:[0,0,0] op_sel_hi:[1,0,0]\n\t"
      "v_mfma_f32_16x16x32_bf16 v[164:167], v[52:55], v[108:111], v[164:167]\n\t"
      "v_fma_mix_f32 v35, v31, v24, -v22 op_sel:[0,0,0] op_sel_hi:[1,0,0]\n\t"
      "v_fma_mix_f32 v36, v28, v20, -v22 op_sel:[1,0,0] op_sel_hi:[1,0,0]\n\t"
      "v_mfma_f32_16x16x32_bf16 v[160:163], v[48:51], v[108:111], v[160:163]\n\t"
      "ds_read_b128 v[108:111], v8 offset:22528\n\t"
      "v_fma_mix_f32 v37, v29, v24, -v22 op_sel:[1,0,0] op_sel_hi:[1,0,0]\n\t"
      "v_mfma_f32_16x16x32_bf16 v[160:163], v[40:43], v[104:107], v[160:163]\n\t"
      "ds_read_b128 v[104:107], v9 offset:22528\n\t"
      "v_cvt_pk_bf16_f32 v56, v32, v33\n\t"
      "v_mfma_f32_16x16x32_bf16 v[168:171], v[40:43], v[96:99], v[168:171]\n\t"
      "v_cvt_pk_bf16_f32 v57, v34, v35\n\t"
      "v_fma_mix_f32 v38, v30, v20, -v22 op_sel:[1,0,0] op_sel_hi:[1,0,0]\n\t"
      "v_mfma_f32_16x16x32_bf16 v[168:171], v[48:51], v[100:103], v[168:171]\n\t"
      "v_fma_mix_f32 v39, v31, v24, -v22 op_sel:[1,0,0] op_sel_hi:[1,0,0]\n\t"
      "v_cvt_pk_bf16_f32 v58, v36, v37\n\t"
      "v_mfma_f32_16x16x32_bf16 v[172:175], v[52:55], v[100:103], v[172:175]\n\t"
      "ds_read_b128 v[100:103], v8 offset:24576\n\t"
      "v_cvt_pk_bf16_f32 v59, v38, v39\n\t"
      "v_mfma_f32_16x16x32_bf16 v[172:175], v[44:47], v[96:99], v[172:175]\n\t"
      "ds_read_b128 v[96:99], v9 offset:24576\n\t"
      "v_lshrrev_b32 v31, 8, v17\n\t"
      "v_mfma_f32_16x16x32_bf16 v[180:183], v[44:47], v[88:91], v[180:183]\n\t"
      "v_and_b32 v28, v13, v17\n\t"
      "v_and_b32 v29, v14, v17\n\t"
      "v_mfma_f32_16x16x32_bf16 v[180:183], v[52:55], v[92:95], v[180:183]\n\t"
      "v_and_b32 v30, v13, v31\n\t"
      "v_and_b32 v31, v14, v31\n\t"
      "v_mfma_f32_16x16x32_bf16 v[176:179], v[48:51], v[92:95], v[176:179]\n\t"
      "ds_read_b128 v[92:95], v8 offset:26624\n\t"
      "ds_read_b64 v[16:17], v10 offset:0\n\t"
      "v_mfma_f32_16x16x32_bf16 v[176:179], v[40:43], v[88:91], v[176:179]\n\t"
      "ds_read_b128 v[88:91], v9 offset:26624\n\t"
      "v_fma_mix_f32 v32, v28, v21, -v23 op_sel:[0,0,0] op_sel_hi:[1,0,0]\n\t"
      "v_mfma_f32_16x16x32_bf16 v[184:187], v[40:43], v[80:83], v[184:187]\n\t"
      "v_fma_mix_f32 v33, v29, v25, -v23 op_sel:[0,0,0] op_sel_hi:[1,0,0]\n\t"
      "s_waitcnt lgkmcnt(4)\n\t"
      "v_mfma_f32_16x16x32_bf16 v[184:187], v[48:51], v[84:87], v[184:187]\n\t"
      "v_fma_mix_f32 v34, v30, v21, -v23 op_sel:[0,0,0] op_sel_hi:[1,0,0]\n\t"
      "v_fma_mix_f32 v35, v31, v25, -v23 op_sel:[0,0,0] op_sel_hi:[1,0,0]\n\t"
      "v_mfma_f32_16x16x32_bf16 v[188:191], v[52:55], v[84:87], v[188:191]\n\t"
      "ds_read_b128 v[84:87], v8 offset:28672\n\t"
      "v_fma_mix_f32 v36, v28, v21, -v23 op_sel:[1,0,0] op_sel_hi:[1,0,0]\n\t"
      "v_mfma_f32_16x16x32_bf16 v[188:191], v[44:47], v[80:83], v[188:191]\n\t"
      "ds_read_b128 v[80:83], v9 offset:28672\n\t"
      "v_fma_mix_f32 v37, v29, v25, -v23 op_sel:[1,0,0] op_sel_hi:[1,0,0]\n\t"
      "v_mfma_f32_16x16x32_bf16 v[196:199], v[44:47], v[132:135], v[196:199]\n\t"
      "v_cvt_pk_bf16_f32 v60, v32, v33\n\t"
      "v_cvt_pk_bf16_f32 v61, v34, v35\n\t"
      "v_mfma_f32_16x16x32_bf16 v[196:199], v[52:55], v[128:131], v[196:199]\n\t"
      "s_barrier\n\t"
      "v_mfma_f32_16x16x32_bf16 v[192:195], v[48:51], v[128:131], v[192:195]\n\t"
      "ds_read_b128 v[128:131], v8 offset:32768\n\t"
      "v_fma_mix_f32 v38, v30, v21, -v23 op_sel:[1,0,0] op_sel_hi:[1,0,0]\n\t"
      "v_mfma_f32_16x16x32_bf16 v[192:195], v[40:43], v[132:135], v[192:195]\n\t"
      "ds_read_b128 v[132:135], v9 offset:32768\n\t"
      "v_fma_mix_f32 v39, v31, v25, -v23 op_sel:[1,0,0] op_sel_hi:[1,0,0]\n\t"
      "v_mfma_f32_16x16x32_bf16 v[200:203], v[40:43], v[72:75], v[200:203]\n\t"
      "v_cvt_pk_bf16_f32 v62, v36, v37\n\t"
      "v_cvt_pk_bf16_f32 v63, v38, v39\n\t"
      "v_mfma_f32_16x16x32_bf16 v[200:203], v[48:51], v[76:79], v[200:203]\n\t"
      "v_lshrrev_b32 v31, 8, v18\n\t"
      "v_and_b32 v28, v13, v18\n\t"
      "v_mfma_f32_16x16x32_bf16 v[204:207], v[52:55], v[76:79], v[204:207]\n\t"
      "ds_read_b128 v[76:79], v8 offset:34816\n\t"
      "v_and_b32 v29, v14, v18\n\t"
      "v_mfma_f32_16x16x32_bf16 v[204:207], v[44:47], v[72:75], v[204:207]\n\t"
      "ds_read_b128 v[72:75], v9 offset:34816\n\t"
      "v_and_b32 v30, v13, v31\n\t"
      "v_mfma_f32_16x16x32_bf16 v[212:215], v[44:47], v[124:127], v[212:215]\n\t"
      "v_and_b32 v31, v14, v31\n\t"
      "v_fma_mix_f32 v32, v28, v20, -v22 op_sel:[0,0,0] op_sel_hi:[1,0,0]\n\t"
      "v_mfma_f32_16x16x32_bf16 v[212:215], v[52:55], v[120:123], v[212:215]\n\t"
      "v_fma_mix_f32 v33, v29, v24, -v22 op_sel:[0,0,0] op_sel_hi:[1,0,0]\n\t"
      "v_fma_mix_f32 v34, v30, v20, -v22 op_sel:[0,0,0] op_sel_hi:[1,0,0]\n\t"
      "v_mfma_f32_16x16x32_bf16 v[208:211], v[48:51], v[120:123], v[208:211]\n\t"
      "ds_read_b128 v[120:123], v8 offset:36864\n\t"
      "v_fma_mix_f32 v35, v31, v24, -v22 op_sel:[0,0,0] op_sel_hi:[1,0,0]\n\t"
      "v_mfma_f32_16x16x32_bf16 v[208:211], v[40:43], v[124:127], v[208:211]\n\t"
      "ds_read_b128 v[124:127], v9 offset:36864\n\t"
      "v_fma_mix_f32 v36, v28, v20, -v22 op_sel:[1,0,0] op_sel_hi:[1,0,0]\n\t"
      "v_mfma_f32_16x16x32_bf16 v[216:219], v[40:43], v[116:119], v[216:219]\n\t"
      "v_fma_mix_f32 v37, v29, v24, -v22 op_sel:[1,0,0] op_sel_hi:[1,0,0]\n\t"
      "v_cvt_pk_bf16_f32 v64, v32, v33\n\t"
      "v_mfma_f32_16x16x32_bf16 v[216:219], v[48:51], v[112:115], v[216:219]\n\t"
      "v_cvt_pk_bf16_f32 v65, v34, v35\n\t"
      "v_fma_mix_f32 v38, v30, v20, -v22 op_sel:[1,0,0] op_sel_hi:[1,0,0]\n\t"
      "v_mfma_f32_16x16x32_bf16 v[220:223], v[52:55], v[112:115], v[220:223]\n\t"
      "ds_read_b128 v[112:115], v8 offset:38912\n\t"
      "v_fma_mix_f32 v39, v31, v24, -v22 op_sel:[1,0,0] op_sel_hi:[1,0,0]\n\t"
      "v_mfma_f32_16x16x32_bf16 v[220:223], v[44:47], v[116:119], v[220:223]\n\t"
      "ds_read_b128 v[116:119], v9 offset:38912\n\t"
      "v_cvt_pk_bf16_f32 v66, v36, v37\n\t"
      "v_mfma_f32_16x16x32_bf16 v[228:231], v[44:47], v[108:111], v[228:231]\n\t"
      "v_cvt_pk_bf16_f32 v67, v38, v39\n\t"
      "v_lshrrev_b32 v31, 8, v19\n\t"
      "v_mfma_f32_16x16x32_bf16 v[228:231], v[52:55], v[104:107], v[228:231]\n\t"
      "v_and_b32 v28, v13, v19\n\t"
      "v_and_b32 v29, v14, v19\n\t"
      "v_mfma_f32_16x16x32_bf16 v[224:227], v[48:51], v[104:107], v[224:227]\n\t"
      "ds_read_b128 v[104:107], v8 offset:40960\n\t"
      "v_and_b32 v30, v13, v31\n\t"
      "v_mfma_f32_16x16x32_bf16 v[224:227], v[40:43], v[108:111], v[224:227]\n\t"
      "ds_read_b128 v[108:111], v9 offset:40960\n\t"
      "v_and_b32 v31, v14, v31\n\t"
      "v_mfma_f32_16x16x32_bf16 v[232:235], v[40:43], v[100:103], v[232:235]\n\t"
      "ds_read_b64 v[18:19], v10 offset:2048\n\t"
      "s_waitcnt lgkmcnt(4)\n\t"
      "v_mfma_f32_16x16x32_bf16 v[232:235], v[48:51], v[96:99], v[232:235]\n\t"
      "v_fma_mix_f32 v32, v28, v21, -v23 op_sel:[0,0,0] op_sel_hi:[1,0,0]\n\t"
      "v_fma_mix_f32 v33, v29, v25, -v23 op_sel:[0,0,0] op_sel_hi:[1,0,0]\n\t"
      "v_mfma_f32_16x16x32_bf16 v[236:239], v[52:55], v[96:99], v[236:239]\n\t"
      "ds_read_b128 v[96:99], v8 offset:43008\n\t"
      "v_fma_mix_f32 v34, v30, v21, -v23 op_sel:[0,0,0] op_sel_hi:[1,0,0]\n\t"
      "v_mfma_f32_16x16x32_bf16 v[236:239], v[44:47], v[100:103], v[236:239]\n\t"
      "ds_read_b128 v[100:103], v9 offset:43008\n\t"
      "v_fma_mix_f32 v35, v31, v25, -v23 op_sel:[0,0,0] op_sel_hi:[1,0,0]\n\t"
      "v_mfma_f32_16x16x32_bf16 v[244:247], v[44:47], v[92:95], v[244:247]\n\t"
      "v_fma_mix_f32 v36, v28, v21, -v23 op_sel:[1,0,0] op_sel_hi:[1,0,0]\n\t"
      "v_fma_mix_f32 v37, v29, v25, -v23 op_sel:[1,0,0] op_sel_hi:[1,0,0]\n\t"
      "v_mfma_f32_16x16x32_bf16 v[244:247], v[52:55], v[88:91], v[244:247]\n\t"
      "v_cvt_pk_bf16_f32 v68, v32, v33\n\t"
      "v_cvt_pk_bf16_f32 v69, v34, v35\n\t"
      "v_mfma_f32_16x16x32_bf16 v[240:243], v[48:51], v[88:91], v[240:243]\n\t"
      "ds_read_b128 v[88:91], v8 offset:45056\n\t"
      "v_fma_mix_f32 v38, v30, v21, -v23 op_sel:[1,0,0] op_sel_hi:[1,0,0]\n\t"
      "v_mfma_f32_16x16x32_bf16 v[240:243], v[40:43], v[92:95], v[240:243]\n\t"
      "ds_read_b128 v[92:95], v9 offset:45056\n\t"
      "v_fma_mix_f32 v39, v31, v25, -v23 op_sel:[1,0,0] op_sel_hi:[1,0,0]\n\t"
      "v_mfma_f32_16x16x32_bf16 v[248:251], v[40:43], v[84:87], v[248:251]\n\t"
      "v_cvt_pk_bf16_f32 v70, v36, v37\n\t"
      "v_cvt_pk_bf16_f32 v71, v38, v39\n\t"
      "v_mfma_f32_16x16x32_bf16 v[248:251], v[48:51], v[80:83], v[248:251]\n\t"
      "v_mfma_f32_16x16x32_bf16 v[252:255], v[52:55], v[80:83], v[252:255]\n\t"
      "ds_read_b128 v[80:83], v8 offset:47104\n\t"
      "v_mfma_f32_16x16x32_bf16 v[252:255], v[44:47], v[84:87], v[252:255]\n\t"
      "ds_read_b128 v[84:87], v9 offset:47104\n\t"
      "v_mfma_f32_16x16x32_bf16 v[140:143], v[60:63], v[128:131], v[140:143]\n\t"
      "v_lshlrev_b32 v28, 16, v26\n\t"
      "v_and_b32 v29, 0xffff0000, v26\n\t"
      "v_mfma_f32_16x16x32_bf16 v[140:143], v[68:71], v[132:135], v[140:143]\n\t"
      "v_lshlrev_b32 v30, 16, v27\n\t"
      "v_and_b32 v31, 0xffff0000, v27\n\t"
      "v_mfma_f32_16x16x32_bf16 v[136:139], v[64:67], v[132:135], v[136:139]\n\t"
      "ds_read_b128 v[132:135], v8 offset:49152\n\t"
      "v_mul_f32 v20, 0x4b800000, v28\n\t"
      "v_mfma_f32_16x16x32_bf16 v[136:139], v[56:59], v[128:131], v[136:139]\n\t"
      "ds_read_b128 v[128:131], v9 offset:49152\n\t"
      "v_mul_f32 v24, 0x49800000, v28\n\t"
      "v_mfma_f32_16x16x32_bf16 v[144:147], v[56:59], v[76:79], v[144:147]\n\t"
      "v_mul_f32 v22, v28, v30\n\t"
      "v_mul_f32 v21, 0x4b800000, v29\n\t"
      "v_mfma_f32_16x16x32_bf16 v[144:147], v[64:67], v[72:75], v[144:147]\n\t"
      "v_mul_f32 v25, 0x49800000, v29\n\t"
      "v_mul_f32 v23, v29, v31\n\t"
      "v_mfma_f32_16x16x32_bf16 v[148:151], v[68:71], v[72:75], v[148:151]\n\t"
      "ds_read_b128 v[72:75], v8 offset:51200\n\t"
      "ds_read2st64_b32 v[26:27], v11 offset0:13 offset1:29\n\t"
      "v_mfma_f32_16x16x32_bf16 v[148:151], v[60:63], v[76:79], v[148:151]\n\t"
      "ds_read_b128 v[76:79], v9 offset:51200\n\t"
      "v_lshrrev_b32 v31, 8, v16\n\t"
      "v_mfma_f32_16x16x32_bf16 v[156:159], v[60:63], v[120:123], v[156:159]\n\t"
      "v_and_b32 v28, v13, v16\n\t"
      "v_and_b32 v29, v14, v16\n\t"
      "v_mfma_f32_16x16x32_bf16 v[156:159], v[68:71], v[124:127], v[156:159]\n\t"
      "v_and_b32 v30, v13, v31\n\t"
      "v_and_b32 v31, v14, v31\n\t"
      "v_mfma_f32_16x16x32_bf16 v[152:155], v[64:67], v[124:127], v[152:155]\n\t"
      "ds_read_b128 v[124:127], v8 offset:53248\n\t"
      "v_fma_mix_f32 v32, v28, v20, -v22 op_sel:[0,0,0] op_sel_hi:[1,0,0]\n\t"
      "v_mfma_f32_16x16x32_bf16 v[152:155], v[56:59], v[120:123], v[152:155]\n\t"
      "ds_read_b128 v[120:123], v9 offset:53248\n\t"
      "v_fma_mix_f32 v33, v29, v24, -v22 op_sel:[0,0,0] op_sel_hi:[1,0,0]\n\t"
      "v_mfma_f32_16x16x32_bf16 v[160:163], v[56:59], v[112:115], v[160:163]\n\t"
      "v_fma_mix_f32 v34, v30, v20, -v22 op_sel:[0,0,0] op_sel_hi:[1,0,0]\n\t"
      "s_waitcnt lgkmcnt(3)\n\t"
      "v_mfma_f32_16x16x32_bf16 v[160:163], v[64:67], v[116:119], v[160:163]\n\t"
      "v_fma_mix_f32 v35, v31, v24, -v22 op_sel:[0,0,0] op_sel_hi:[1,0,0]\n\t"
      "v_fma_mix_f32 v36, v28, v20, -v22 op_sel:[1,0,0] op_sel_hi:[1,0,0]\n\t"
      "v_mfma_f32_16x16x32_bf16 v[164:167], v[68:71], v[116:119], v[164:167]\n\t"
      "ds_read_b128 v[116:119], v8 offset:55296\n\t"
      "v_fma_mix_f32 v37, v29, v24, -v22 op_sel:[1,0,0] op_sel_hi:[1,0,0]\n\t"
      "v_mfma_f32_16x16x32_bf16 v[164:167], v[60:63], v[112:115], v[164:167]\n\t"
      "ds_read_b128 v[112:115], v9 offset:55296\n\t"
      "v_cvt_pk_bf16_f32 v40, v32, v33\n\t"
      "v_mfma_f32_16x16x32_bf16 v[172:175], v[60:63], v[104:107], v[172:175]\n\t"
      "v_cvt_pk_bf16_f32 v41, v34, v35\n\t"
      "v_fma_mix_f32 v38, v30, v20, -v22 op_sel:[1,0,0] op_sel_hi:[1,0,0]\n\t"
      "v_mfma_f32_16x16x32_bf16 v[172:175], v[68:71], v[108:111], v[172:175]\n\t"
      "v_fma_mix_f32 v39, v31, v24, -v22 op_sel:[1,0,0] op_sel_hi:[1,0,0]\n\t"
      "v_cvt_pk_bf16_f32 v42, v36, v37\n\t"
      "v_mfma_f32_16x16x32_bf16 v[168:171], v[64:67], v[108:111], v[168:171]\n\t"
      "ds_read_b128 v[108:111], v8 offset:57344\n\t"
      "v_cvt_pk_bf16_f32 v43, v38, v39\n\t"
      "v_mfma_f32_16x16x32_bf16 v[168:171], v[56:59], v[104:107], v[168:171]\n\t"
      "ds_read_b128 v[104:107], v9 offset:57344\n\t"
      "v_lshrrev_b32 v31, 8, v17\n\t"
      "v_mfma_f32_16x16x32_bf16 v[176:179], v[56:59], v[96:99], v[176:179]\n\t"
      "v_and_b32 v28, v13, v17\n\t"
      "v_and_b32 v29, v14, v17\n\t"
      "v_mfma_f32_16x16x32_bf16 v[176:179], v[64:67], v[100:103], v[176:179]\n\t"
      "v_and_b32 v30, v13, v31\n\t"
      "v_and_b32 v31, v14, v31\n\t"
      "v_mfma_f32_16x16x32_bf16 v[180:183], v[68:71], v[100:103], v[180:183]\n\t"
      "ds_read_b128 v[100:103], v8 offset:59392\n\t"
      "ds_read_b64 v[16:17], v10 offset:4096\n\t"
      "v_mfma_f32_16x16x32_bf16 v[180:183], v[60:63], v[96:99], v[180:183]\n\t"
      "ds_read_b128 v[96:99], v9 offset:59392\n\t"
      "v_fma_mix_f32 v32, v28, v21, -v23 op_sel:[0,0,0] op_sel_hi:[1,0,0]\n\t"
      "v_mfma_f32_16x16x32_bf16 v[188:191], v[60:63], v[88:91], v[188:191]\n\t"
      "v_fma_mix_f32 v33, v29, v25, -v23 op_sel:[0,0,0] op_sel_hi:[1,0,0]\n\t"
      "v_fma_mix_f32 v34, v30, v21, -v23 op_sel:[0,0,0] op_sel_hi:[1,0,0]\n\t"
      "v_mfma_f32_16x16x32_bf16 v[188:191], v[68:71], v[92:95], v[188:191]\n\t"
      "v_fma_mix_f32 v35, v31, v25, -v23 op_sel:[0,0,0] op_sel_hi:[1,0,0]\n\t"
      "v_fma_mix_f32 v36, v28, v21, -v23 op_sel:[1,0,0] op_sel_hi:[1,0,0]\n\t"
      "v_mfma_f32_16x16x32_bf16 v[184:187], v[64:67], v[92:95], v[184:187]\n\t"
      "ds_read_b128 v[92:95], v8 offset:61440\n\t"
      "v_fma_mix_f32 v37, v29, v25, -v23 op_sel:[1,0,0] op_sel_hi:[1,0,0]\n\t"
      "v_mfma_f32_16x16x32_bf16 v[184:187], v[56:59], v[88:91], v[184:187]\n\t"
      "ds_read_b128 v[88:91], v9 offset:61440\n\t"
      "v_cvt_pk_bf16_f32 v44, v32, v33\n\t"
      "v_mfma_f32_16x16x32_bf16 v[192:195], v[56:59], v[80:83], v[192:195]\n\t"
      "v_cvt_pk_bf16_f32 v45, v34, v35\n\t"
      "v_fma_mix_f32 v38, v30, v21, -v23 op_sel:[1,0,0] op_sel_hi:[1,0,0]\n\t"
      "v_mfma_f32_16x16x32_bf16 v[192:195], v[64:67], v[84:87], v[192:195]\n\t"
      "s_barrier\n\t"
      "v_mfma_f32_16x16x32_bf16 v[196:199], v[68:71], v[84:87], v[196:199]\n\t"
      "ds_read_b128 v[84:87], v6 offset:0\n\t"
      "v_fma_mix_f32 v39, v31, v25, -v23 op_sel:[1,0,0] op_sel_hi:[1,0,0]\n\t"
      "v_mfma_f32_16x16x32_bf16 v[196:199], v[60:63], v[80:83], v[196:199]\n\t"
      "ds_read_b128 v[80:83], v7 offset:0\n\t"
      "v_cvt_pk_bf16_f32 v46, v36, v37\n\t"
      "v_mfma_f32_16x16x32_bf16 v[204:207], v[60:63], v[132:135], v[204:207]\n\t"
      "v_cvt_pk_bf16_f32 v47, v38, v39\n\t"
      "v_lshrrev_b32 v31, 8, v18\n\t"
      "v_mfma_f32_16x16x32_bf16 v[204:207], v[68:71], v[128:131], v[204:207]\n\t"
      "v_and_b32 v28, v13, v18\n\t"
      "v_and_b32 v29, v14, v18\n\t"
      "v_mfma_f32_16x16x32_bf16 v[200:203], v[64:67], v[128:131], v[200:203]\n\t"
      "ds_read_b128 v[128:131], v6 offset:2048\n\t"
      "v_and_b32 v30, v13, v31\n\t"
      "v_mfma_f32_16x16x32_bf16 v[200:203], v[56:59], v[132:135], v[200:203]\n\t"
      "ds_read_b128 v[132:135], v7 offset:2048\n\t"
      "v_and_b32 v31, v14, v31\n\t"
      "v_mfma_f32_16x16x32_bf16 v[208:211], v[56:59], v[72:75], v[208:211]\n\t"
      "v_fma_mix_f32 v32, v28, v20, -v22 op_sel:[0,0,0] op_sel_hi:[1,0,0]\n\t"
      "s_waitcnt lgkmcnt(3)\n\t"
      "v_mfma_f32_16x16x32_bf16 v[208:211], v[64:67], v[76:79], v[208:211]\n\t"
      "v_fma_mix_f32 v33, v29, v24, -v22 op_sel:[0,0,0] op_sel_hi:[1,0,0]\n\t"
      "v_fma_mix_f32 v34, v30, v20, -v22 op_sel:[0,0,0] op_sel_hi:[1,0,0]\n\t"
      "v_mfma_f32_16x16x32_bf16 v[212:215], v[68:71], v[76:79], v[212:215]\n\t"
      "ds_read_b128 v[76:79], v6 offset:4096\n\t"
      "v_fma_mix_f32 v35, v31, v24, -v22 op_sel:[0,0,0] op_sel_hi:[1,0,0]\n\t"
      "v_mfma_f32_16x16x32_bf16 v[212:215], v[60:63], v[72:75], v[212:215]\n\t"
      "ds_read_b128 v[72:75], v7 offset:4096\n\t"
      "v_fma_mix_f32 v36, v28, v20, -v22 op_sel:[1,0,0] op_sel_hi:[1,0,0]\n\t"
      "v_mfma_f32_16x16x32_bf16 v[220:223], v[60:63], v[124:127], v[220:223]\n\t"
      "v_fma_mix_f32 v37, v29, v24, -v22 op_sel:[1,0,0] op_sel_hi:[1,0,0]\n\t"
      "v_cvt_pk_bf16_f32 v48, v32, v33\n\t"
      "v_mfma_f32_16x16x32_bf16 v[220:223], v[68:71], v[120:123], v[220:223]\n\t"
      "v_cvt_pk_bf16_f32 v49, v34, v35\n\t"
      "v_fma_mix_f32 v38, v30, v20, -v22 op_sel:[1,0,0] op_sel_hi:[1,0,0]\n\t"
      "v_mfma_f32_16x16x32_bf16 v[216:219], v[64:67], v[120:123], v[216:219]\n\t"
      "ds_read_b128 v[120:123], v6 offset:6144\n\t"
      "v_fma_mix_f32 v39, v31, v24, -v22 op_sel:[1,0,0] op_sel_hi:[1,0,0]\n\t"
      "v_mfma_f32_16x16x32_bf16 v[216:219], v[56:59], v[124:127], v[216:219]\n\t"
      "ds_read_b128 v[124:127], v7 offset:6144\n\t"
      "v_cvt_pk_bf16_f32 v50, v36, v37\n\t"
      "v_mfma_f32_16x16x32_bf16 v[224:227], v[56:59], v[116:119], v[224:227]\n\t"
      "v_cvt_pk_bf16_f32 v51, v38, v39\n\t"
      "v_lshrrev_b32 v31, 8, v19\n\t"
      "v_mfma_f32_16x16x32_bf16 v[224:227], v[64:67], v[112:115], v[224:227]\n\t"
      "v_and_b32 v28, v13, v19\n\t"
      "v_and_b32 v29, v14, v19\n\t"
      "v_mfma_f32_16x16x32_bf16 v[228:231], v[68:71], v[112:115], v[228:231]\n\t"
      "ds_read_b128 v[112:115], v6 offset:8192\n\t"
      "v_and_b32 v30, v13, v31\n\t"
      "v_mfma_f32_16x16x32_bf16 v[228:231], v[60:63], v[116:119], v[228:231]\n\t"
      "ds_read_b128 v[116:119], v7 offset:8192\n\t"
      "v_and_b32 v31, v14, v31\n\t"
      "v_mfma_f32_16x16x32_bf16 v[236:239], v[60:63], v[108:111], v[236:239]\n\t"
      "ds_read_b64 v[18:19], v10 offset:6144\n\t"
      "v_fma_mix_f32 v32, v28, v21, -v23 op_sel:[0,0,0] op_sel_hi:[1,0,0]\n\t"
      "v_mfma_f32_16x16x32_bf16 v[236:239], v[68:71], v[104:107], v[236:239]\n\t"
      "v_fma_mix_f32 v33, v29, v25, -v23 op_sel:[0,0,0] op_sel_hi:[1,0,0]\n\t"
      "v_fma_mix_f32 v34, v30, v21, -v23 op_sel:[0,0,0] op_sel_hi:[1,0,0]\n\t"
      "v_mfma_f32_16x16x32_bf16 v[232:235], v[64:67], v[104:107], v[232:235]\n\t"
      "ds_read_b128 v[104:107], v6 offset:10240\n\t"
      "v_fma_mix_f32 v35, v31, v25, -v23 op_sel:[0,0,0] op_sel_hi:[1,0,0]\n\t"
      "v_mfma_f32_16x16x32_bf16 v[232:235], v[56:59], v[108:111], v[232:235]\n\t"
      "ds_read_b128 v[108:111], v7 offset:10240\n\t"
      "v_fma_mix_f32 v36, v28, v21, -v23 op_sel:[1,0,0] op_sel_hi:[1,0,0]\n\t"
      "v_mfma_f32_16x16x32_bf16 v[240:243], v[56:59], v[100:103], v[240:243]\n\t"
      "v_fma_mix_f32 v37, v29, v25, -v23 op_sel:[1,0,0] op_sel_hi:[1,0,0]\n\t"
      "v_cvt_pk_bf16_f32 v52, v32, v33\n\t"
      "v_mfma_f32_16x16x32_bf16 v[240:243], v[64:67], v[96:99], v[240:243]\n\t"
      "v_cvt_pk_bf16_f32 v53, v34, v35\n\t"
      "v_fma_mix_f32 v38, v30, v21, -v23 op_sel:[1,0,0] op_sel_hi:[1,0,0]\n\t"
      "v_mfma_f32_16x16x32_bf16 v[244:247], v[68:71], v[96:99], v[244:247]\n\t"
      "ds_read_b128 v[96:99], v6 offset:12288\n\t"
      "v_fma_mix_f32 v39, v31, v25, -v23 op_sel:[1,0,0] op_sel_hi:[1,0,0]\n\t"
      "v_mfma_f32_16x16x32_bf16 v[244:247], v[60:63], v[100:103], v[244:247]\n\t"
      "ds_read_b128 v[100:103], v7 offset:12288\n\t"
      "v_cvt_pk_bf16_f32 v54, v36, v37\n\t"
      "v_mfma_f32_16x16x32_bf16 v[252:255], v[60:63], v[92:95], v[252:255]\n\t"
      "v_cvt_pk_bf16_f32 v55, v38, v39\n\t"
      "v_mfma_f32_16x16x32_bf16 v[252:255], v[68:71], v[88:91], v[252:255]\n\t"
      "v_mfma_f32_16x16x32_bf16 v[248:251], v[64:67], v[88:91], v[248:251]\n\t"
      "ds_read_b128 v[88:91], v6 offset:14336\n\t"
      "v_mfma_f32_16x16x32_bf16 v[248:251], v[56:59], v[92:95], v[248:251]\n\t"
      "ds_read_b128 v[92:95], v7 offset:14336\n\t"
      "v_mfma_f32_16x16x32_bf16 v[136:139], v[40:43], v[84:87], v[136:139]\n\t"
      "v_lshlrev_b32 v28, 16, v26\n\t"
      "s_waitcnt lgkmcnt(3)\n\t"
      "v_mfma_f32_16x16x32_bf16 v[136:139], v[48:51], v[80:83], v[136:139]\n\t"
      "v_and_b32 v29, 0xffff0000, v26\n\t"
      "v_lshlrev_b32 v30, 16, v27\n\t"
      "v_mfma_f32_16x16x32_bf16 v[140:143], v[52:55], v[80:83], v[140:143]\n\t"
      "ds_read_b128 v[80:83], v6 offset:16384\n\t"
      "v_and_b32 v31, 0xffff0000, v27\n\t"
      "v_mfma_f32_16x16x32_bf16 v[140:143], v[44:47], v[84:87], v[140:143]\n\t"
      "ds_read_b128 v[84:87], v7 offset:16384\n\t"
      "v_mul_f32 v20, 0x4b800000, v28\n\t"
      "v_mfma_f32_16x16x32_bf16 v[148:151], v[44:47], v[128:131], v[148:151]\n\t"
      "v_mul_f32 v24, 0x49800000, v28\n\t"
      "v_mul_f32 v22, v28, v30\n\t"
      "v_mfma_f32_16x16x32_bf16 v[148:151], v[52:55], v[132:135], v[148:151]\n\t"
      "v_mul_f32 v21, 0x4b800000, v29\n\t"
      "v_mul_f32 v25, 0x49800000, v29\n\t"
      "v_mfma_f32_16x16x32_bf16 v[144:147], v[48:51], v[132:135], v[144:147]\n\t"
      "ds_read_b128 v[132:135], v6 offset:18432\n\t"
      "v_mul_f32 v23, v29, v31\n\t"
      "v_mfma_f32_16x16x32_bf16 v[144:147], v[40:43], v[128:131], v[144:147]\n\t"
      "ds_read_b128 v[128:131], v7 offset:18432\n\t"
      "ds_read2st64_b32 v[26:27], v11 offset0:14 offset1:30\n\t"
      "v_mfma_f32_16x16x32_bf16 v[152:155], v[40:43], v[76:79], v[152:155]\n\t"
      "v_lshrrev_b32 v31, 8, v16\n\t"
      "v_and_b32 v28, v13, v16\n\t"
      "v_mfma_f32_16x16x32_bf16 v[152:155], v[48:51], v[72:75], v[152:155]\n\t"
      "v_and_b32 v29, v14, v16\n\t"
      "v_and_b32 v30, v13, v31\n\t"
      "v_mfma_f32_16x16x32_bf16 v[156:159], v[52:55], v[72:75], v[156:159]\n\t"
      "ds_read_b128 v[72:75], v6 offset:20480\n\t"
      "v_and_b32 v31, v14, v31\n\t"
      "v_mfma_f32_16x16x32_bf16 v[156:159], v[44:47], v[76:79], v[156:159]\n\t"
      "ds_read_b128 v[76:79], v7 offset:20480\n\t"
      "v_fma_mix_f32 v32, v28, v20, -v22 op_sel:[0,0,0] op_sel_hi:[1,0,0]\n\t"
      "v_mfma_f32_16x16x32_bf16 v[164:167], v[44:47], v[120:123], v[164:167]\n\t"
      "v_fma_mix_f32 v33, v29, v24, -v22 op_sel:[0,0,0] op_sel_hi:[1,0,0]\n\t"
      "v_fma_mix_f32 v34, v30, v20, -v22 op_sel:[0,0,0] op_sel_hi:[1,0,0]\n\t"
      "v_mfma_f32_16x16x32_bf16 v[164:167], v[52:55], v[124:127], v[164:167]\n\t"
      "v_fma_mix_f32 v35, v31, v24, -v22 op_sel:[0,0,0] op_sel_hi:[1,0,0]\n\t"
      "v_fma_mix_f32 v36, v28, v20, -v22 op_sel:[1,0,0] op_sel_hi:[1,0,0]\n\t"
      "v_mfma_f32_16x16x32_bf16 v[160:163], v[48:51], v[124:127], v[160:163]\n\t"
      "ds_read_b128 v[124:127], v6 offset:22528\n\t"
      "v_fma_mix_f32 v37, v29, v24, -v22 op_sel:[1,0,0] op_sel_hi:[1,0,0]\n\t"
      "v_mfma_f32_16x16x32_bf16 v[160:163], v[40:43], v[120:123], v[160:163]\n\t"
      "ds_read_b128 v[120:123], v7 offset:22528\n\t"
      "v_cvt_pk_bf16_f32 v56, v32, v33\n\t"
      "v_mfma_f32_16x16x32_bf16 v[168:171], v[40:43], v[112:115], v[168:171]\n\t"
      "v_cvt_pk_bf16_f32 v57, v34, v35\n\t"
      "v_fma_mix_f32 v38, v30, v20, -v22 op_sel:[1,0,0] op_sel_hi:[1,0,0]\n\t"
      "v_mfma_f32_16x16x32_bf16 v[168:171], v[48:51], v[116:119], v[168:171]\n\t"
      "v_fma_mix_f32 v39, v31, v24, -v22 op_sel:[1,0,0] op_sel_hi:[1,0,0]\n\t"
      "v_cvt_pk_bf16_f32 v58, v36, v37\n\t"
      "v_mfma_f32_16x16x32_bf16 v[172:175], v[52:55], v[116:119], v[172:175]\n\t"
      "ds_read_b128 v[116:119], v6 offset:24576\n\t"
      "v_cvt_pk_bf16_f32 v59, v38, v39\n\t"
      "v_mfma_f32_16x16x32_bf16 v[172:175], v[44:47], v[112:115], v[172:175]\n\t"
      "ds_read_b128 v[112:115], v7 offset:24576\n\t"
      "v_lshrrev_b32 v31, 8, v17\n\t"
      "v_mfma_f32_16x16x32_bf16 v[180:183], v[44:47], v[104:107], v[180:183]\n\t"
      "v_and_b32 v28, v13, v17\n\t"
      "v_and_b32 v29, v14, v17\n\t"
      "v_mfma_f32_16x16x32_bf16 v[180:183], v[52:55], v[108:111], v[180:183]\n\t"
      "v_and_b32 v30, v13, v31\n\t"
      "v_and_b32 v31, v14, v31\n\t"
      "v_mfma_f32_16x16x32_bf16 v[176:179], v[48:51], v[108:111], v[176:179]\n\t"
      "ds_read_b128 v[108:111], v6 offset:26624\n\t"
      "ds_read_b64 v[16:17], v10 offset:8192\n\t"
      "v_mfma_f32_16x16x32_bf16 v[176:179], v[40:43], v[104:107], v[176:179]\n\t"
      "ds_read_b128 v[104:107], v7 offset:26624\n\t"
      "v_fma_mix_f32 v32, v28, v21, -v23 op_sel:[0,0,0] op_sel_hi:[1,0,0]\n\t"
      "v_mfma_f32_16x16x32_bf16 v[184:187], v[40:43], v[96:99], v[184:187]\n\t"
      "v_fma_mix_f32 v33, v29, v25, -v23 op_sel:[0,0,0] op_sel_hi:[1,0,0]\n\t"
      "s_waitcnt lgkmcnt(4)\n\t"
      "v_mfma_f32_16x16x32_bf16 v[184:187], v[48:51], v[100:103], v[184:187]\n\t"
      "v_fma_mix_f32 v34, v30, v21, -v23 op_sel:[0,0,0] op_sel_hi:[1,0,0]\n\t"
      "v_fma_mix_f32 v35, v31, v25, -v23 op_sel:[0,0,0] op_sel_hi:[1,0,0]\n\t"
      "v_mfma_f32_16x16x32_bf16 v[188:191], v[52:55], v[100:103], v[188:191]\n\t"
      "ds_read_b128 v[100:103], v6 offset:28672\n\t"
      "v_fma_mix_f32 v36, v28, v21, -v23 op_sel:[1,0,0] op_sel_hi:[1,0,0]\n\t"
      "v_mfma_f32_16x16x32_bf16 v[188:191], v[44:47], v[96:99], v[188:191]\n\t"
      "ds_read_b128 v[96:99], v7 offset:28672\n\t"
      "v_fma_mix_f32 v37, v29, v25, -v23 op_sel:[1,0,0] op_sel_hi:[1,0,0]\n\t"
      "v_mfma_f32_16x16x32_bf16 v[196:199], v[44:47], v[88:91], v[196:199]\n\t"
      "v_cvt_pk_bf16_f32 v60, v32, v33\n\t"
      "v_cvt_pk_bf16_f32 v61, v34, v35\n\t"
      "v_mfma_f32_16x16x32_bf16 v[196:199], v[52:55], v[92:95], v[196:199]\n\t"
      "s_barrier\n\t"
      "v_mfma_f32_16x16x32_bf16 v[192:195], v[48:51], v[92:95], v[192:195]\n\t"
      "v_fma_mix_f32 v38, v30, v21, -v23 op_sel:[1,0,0] op_sel_hi:[1,0,0]\n\t"
      "v_fma_mix_f32 v39, v31, v25, -v23 op_sel:[1,0,0] op_sel_hi:[1,0,0]\n\t"
      "v_mfma_f32_16x16x32_bf16 v[192:195], v[40:43], v[88:91], v[192:195]\n\t"
      "v_cvt_pk_bf16_f32 v62, v36, v37\n\t"
      "v_cvt_pk_bf16_f32 v63, v38, v39\n\t"
      "v_mfma_f32_16x16x32_bf16 v[200:203], v[40:43], v[80:83], v[200:203]\n\t"
      "v_lshrrev_b32 v31, 8, v18\n\t"
      "v_and_b32 v28, v13, v18\n\t"
      "v_mfma_f32_16x16x32_bf16 v[200:203], v[48:51], v[84:87], v[200:203]\n\t"
      "v_and_b32 v29, v14, v18\n\t"
      "v_and_b32 v30, v13, v31\n\t"
      "v_mfma_f32_16x16x32_bf16 v[204:207], v[52:55], v[84:87], v[204:207]\n\t"
      "v_and_b32 v31, v14, v31\n\t"
      "LH15x4_%=:\n\t"
      "s_cbranch_execz LHRET_%=\n\t"
      "v_fma_mix_f32 v32, v28, v20, -v22 op_sel:[0,0,0] op_sel_hi:[1,0,0]\n\t"
      "v_mfma_f32_16x16x32_bf16 v[204:207], v[44:47], v[80:83], v[204:207]\n\t"
      "v_fma_mix_f32 v33, v29, v24, -v22 op_sel:[0,0,0] op_sel_hi:[1,0,0]\n\t"
      "v_fma_mix_f32 v34, v30, v20, -v22 op_sel:[0,0,0] op_sel_hi:[1,0,0]\n\t"
      "v_mfma_f32_16x16x32_bf16 v[212:215], v[44:47], v[132:135], v[212:215]\n\t"
      "v_fma_mix_f32 v35, v31, v24, -v22 op_sel:[0,0,0] op_sel_hi:[1,0,0]\n\t"
      "v_fma_mix_f32 v36, v28, v20, -v22 op_sel:[1,0,0] op_sel_hi:[1,0,0]\n\t"
      "v_mfma_f32_16x16x32_bf16 v[212:215], v[52:55], v[128:131], v[212:215]\n\t"
      "v_fma_mix_f32 v37, v29, v24, -v22 op_sel:[1,0,0] op_sel_hi:[1,0,0]\n\t"
      "v_cvt_pk_bf16_f32 v64, v32, v33\n\t"
      "v_mfma_f32_16x16x32_bf16 v[208:211], v[48:51], v[128:131], v[208:211]\n\t"
      "ds_read_b128 v[128:131], v6 offset:32768\n\t"
      "v_cvt_pk_bf16_f32 v65, v34, v35\n\t"
      "v_mfma_f32_16x16x32_bf16 v[208:211], v[40:43], v[132:135], v[208:211]\n\t"
      "ds_read_b128 v[132:135], v7 offset:32768\n\t"
      "v_fma_mix_f32 v38, v30, v20, -v22 op_sel:[1,0,0] op_sel_hi:[1,0,0]\n\t"
      "v_mfma_f32_16x16x32_bf16 v[216:219], v[40:43], v[72:75], v[216:219]\n\t"
      "v_fma_mix_f32 v39, v31, v24, -v22 op_sel:[1,0,0] op_sel_hi:[1,0,0]\n\t"
      "v_cvt_pk_bf16_f32 v66, v36, v37\n\t"
      "v_mfma_f32_16x16x32_bf16 v[216:219], v[48:51], v[76:79], v[216:219]\n\t"
      "v_cvt_pk_bf16_f32 v67, v38, v39\n\t"
      "v_lshrrev_b32 v31, 8, v19\n\t"
      "v_mfma_f32_16x16x32_bf16 v[220:223], v[52:55], v[76:79], v[220:223]\n\t"
      "v_and_b32 v28, v13, v19\n\t"
      "v_and_b32 v29, v14, v19\n\t"
      "v_mfma_f32_16x16x32_bf16 v[220:223], v[44:47], v[72:75], v[220:223]\n\t"
      "v_and_b32 v30, v13, v31\n\t"
      "v_and_b32 v31, v14, v31\n\t"
      "v_mfma_f32_16x16x32_bf16 v[228:231], v[44:47], v[124:127], v[228:231]\n\t"
      "ds_read_b64 v[18:19], v10 offset:10240\n\t"
      "v_fma_mix_f32 v32, v28, v21, -v23 op_sel:[0,0,0] op_sel_hi:[1,0,0]\n\t"
      "v_mfma_f32_16x16x32_bf16 v[228:231], v[52:55], v[120:123], v[228:231]\n\t"
      "v_fma_mix_f32 v33, v29, v25, -v23 op_sel:[0,0,0] op_sel_hi:[1,0,0]\n\t"
      "v_fma_mix_f32 v34, v30, v21, -v23 op_sel:[0,0,0] op_sel_hi:[1,0,0]\n\t"
      "v_mfma_f32_16x16x32_bf16 v[224:227], v[48:51], v[120:123], v[224:227]\n\t"
      "ds_read_b128 v[120:123], v6 offset:34816\n\t"
      "v_fma_mix_f32 v35, v31, v25, -v23 op_sel:[0,0,0] op_sel_hi:[1,0,0]\n\t"
      "v_mfma_f32_16x16x32_bf16 v[224:227], v[40:43], v[124:127], v[224:227]\n\t"
      "ds_read_b128 v[124:127], v7 offset:34816\n\t"
      "v_fma_mix_f32 v36, v28, v21, -v23 op_sel:[1,0,0] op_sel_hi:[1,0,0]\n\t"
      "v_mfma_f32_16x16x32_bf16 v[232:235], v[40:43], v[116:119], v[232:235]\n\t"
      "v_fma_mix_f32 v37, v29, v25, -v23 op_sel:[1,0,0] op_sel_hi:[1,0,0]\n\t"
      "s_waitcnt lgkmcnt(3)\n\t"
      "v_mfma_f32_16x16x32_bf16 v[232:235], v[48:51], v[112:115], v[232:235]\n\t"
      "v_cvt_pk_bf16_f32 v68, v32, v33\n\t"
      "v_cvt_pk_bf16_f32 v69, v34, v35\n\t"
      "v_mfma_f32_16x16x32_bf16 v[236:239], v[52:55], v[112:115], v[236:239]\n\t"
      "ds_read_b128 v[112:115], v6 offset:36864\n\t"
      "v_fma_mix_f32 v38, v30, v21, -v23 op_sel:[1,0,0] op_sel_hi:[1,0,0]\n\t"
      "v_mfma_f32_16x16x32_bf16 v[236:239], v[44:47], v[116:119], v[236:239]\n\t"
      "ds_read_b128 v[116:119], v7 offset:36864\n\t"
      "v_fma_mix_f32 v39, v31, v25, -v23 op_sel:[1,0,0] op_sel_hi:[1,0,0]\n\t"
      "v_mfma_f32_16x16x32_bf16 v[244:247], v[44:47], v[108:111], v[244:247]\n\t"
      "v_cvt_pk_bf16_f32 v70, v36, v37\n\t"
      "v_cvt_pk_bf16_f32 v71, v38, v39\n\t"
      "v_mfma_f32_16x16x32_bf16 v[244:247], v[52:55], v[104:107], v[244:247]\n\t"
      "v_mfma_f32_16x16x32_bf16 v[240:243], v[48:51], v[104:107], v[240:243]\n\t"
      "ds_read_b128 v[104:107], v6 offset:38912\n\t"
      "v_mfma_f32_16x16x32_bf16 v[240:243], v[40:43], v[108:111], v[240:243]\n\t"
      "ds_read_b128 v[108:111], v7 offset:38912\n\t"
      "v_mfma_f32_16x16x32_bf16 v[248:251], v[40:43], v[100:103], v[248:251]\n\t"
      "v_mfma_f32_16x16x32_bf16 v[248:251], v[48:51], v[96:99], v[248:251]\n\t"
      "v_mfma_f32_16x16x32_bf16 v[252:255], v[52:55], v[96:99], v[252:255]\n\t"
      "v_mfma_f32_16x16x32_bf16 v[252:255], v[44:47], v[100:103], v[252:255]\n\t"
      "ds_read_b128 v[100:103], v6 offset:40960\n\t"
      "v_mfma_f32_16x16x32_bf16 v[140:143], v[60:63], v[128:131], v[140:143]\n\t"
      "v_lshlrev_b32 v28, 16, v26\n\t"
      "v_and_b32 v29, 0xffff0000, v26\n\t"
      "v_lshlrev_b32 v30, 16, v27\n\t"
      "v_and_b32 v31, 0xffff0000, v27\n\t"
      "v_mul_f32 v20, 0x4b800000, v28\n\t"
      "v_mul_f32 v24, 0x49800000, v28\n\t"
      "v_mul_f32 v22, v28, v30\n\t"
      "v_mul_f32 v21, 0x4b800000, v29\n\t"
      "v_mul_f32 v25, 0x49800000, v29\n\t"
      "v_mul_f32 v23, v29, v31\n\t"
      "ds_read2st64_b32 v[26:27], v11 offset0:15 offset1:31\n\t"
      "v_mfma_f32_16x16x32_bf16 v[140:143], v[68:71], v[132:135], v[140:143]\n\t"
      "v_lshrrev_b32 v31, 8, v16\n\t"
      "v_and_b32 v28, v13, v16\n\t"
      "v_and_b32 v29, v14, v16\n\t"
      "v_and_b32 v30, v13, v31\n\t"
      "v_and_b32 v31, v14, v31\n\t"
      "v_fma_mix_f32 v32, v28, v20, -v22 op_sel:[0,0,0] op_sel_hi:[1,0,0]\n\t"
      "v_fma_mix_f32 v33, v29, v24, -v22 op_sel:[0,0,0] op_sel_hi:[1,0,0]\n\t"
      "v_fma_mix_f32 v34, v30, v20, -v22 op_sel:[0,0,0] op_sel_hi:[1,0,0]\n\t"
      "v_fma_mix_f32 v35, v31, v24, -v22 op_sel:[0,0,0] op_sel_hi:[1,0,0]\n\t"
      "v_fma_mix_f32 v36, v28, v20, -v22 op_sel:[1,0,0] op_sel_hi:[1,0,0]\n\t"
      "v_fma_mix_f32 v37, v29, v24, -v22 op_sel:[1,0,0] op_sel_hi:[1,0,0]\n\t"
      "v_mfma_f32_16x16x32_bf16 v[136:139], v[64:67], v[132:135], v[136:139]\n\t"
      "ds_read_b128 v[132:135], v7 offset:40960\n\t"
      "v_cvt_pk_bf16_f32 v40, v32, v33\n\t"
      "v_cvt_pk_bf16_f32 v41, v34, v35\n\t"
      "v_fma_mix_f32 v38, v30, v20, -v22 op_sel:[1,0,0] op_sel_hi:[1,0,0]\n\t"
      "v_fma_mix_f32 v39, v31, v24, -v22 op_sel:[1,0,0] op_sel_hi:[1,0,0]\n\t"
      "v_cvt_pk_bf16_f32 v42, v36, v37\n\t"
      "v_cvt_pk_bf16_f32 v43, v38, v39\n\t"
      "v_lshrrev_b32 v31, 8, v17\n\t"
      "v_and_b32 v28, v13, v17\n\t"
      "v_and_b32 v29, v14, v17\n\t"
      "v_and_b32 v30, v13, v31\n\t"
      "v_mfma_f32_16x16x32_bf16 v[136:139], v[56:59], v[128:131], v[136:139]\n\t"
      "ds_read_b128 v[128:131], v6 offset:43008\n\t"
      "v_and_b32 v31, v14, v31\n\t"
      "ds_read_b64 v[16:17], v10 offset:12288\n\t"
      "v_fma_mix_f32 v32, v28, v21, -v23 op_sel:[0,0,0] op_sel_hi:[1,0,0]\n\t"
      "v_fma_mix_f32 v33, v29, v25, -v23 op_sel:[0,0,0] op_sel_hi:[1,0,0]\n\t"
      "v_fma_mix_f32 v34, v30, v21, -v23 op_sel:[0,0,0] op_sel_hi:[1,0,0]\n\t"
      "v_fma_mix_f32 v35, v31, v25, -v23 op_sel:[0,0,0] op_sel_hi:[1,0,0]\n\t"
      "v_fma_mix_f32 v36, v28, v21, -v23 op_sel:[1,0,0] op_sel_hi:[1,0,0]\n\t"
      "v_fma_mix_f32 v37, v29, v25, -v23 op_sel:[1,0,0] op_sel_hi:[1,0,0]\n\t"
      "v_cvt_pk_bf16_f32 v44, v32, v33\n\t"
      "s_waitcnt lgkmcnt(3)\n\t"
      "v_mfma_f32_16x16x32_bf16 v[144:147], v[56:59], v[120:123], v[144:147]\n\t"
      "v_cvt_pk_bf16_f32 v45, v34, v35\n\t"
      "v_fma_mix_f32 v38, v30, v21, -v23 op_sel:[1,0,0] op_sel_hi:[1,0,0]\n\t"
      "v_fma_mix_f32 v39, v31, v25, -v23 op_sel:[1,0,0] op_sel_hi:[1,0,0]\n\t"
      "v_cvt_pk_bf16_f32 v46, v36, v37\n\t"
      "v_cvt_pk_bf16_f32 v47, v38, v39\n\t"
      "v_lshrrev_b32 v31, 8, v18\n\t"
      "v_and_b32 v28, v13, v18\n\t"
      "v_and_b32 v29, v14, v18\n\t"
      "v_and_b32 v30, v13, v31\n\t"
      "v_and_b32 v31, v14, v31\n\t"
      "v_fma_mix_f32 v32, v28, v20, -v22 op_sel:[0,0,0] op_sel_hi:[1,0,0]\n\t"
      "v_mfma_f32_16x16x32_bf16 v[144:147], v[64:67], v[124:127], v[144:147]\n\t"
      "v_fma_mix_f32 v33, v29, v24, -v22 op_sel:[0,0,0] op_sel_hi:[1,0,0]\n\t"
      "v_fma_mix_f32 v34, v30, v20, -v22 op_sel:[0,0,0] op_sel_hi:[1,0,0]\n\t"
      "v_fma_mix_f32 v35, v31, v24, -v22 op_sel:[0,0,0] op_sel_hi:[1,0,0]\n\t"
      "v_fma_mix_f32 v36, v28, v20, -v22 op_sel:[1,0,0] op_sel_hi:[1,0,0]\n\t"
      "v_fma_mix_f32 v37, v29, v24, -v22 op_sel:[1,0,0] op_sel_hi:[1,0,0]\n\t"
      "v_cvt_pk_bf16_f32 v48, v32, v33\n\t"
      "v_cvt_pk_bf16_f32 v49, v34, v35\n\t"
      "v_fma_mix_f32 v38, v30, v20, -v22 op_sel:[1,0,0] op_sel_hi:[1,0,0]\n\t"
      "v_fma_mix_f32 v39, v31, v24, -v22 op_sel:[1,0,0] op_sel_hi:[1,0,0]\n\t"
      "v_cvt_pk_bf16_f32 v50, v36, v37\n\t"
      "v_mfma_f32_16x16x32_bf16 v[148:151], v[68:71], v[124:127], v[148:151]\n\t"
      "ds_read_b128 v[124:127], v7 offset:43008\n\t"
      "v_cvt_pk_bf16_f32 v51, v38, v39\n\t"
      "v_lshrrev_b32 v31, 8, v19\n\t"
      "v_and_b32 v28, v13, v19\n\t"
      "v_and_b32 v29, v14, v19\n\t"
      "v_and_b32 v30, v13, v31\n\t"
      "v_and_b32 v31, v14, v31\n\t"
      "ds_read_b64 v[18:19], v10 offset:14336\n\t"
      "v_fma_mix_f32 v32, v28, v21, -v23 op_sel:[0,0,0] op_sel_hi:[1,0,0]\n\t"
      "v_fma_mix_f32 v33, v29, v25, -v23 op_sel:[0,0,0] op_sel_hi:[1,0,0]\n\t"
      "v_mfma_f32_16x16x32_bf16 v[148:151], v[60:63], v[120:123], v[148:151]\n\t"
      "v_fma_mix_f32 v34, v30, v21, -v23 op_sel:[0,0,0] op_sel_hi:[1,0,0]\n\t"
      "v_fma_mix_f32 v35, v31, v25, -v23 op_sel:[0,0,0] op_sel_hi:[1,0,0]\n\t"
      "v_fma_mix_f32 v36, v28, v21, -v23 op_sel:[1,0,0] op_sel_hi:[1,0,0]\n\t"
      "v_fma_mix_f32 v37, v29, v25, -v23 op_sel:[1,0,0] op_sel_hi:[1,0,0]\n\t"
      "v_cvt_pk_bf16_f32 v52, v32, v33\n\t"
      "v_cvt_pk_bf16_f32 v53, v34, v35\n\t"
      "v_fma_mix_f32 v38, v30, v21, -v23 op_sel:[1,0,0] op_sel_hi:[1,0,0]\n\t"
      "v_fma_mix_f32 v39, v31, v25, -v23 op_sel:[1,0,0] op_sel_hi:[1,0,0]\n\t"
      "v_cvt_pk_bf16_f32 v54, v36, v37\n\t"
      "v_cvt_pk_bf16_f32 v55, v38, v39\n\t"
      "v_mfma_f32_16x16x32_bf16 v[140:143], v[44:47], v[112:115], v[140:143]\n\t"
      "v_lshlrev_b32 v28, 16, v26\n\t"
      "v_and_b32 v29, 0xffff0000, v26\n\t"
      "v_lshlrev_b32 v30, 16, v27\n\t"
      "v_and_b32 v31, 0xffff0000, v27\n\t"
      "v_mul_f32 v20, 0x4b800000, v28\n\t"
      "v_mul_f32 v24, 0x49800000, v28\n\t"
      "v_mul_f32 v22, v28, v30\n\t"
      "v_mul_f32 v21, 0x4b800000, v29\n\t"
      "v_mul_f32 v25, 0x49800000, v29\n\t"
      "v_mul_f32 v23, v29, v31\n\t"
      "s_waitcnt lgkmcnt(0)\n\t"
      "v_lshrrev_b32 v31, 8, v16\n\t"
      "v_mfma_f32_16x16x32_bf16 v[140:143], v[52:55], v[116:119], v[140:143]\n\t"
      "s_barrier\n\t"
      "v_mfma_f32_16x16x32_bf16 v[136:139], v[48:51], v[116:119], v[136:139]\n\t"
      "ds_read_b128 v[120:123], v6 offset:45056\n\t"
      "v_and_b32 v28, v13, v16\n\t"
      "v_and_b32 v29, v14, v16\n\t"
      "v_and_b32 v30, v13, v31\n\t"
      "v_and_b32 v31, v14, v31\n\t"
      "v_fma_mix_f32 v32, v28, v20, -v22 op_sel:[0,0,0] op_sel_hi:[1,0,0]\n\t"
      "v_fma_mix_f32 v33, v29, v24, -v22 op_sel:[0,0,0] op_sel_hi:[1,0,0]\n\t"
      "v_fma_mix_f32 v34, v30, v20, -v22 op_sel:[0,0,0] op_sel_hi:[1,0,0]\n\t"
      "v_fma_mix_f32 v35, v31, v24, -v22 op_sel:[0,0,0] op_sel_hi:[1,0,0]\n\t"
      "v_fma_mix_f32 v36, v28, v20, -v22 op_sel:[1,0,0] op_sel_hi:[1,0,0]\n\t"
      "v_fma_mix_f32 v37, v29, v24, -v22 op_sel:[1,0,0] op_sel_hi:[1,0,0]\n\t"
      "v_cvt_pk_bf16_f32 v72, v32, v33\n\t"
      "v_mfma_f32_16x16x32_bf16 v[136:139], v[40:43], v[112:115], v[136:139]\n\t"
      "ds_read_b128 v[116:119], v7 offset:45056\n\t"
      "v_cvt_pk_bf16_f32 v73, v34, v35\n\t"
      "v_fma_mix_f32 v38, v30, v20, -v22 op_sel:[1,0,0] op_sel_hi:[1,0,0]\n\t"
      "v_fma_mix_f32 v39, v31, v24, -v22 op_sel:[1,0,0] op_sel_hi:[1,0,0]\n\t"
      "v_cvt_pk_bf16_f32 v74, v36, v37\n\t"
      "v_cvt_pk_bf16_f32 v75, v38, v39\n\t"
      "v_lshrrev_b32 v31, 8, v17\n\t"
      "v_and_b32 v28, v13, v17\n\t"
      "v_and_b32 v29, v14, v17\n\t"
      "v_and_b32 v30, v13, v31\n\t"
      "v_and_b32 v31, v14, v31\n\t"
      "v_fma_mix_f32 v32, v28, v21, -v23 op_sel:[0,0,0] op_sel_hi:[1,0,0]\n\t"
      "v_mfma_f32_16x16x32_bf16 v[144:147], v[40:43], v[104:107], v[144:147]\n\t"
      "ds_read_b128 v[112:115], v6 offset:47104\n\t"
      "v_fma_mix_f32 v33, v29, v25, -v23 op_sel:[0,0,0] op_sel_hi:[1,0,0]\n\t"
      "v_fma_mix_f32 v34, v30, v21, -v23 op_sel:[0,0,0] op_sel_hi:[1,0,0]\n\t"
      "v_fma_mix_f32 v35, v31, v25, -v23 op_sel:[0,0,0] op_sel_hi:[1,0,0]\n\t"
      "v_fma_mix_f32 v36, v28, v21, -v23 op_sel:[1,0,0] op_sel_hi:[1,0,0]\n\t"
      "v_fma_mix_f32 v37, v29, v25, -v23 op_sel:[1,0,0] op_sel_hi:[1,0,0]\n\t"
      "v_cvt_pk_bf16_f32 v76, v32, v33\n\t"
      "v_cvt_pk_bf16_f32 v77, v34, v35\n\t"
      "v_fma_mix_f32 v38, v30, v21, -v23 op_sel:[1,0,0] op_sel_hi:[1,0,0]\n\t"
      "v_fma_mix_f32 v39, v31, v25, -v23 op_sel:[1,0,0] op_sel_hi:[1,0,0]\n\t"
      "v_cvt_pk_bf16_f32 v78, v36, v37\n\t"
      "v_cvt_pk_bf16_f32 v79, v38, v39\n\t"
      "v_mfma_f32_16x16x32_bf16 v[144:147], v[48:51], v[108:111], v[144:147]\n\t"
      "v_lshrrev_b32 v31, 8, v18\n\t"
      "v_and_b32 v28, v13, v18\n\t"
      "v_and_b32 v29, v14, v18\n\t"
      "v_and_b32 v30, v13, v31\n\t"
      "v_and_b32 v31, v14, v31\n\t"
      "v_fma_mix_f32 v32, v28, v20, -v22 op_sel:[0,0,0] op_sel_hi:[1,0,0]\n\t"
      "v_fma_mix_f32 v33, v29, v24, -v22 op_sel:[0,0,0] op_sel_hi:[1,0,0]\n\t"
      "v_fma_mix_f32 v34, v30, v20, -v22 op_sel:[0,0,0] op_sel_hi:[1,0,0]\n\t"
      "v_fma_mix_f32 v35, v31, v24, -v22 op_sel:[0,0,0] op_sel_hi:[1,0,0]\n\t"
      "v_fma_mix_f32 v36, v28, v20, -v22 op_sel:[1,0,0] op_sel_hi:[1,0,0]\n\t"
      "v_fma_mix_f32 v37, v29, v24, -v22 op_sel:[1,0,0] op_sel_hi:[1,0,0]\n\t"
      "v_cvt_pk_bf16_f32 v80, v32, v33\n\t"
      "v_mfma_f32_16x16x32_bf16 v[148:151], v[52:55], v[108:111], v[148:151]\n\t"
      "ds_read_b128 v[108:111], v7 offset:47104\n\t"
      "v_cvt_pk_bf16_f32 v81, v34, v35\n\t"
      "v_fma_mix_f32 v38, v30, v20, -v22 op_sel:[1,0,0] op_sel_hi:[1,0,0]\n\t"
      "v_fma_mix_f32 v39, v31, v24, -v22 op_sel:[1,0,0] op_sel_hi:[1,0,0]\n\t"
      "v_cvt_pk_bf16_f32 v82, v36, v37\n\t"
      "v_cvt_pk_bf16_f32 v83, v38, v39\n\t"
      "v_lshrrev_b32 v31, 8, v19\n\t"
      "v_and_b32 v28, v13, v19\n\t"
      "v_and_b32 v29, v14, v19\n\t"
      "v_and_b32 v30, v13, v31\n\t"
      "v_and_b32 v31, v14, v31\n\t"
      "v_fma_mix_f32 v32, v28, v21, -v23 op_sel:[0,0,0] op_sel_hi:[1,0,0]\n\t"
      "v_mfma_f32_16x16x32_bf16 v[148:151], v[44:47], v[104:107], v[148:151]\n\t"
      "ds_read_b128 v[104:107], v6 offset:49152\n\t"
      "v_fma_mix_f32 v33, v29, v25, -v23 op_sel:[0,0,0] op_sel_hi:[1,0,0]\n\t"
      "v_fma_mix_f32 v34, v30, v21, -v23 op_sel:[0,0,0] op_sel_hi:[1,0,0]\n\t"
      "v_fma_mix_f32 v35, v31, v25, -v23 op_sel:[0,0,0] op_sel_hi:[1,0,0]\n\t"
      "v_fma_mix_f32 v36, v28, v21, -v23 op_sel:[1,0,0] op_sel_hi:[1,0,0]\n\t"
      "v_fma_mix_f32 v37, v29, v25, -v23 op_sel:[1,0,0] op_sel_hi:[1,0,0]\n\t"
      "v_cvt_pk_bf16_f32 v84, v32, v33\n\t"
      "v_cvt_pk_bf16_f32 v85, v34, v35\n\t"
      "v_fma_mix_f32 v38, v30, v21, -v23 op_sel:[1,0,0] op_sel_hi:[1,0,0]\n\t"
      "v_fma_mix_f32 v39, v31, v25, -v23 op_sel:[1,0,0] op_sel_hi:[1,0,0]\n\t"
      "v_cvt_pk_bf16_f32 v86, v36, v37\n\t"
      "v_cvt_pk_bf16_f32 v87, v38, v39\n\t"
      "v_mfma_f32_16x16x32_bf16 v[140:143], v[76:79], v[100:103], v[140:143]\n\t"
      "ds_read_b128 v[24:27], v7 offset:49152\n\t"
      "ds_read_b128 v[28:31], v6 offset:51200\n\t"
      "v_mfma_f32_16x16x32_bf16 v[140:143], v[84:87], v[132:135], v[140:143]\n\t"
      "ds_read_b128 v[32:35], v7 offset:51200\n\t"
      "ds_read_b128 v[36:39], v6 offset:53248\n\t"
      "v_mfma_f32_16x16x32_bf16 v[136:139], v[80:83], v[132:135], v[136:139]\n\t"
      "ds_read_b128 v[132:135], v7 offset:53248\n\t"
      "v_mfma_f32_16x16x32_bf16 v[136:139], v[72:75], v[100:103], v[136:139]\n\t"
      "ds_read_b128 v[100:103], v6 offset:55296\n\t"
      "v_mfma_f32_16x16x32_bf16 v[144:147], v[72:75], v[128:131], v[144:147]\n\t"
      "v_mfma_f32_16x16x32_bf16 v[144:147], v[80:83], v[124:127], v[144:147]\n\t"
      "v_mfma_f32_16x16x32_bf16 v[148:151], v[84:87], v[124:127], v[148:151]\n\t"
      "ds_read_b128 v[124:127], v7 offset:55296\n\t"
      "v_mfma_f32_16x16x32_bf16 v[148:151], v[76:79], v[128:131], v[148:151]\n\t"
      "ds_read_b128 v[128:131], v6 offset:57344\n\t"
      "s_waitcnt lgkmcnt(4)\n\t"
      "v_mfma_f32_16x16x32_bf16 v[156:159], v[60:63], v[120:123], v[156:159]\n\t"
      "v_mfma_f32_16x16x32_bf16 v[156:159], v[68:71], v[116:119], v[156:159]\n\t"
      "v_mfma_f32_16x16x32_bf16 v[152:155], v[64:67], v[116:119], v[152:155]\n\t"
      "ds_read_b128 v[116:119], v7 offset:57344\n\t"
      "v_cvt_pk_bf16_f32 v16, v136, v140\n\t"
      "v_mfma_f32_16x16x32_bf16 v[152:155], v[56:59], v[120:123], v[152:155]\n\t"
      "ds_read_b128 v[120:123], v6 offset:59392\n\t"
      "v_cvt_pk_bf16_f32 v17, v137, v141\n\t"
      "v_mfma_f32_16x16x32_bf16 v[160:163], v[56:59], v[112:115], v[160:163]\n\t"
      "v_cvt_pk_bf16_f32 v18, v138, v142\n\t"
      "v_mfma_f32_16x16x32_bf16 v[160:163], v[64:67], v[108:111], v[160:163]\n\t"
      "ds_write_b64 v12, v[16:17] offset:0\n\t"
      "v_mfma_f32_16x16x32_bf16 v[164:167], v[68:71], v[108:111], v[164:167]\n\t"
      "ds_read_b128 v[108:111], v7 offset:59392\n\t"
      "v_cvt_pk_bf16_f32 v19, v139, v143\n\t"
      "v_mfma_f32_16x16x32_bf16 v[164:167], v[60:63], v[112:115], v[164:167]\n\t"
      "ds_read_b128 v[112:115], v6 offset:61440\n\t"
      "v_mfma_f32_16x16x32_bf16 v[156:159], v[44:47], v[104:107], v[156:159]\n\t"
      "ds_write_b64 v12, v[18:19] offset:8\n\t"
      "v_mfma_f32_16x16x32_bf16 v[156:159], v[52:55], v[24:27], v[156:159]\n\t"
      "v_cvt_pk_bf16_f32 v20, v144, v148\n\t"
      "v_cvt_pk_bf16_f32 v21, v145, v149\n\t"
      "v_mfma_f32_16x16x32_bf16 v[152:155], v[48:51], v[24:27], v[152:155]\n\t"
      "ds_read_b128 v[24:27], v7 offset:61440\n\t"
      "v_cvt_pk_bf16_f32 v22, v146, v150\n\t"
      "v_mfma_f32_16x16x32_bf16 v[152:155], v[40:43], v[104:107], v[152:155]\n\t"
      "ds_read_b128 v[104:107], v6 offset:63488\n\t"
      "v_mfma_f32_16x16x32_bf16 v[160:163], v[40:43], v[28:31], v[160:163]\n\t"
      "ds_write_b64 v12, v[20:21] offset:4096\n\t"
      "v_mfma_f32_16x16x32_bf16 v[160:163], v[48:51], v[32:35], v[160:163]\n\t"
      "v_cvt_pk_bf16_f32 v23, v147, v151\n\t"
      "v_mfma_f32_16x16x32_bf16 v[164:167], v[52:55], v[32:35], v[164:167]\n\t"
      "ds_read_b128 v[32:35], v7 offset:63488\n\t"
      "v_mfma_f32_16x16x32_bf16 v[164:167], v[44:47], v[28:31], v[164:167]\n\t"
      "ds_read_b128 v[28:31], v8 offset:0\n\t"
      "v_mfma_f32_16x16x32_bf16 v[156:159], v[76:79], v[36:39], v[156:159]\n\t"
      "ds_write_b64 v12, v[22:23] offset:4104\n\t"
      "s_waitcnt lgkmcnt(5)\n\t"
      "v_mfma_f32_16x16x32_bf16 v[156:159], v[84:87], v[132:135], v[156:159]\n\t"
      "v_mfma_f32_16x16x32_bf16 v[152:155], v[80:83], v[132:135], v[152:155]\n\t"
      "ds_read_b128 v[132:135], v9 offset:0\n\t"
      "v_mfma_f32_16x16x32_bf16 v[152:155], v[72:75], v[36:39], v[152:155]\n\t"
      "ds_read_b128 v[36:39], v8 offset:2048\n\t"
      "v_mfma_f32_16x16x32_bf16 v[160:163], v[72:75], v[100:103], v[160:163]\n\t"
      "v_mfma_f32_16x16x32_bf16 v[160:163], v[80:83], v[124:127], v[160:163]\n\t"
      "v_mfma_f32_16x16x32_bf16 v[164:167], v[84:87], v[124:127], v[164:167]\n\t"
      "ds_read_b128 v[124:127], v9 offset:2048\n\t"
      "v_mfma_f32_16x16x32_bf16 v[164:167], v[76:79], v[100:103], v[164:167]\n\t"
      "ds_read_b128 v[100:103], v8 offset:4096\n\t"
      "v_mfma_f32_16x16x32_bf16 v[172:175], v[60:63], v[128:131], v[172:175]\n\t"
      "s_waitcnt lgkmcnt(4)\n\t"
      "s_barrier\n\t"
      "v_mfma_f32_16x16x32_bf16 v[172:175], v[68:71], v[116:119], v[172:175]\n\t"
      "v_mfma_f32_16x16x32_bf16 v[168:171], v[64:67], v[116:119], v[168:171]\n\t"
      "ds_read_b128 v[116:119], v9 offset:4096\n\t"
      "v_cvt_pk_bf16_f32 v16, v152, v156\n\t"
      "v_mfma_f32_16x16x32_bf16 v[168:171], v[56:59], v[128:131], v[168:171]\n\t"
      "ds_read_b128 v[128:131], v8 offset:6144\n\t"
      "v_cvt_pk_bf16_f32 v17, v153, v157\n\t"
      "v_mfma_f32_16x16x32_bf16 v[176:179], v[56:59], v[120:123], v[176:179]\n\t"
      "v_cvt_pk_bf16_f32 v18, v154, v158\n\t"
      "v_mfma_f32_16x16x32_bf16 v[176:179], v[64:67], v[108:111], v[176:179]\n\t"
      "ds_write_b64 v12, v[16:17] offset:8192\n\t"
      "v_mfma_f32_16x16x32_bf16 v[180:183], v[68:71], v[108:111], v[180:183]\n\t"
      "ds_read_b128 v[108:111], v9 offset:6144\n\t"
      "v_cvt_pk_bf16_f32 v19, v155, v159\n\t"
      "v_mfma_f32_16x16x32_bf16 v[180:183], v[60:63], v[120:123], v[180:183]\n\t"
      "ds_read_b128 v[120:123], v8 offset:8192\n\t"
      "v_mfma_f32_16x16x32_bf16 v[172:175], v[44:47], v[112:115], v[172:175]\n\t"
      "ds_write_b64 v12, v[18:19] offset:8200\n\t"
      "v_mfma_f32_16x16x32_bf16 v[172:175], v[52:55], v[24:27], v[172:175]\n\t"
      "v_cvt_pk_bf16_f32 v20, v160, v164\n\t"
      "v_cvt_pk_bf16_f32 v21, v161, v165\n\t"
      "v_mfma_f32_16x16x32_bf16 v[168:171], v[48:51], v[24:27], v[168:171]\n\t"
      "ds_read_b128 v[24:27], v9 offset:8192\n\t"
      "v_cvt_pk_bf16_f32 v22, v162, v166\n\t"
      "v_mfma_f32_16x16x32_bf16 v[168:171], v[40:43], v[112:115], v[168:171]\n\t"
      "ds_read_b128 v[112:115], v8 offset:10240\n\t"
      "v_mfma_f32_16x16x32_bf16 v[176:179], v[40:43], v[104:107], v[176:179]\n\t"
      "ds_write_b64 v12, v[20:21] offset:12288\n\t"
      "v_mfma_f32_16x16x32_bf16 v[176:179], v[48:51], v[32:35], v[176:179]\n\t"
      "v_cvt_pk_bf16_f32 v23, v163, v167\n\t"
      "v_mfma_f32_16x16x32_bf16 v[180:183], v[52:55], v[32:35], v[180:183]\n\t"
      "ds_read_b128 v[32:35], v9 offset:10240\n\t"
      "v_mfma_f32_16x16x32_bf16 v[180:183], v[44:47], v[104:107], v[180:183]\n\t"
      "ds_read_b128 v[104:107], v8 offset:12288\n\t"
      "v_mfma_f32_16x16x32_bf16 v[172:175], v[76:79], v[28:31], v[172:175]\n\t"
      "ds_write_b64 v12, v[22:23] offset:12296\n\t"
      "s_waitcnt lgkmcnt(5)\n\t"
      "v_mfma_f32_16x16x32_bf16 v[172:175], v[84:87], v[132:135], v[172:175]\n\t"
      "v_mfma_f32_16x16x32_bf16 v[168:171], v[80:83], v[132:135], v[168:171]\n\t"
      "ds_read_b128 v[132:135], v9 offset:12288\n\t"
      "v_mfma_f32_16x16x32_bf16 v[168:171], v[72:75], v[28:31], v[168:171]\n\t"
      "ds_read_b128 v[28:31], v8 offset:14336\n\t"
      "v_mfma_f32_16x16x32_bf16 v[176:179], v[72:75], v[36:39], v[176:179]\n\t"
      "v_mfma_f32_16x16x32_bf16 v[176:179], v[80:83], v[124:127], v[176:179]\n\t"
      "v_mfma_f32_16x16x32_bf16 v[180:183], v[84:87], v[124:127], v[180:183]\n\t"
      "ds_read_b128 v[124:127], v9 offset:14336\n\t"
      "v_mfma_f32_16x16x32_bf16 v[180:183], v[76:79], v[36:39], v[180:183]\n\t"
      "ds_read_b128 v[36:39], v8 offset:16384\n\t"
      "v_mfma_f32_16x16x32_bf16 v[188:191], v[60:63], v[100:103], v[188:191]\n\t"
      "s_waitcnt lgkmcnt(4)\n\t"
      "s_barrier\n\t"
      "v_mfma_f32_16x16x32_bf16 v[188:191], v[68:71], v[116:119], v[188:191]\n\t"
      "v_mfma_f32_16x16x32_bf16 v[184:187], v[64:67], v[116:119], v[184:187]\n\t"
      "ds_read_b128 v[116:119], v9 offset:16384\n\t"
      "v_cvt_pk_bf16_f32 v16, v168, v172\n\t"
      "v_mfma_f32_16x16x32_bf16 v[184:187], v[56:59], v[100:103], v[184:187]\n\t"
      "ds_read_b128 v[100:103], v8 offset:18432\n\t"
      "v_cvt_pk_bf16_f32 v17, v169, v173\n\t"
      "v_mfma_f32_16x16x32_bf16 v[192:195], v[56:59], v[128:131], v[192:195]\n\t"
      "v_cvt_pk_bf16_f32 v18, v170, v174\n\t"
      "v_mfma_f32_16x16x32_bf16 v[192:195], v[64:67], v[108:111], v[192:195]\n\t"
      "ds_write_b64 v12, v[16:17] offset:16384\n\t"
      "v_mfma_f32_16x16x32_bf16 v[196:199], v[68:71], v[108:111], v[196:199]\n\t"
      "ds_read_b128 v[108:111], v9 offset:18432\n\t"
      "v_cvt_pk_bf16_f32 v19, v171, v175\n\t"
      "v_mfma_f32_16x16x32_bf16 v[196:199], v[60:63], v[128:131], v[196:199]\n\t"
      "ds_read_b128 v[128:131], v8 offset:20480\n\t"
      "v_mfma_f32_16x16x32_bf16 v[188:191], v[44:47], v[120:123], v[188:191]\n\t"
      "ds_write_b64 v12, v[18:19] offset:16392\n\t"
      "v_mfma_f32_16x16x32_bf16 v[188:191], v[52:55], v[24:27], v[188:191]\n\t"
      "v_cvt_pk_bf16_f32 v20, v176, v180\n\t"
      "v_cvt_pk_bf16_f32 v21, v177, v181\n\t"
      "v_mfma_f32_16x16x32_bf16 v[184:187], v[48:51], v[24:27], v[184:187]\n\t"
      "ds_read_b128 v[24:27], v9 offset:20480\n\t"
      "v_cvt_pk_bf16_f32 v22, v178, v182\n\t"
      "v_mfma_f32_16x16x32_bf16 v[184:187], v[40:43], v[120:123], v[184:187]\n\t"
      "ds_read_b128 v[120:123], v8 offset:22528\n\t"
      "v_mfma_f32_16x16x32_bf16 v[192:195], v[40:43], v[112:115], v[192:195]\n\t"
      "ds_write_b64 v12, v[20:21] offset:20480\n\t"
      "v_mfma_f32_16x16x32_bf16 v[192:195], v[48:51], v[32:35], v[192:195]\n\t"
      "v_cvt_pk_bf16_f32 v23, v179, v183\n\t"
      "v_mfma_f32_16x16x32_bf16 v[196:199], v[52:55], v[32:35], v[196:199]\n\t"
      "ds_read_b128 v[32:35], v9 offset:22528\n\t"
      "v_mfma_f32_16x16x32_bf16 v[196:199], v[44:47], v[112:115], v[196:199]\n\t"
      "ds_read_b128 v[112:115], v8 offset:24576\n\t"
      "v_mfma_f32_16x16x32_bf16 v[188:191], v[76:79], v[104:107], v[188:191]\n\t"
      "ds_write_b64 v12, v[22:23] offset:20488\n\t"
      "s_waitcnt lgkmcnt(5)\n\t"
      "v_mfma_f32_16x16x32_bf16 v[188:191], v[84:87], v[132:135], v[188:191]\n\t"
      "v_mfma_f32_16x16x32_bf16 v[184:187], v[80:83], v[132:135], v[184:187]\n\t"
      "ds_read_b128 v[132:135], v9 offset:24576\n\t"
      "v_mfma_f32_16x16x32_bf16 v[184:187], v[72:75], v[104:107], v[184:187]\n\t"
      "ds_read_b128 v[104:107], v8 offset:26624\n\t"
      "v_mfma_f32_16x16x32_bf16 v[192:195], v[72:75], v[28:31], v[192:195]\n\t"
      "v_mfma_f32_16x16x32_bf16 v[192:195], v[80:83], v[124:127], v[192:195]\n\t"
      "v_mfma_f32_16x16x32_bf16 v[196:199], v[84:87], v[124:127], v[196:199]\n\t"
      "ds_read_b128 v[124:127], v9 offset:26624\n\t"
      "v_mfma_f32_16x16x32_bf16 v[196:199], v[76:79], v[28:31], v[196:199]\n\t"
      "ds_read_b128 v[28:31], v8 offset:28672\n\t"
      "v_mfma_f32_16x16x32_bf16 v[204:207], v[60:63], v[36:39], v[204:207]\n\t"
      "s_waitcnt lgkmcnt(4)\n\t"
      "s_barrier\n\t"
      "v_mfma_f32_16x16x32_bf16 v[204:207], v[68:71], v[116:119], v[204:207]\n\t"
      "v_mfma_f32_16x16x32_bf16 v[200:203], v[64:67], v[116:119], v[200:203]\n\t"
      "ds_read_b128 v[116:119], v9 offset:28672\n\t"
      "v_cvt_pk_bf16_f32 v16, v184, v188\n\t"
      "v_mfma_f32_16x16x32_bf16 v[200:203], v[56:59], v[36:39], v[200:203]\n\t"
      "ds_read_b128 v[36:39], v8 offset:30720\n\t"
      "v_cvt_pk_bf16_f32 v17, v185, v189\n\t"
      "v_mfma_f32_16x16x32_bf16 v[208:211], v[56:59], v[100:103], v[208:211]\n\t"
      "v_cvt_pk_bf16_f32 v18, v186, v190\n\t"
      "v_mfma_f32_16x16x32_bf16 v[208:211], v[64:67], v[108:111], v[208:211]\n\t"
      "ds_write_b64 v12, v[16:17] offset:24576\n\t"
      "v_mfma_f32_16x16x32_bf16 v[212:215], v[68:71], v[108:111], v[212:215]\n\t"
      "ds_read_b128 v[108:111], v9 offset:30720\n\t"
      "v_cvt_pk_bf16_f32 v19, v187, v191\n\t"
      "v_mfma_f32_16x16x32_bf16 v[212:215], v[60:63], v[100:103], v[212:215]\n\t"
      "ds_read_b128 v[100:103], v8 offset:32768\n\t"
      "v_mfma_f32_16x16x32_bf16 v[204:207], v[44:47], v[128:131], v[204:207]\n\t"
      "ds_write_b64 v12, v[18:19] offset:24584\n\t"
      "v_mfma_f32_16x16x32_bf16 v[204:207], v[52:55], v[24:27], v[204:207]\n\t"
      "v_cvt_pk_bf16_f32 v20, v192, v196\n\t"
      "v_cvt_pk_bf16_f32 v21, v193, v197\n\t"
      "v_mfma_f32_16x16x32_bf16 v[200:203], v[48:51], v[24:27], v[200:203]\n\t"
      "ds_read_b128 v[24:27], v9 offset:32768\n\t"
      "v_cvt_pk_bf16_f32 v22, v194, v198\n\t"
      "v_mfma_f32_16x16x32_bf16 v[200:203], v[40:43], v[128:131], v[200:203]\n\t"
      "ds_read_b128 v[128:131], v8 offset:34816\n\t"
      "v_mfma_f32_16x16x32_bf16 v[208:211], v[40:43], v[120:123], v[208:211]\n\t"
      "ds_write_b64 v12, v[20:21] offset:28672\n\t"
      "v_mfma_f32_16x16x32_bf16 v[208:211], v[48:51], v[32:35], v[208:211]\n\t"
      "v_cvt_pk_bf16_f32 v23, v195, v199\n\t"
      "v_mfma_f32_16x16x32_bf16 v[212:215], v[52:55], v[32:35], v[212:215]\n\t"
      "ds_read_b128 v[32:35], v9 offset:34816\n\t"
      "v_mfma_f32_16x16x32_bf16 v[212:215], v[44:47], v[120:123], v[212:215]\n\t"
      "ds_read_b128 v[120:123], v8 offset:36864\n\t"
      "v_mfma_f32_16x16x32_bf16 v[204:207], v[76:79], v[112:115], v[204:207]\n\t"
      "ds_write_b64 v12, v[22:23] offset:28680\n\t"
      "s_waitcnt lgkmcnt(5)\n\t"
      "v_mfma_f32_16x16x32_bf16 v[204:207], v[84:87], v[132:135], v[204:207]\n\t"
      "v_mfma_f32_16x16x32_bf16 v[200:203], v[80:83], v[132:135], v[200:203]\n\t"
      "ds_read_b128 v[132:135], v9 offset:36864\n\t"
      "v_mfma_f32_16x16x32_bf16 v[200:203], v[72:75], v[112:115], v[200:203]\n\t"
      "ds_read_b128 v[112:115], v8 offset:38912\n\t"
      "v_mfma_f32_16x16x32_bf16 v[208:211], v[72:75], v[104:107], v[208:211]\n\t"
      "v_mfma_f32_16x16x32_bf16 v[208:211], v[80:83], v[124:127], v[208:211]\n\t"
      "v_mfma_f32_16x16x32_bf16 v[212:215], v[84:87], v[124:127], v[212:215]\n\t"
      "ds_read_b128 v[124:127], v9 offset:38912\n\t"
      "v_mfma_f32_16x16x32_bf16 v[212:215], v[76:79], v[104:107], v[212:215]\n\t"
      "ds_read_b128 v[104:107], v8 offset:40960\n\t"
      "v_mfma_f32_16x16x32_bf16 v[220:223], v[60:63], v[28:31], v[220:223]\n\t"
      "s_waitcnt lgkmcnt(4)\n\t"
      "s_barrier\n\t"
      "v_mfma_f32_16x16x32_bf16 v[220:223], v[68:71], v[116:119], v[220:223]\n\t"
      "v_mfma_f32_16x16x32_bf16 v[216:219], v[64:67], v[116:119], v[216:219]\n\t"
      "ds_read_b128 v[116:119], v9 offset:40960\n\t"
      "v_cvt_pk_bf16_f32 v16, v200, v204\n\t"
      "v_mfma_f32_16x16x32_bf16 v[216:219], v[56:59], v[28:31], v[216:219]\n\t"
      "ds_read_b128 v[28:31], v8 offset:43008\n\t"
      "v_cvt_pk_bf16_f32 v17, v201, v205\n\t"
      "v_mfma_f32_16x16x32_bf16 v[224:227], v[56:59], v[36:39], v[224:227]\n\t"
      "v_cvt_pk_bf16_f32 v18, v202, v206\n\t"
      "v_mfma_f32_16x16x32_bf16 v[224:227], v[64:67], v[108:111], v[224:227]\n\t"
      "ds_write_b64 v12, v[16:17] offset:32768\n\t"
      "v_mfma_f32_16x16x32_bf16 v[228:231], v[68:71], v[108:111], v[228:231]\n\t"
      "ds_read_b128 v[108:111], v9 offset:43008\n\t"
      "v_cvt_pk_bf16_f32 v19, v203, v207\n\t"
      "v_mfma_f32_16x16x32_bf16 v[228:231], v[60:63], v[36:39], v[228:231]\n\t"
      "ds_read_b128 v[36:39], v8 offset:45056\n\t"
      "v_mfma_f32_16x16x32_bf16 v[220:223], v[44:47], v[100:103], v[220:223]\n\t"
      "ds_write_b64 v12, v[18:19] offset:32776\n\t"
      "v_mfma_f32_16x16x32_bf16 v[220:223], v[52:55], v[24:27], v[220:223]\n\t"
      "v_cvt_pk_bf16_f32 v20, v208, v212\n\t"
      "v_cvt_pk_bf16_f32 v21, v209, v213\n\t"
      "v_mfma_f32_16x16x32_bf16 v[216:219], v[48:51], v[24:27], v[216:219]\n\t"
      "ds_read_b128 v[24:27], v9 offset:45056\n\t"
      "v_cvt_pk_bf16_f32 v22, v210, v214\n\t"
      "v_mfma_f32_16x16x32_bf16 v[216:219], v[40:43], v[100:103], v[216:219]\n\t"
      "ds_read_b128 v[100:103], v8 offset:49152\n\t"
      "v_mfma_f32_16x16x32_bf16 v[224:227], v[40:43], v[128:131], v[224:227]\n\t"
      "ds_write_b64 v12, v[20:21] offset:36864\n\t"
      "v_mfma_f32_16x16x32_bf16 v[224:227], v[48:51], v[32:35], v[224:227]\n\t"
      "v_cvt_pk_bf16_f32 v23, v211, v215\n\t"
      "v_mfma_f32_16x16x32_bf16 v[228:231], v[52:55], v[32:35], v[228:231]\n\t"
      "ds_read_b128 v[32:35], v9 offset:49152\n\t"
      "v_mfma_f32_16x16x32_bf16 v[228:231], v[44:47], v[128:131], v[228:231]\n\t"
      "ds_read_b128 v[128:131], v8 offset:51200\n\t"
      "v_mfma_f32_16x16x32_bf16 v[220:223], v[76:79], v[120:123], v[220:223]\n\t"
      "ds_write_b64 v12, v[22:23] offset:36872\n\t"
      "s_waitcnt lgkmcnt(5)\n\t"
      "v_mfma_f32_16x16x32_bf16 v[220:223], v[84:87], v[132:135], v[220:223]\n\t"
      "v_mfma_f32_16x16x32_bf16 v[216:219], v[80:83], v[132:135], v[216:219]\n\t"
      "ds_read_b128 v[132:135], v9 offset:51200\n\t"
      "v_mfma_f32_16x16x32_bf16 v[216:219], v[72:75], v[120:123], v[216:219]\n\t"
      "ds_read_b128 v[120:123], v8 offset:53248\n\t"
      "v_mfma_f32_16x16x32_bf16 v[224:227], v[72:75], v[112:115], v[224:227]\n\t"
      "v_mfma_f32_16x16x32_bf16 v[224:227], v[80:83], v[124:127], v[224:227]\n\t"
      "v_mfma_f32_16x16x32_bf16 v[228:231], v[84:87], v[124:127], v[228:231]\n\t"
      "ds_read_b128 v[124:127], v9 offset:53248\n\t"
      "v_mfma_f32_16x16x32_bf16 v[228:231], v[76:79], v[112:115], v[228:231]\n\t"
      "ds_read_b128 v[112:115], v8 offset:57344\n\t"
      "v_mfma_f32_16x16x32_bf16 v[236:239], v[60:63], v[104:107], v[236:239]\n\t"
      "s_waitcnt lgkmcnt(4)\n\t"
      "s_barrier\n\t"
      "v_mfma_f32_16x16x32_bf16 v[236:239], v[68:71], v[116:119], v[236:239]\n\t"
      "v_mfma_f32_16x16x32_bf16 v[232:235], v[64:67], v[116:119], v[232:235]\n\t"
      "ds_read_b128 v[116:119], v9 offset:57344\n\t"
      "v_cvt_pk_bf16_f32 v16, v216, v220\n\t"
      "v_mfma_f32_16x16x32_bf16 v[232:235], v[56:59], v[104:107], v[232:235]\n\t"
      "ds_read_b128 v[104:107], v8 offset:59392\n\t"
      "v_cvt_pk_bf16_f32 v17, v217, v221\n\t"
      "v_mfma_f32_16x16x32_bf16 v[240:243], v[56:59], v[28:31], v[240:243]\n\t"
      "v_cvt_pk_bf16_f32 v18, v218, v222\n\t"
      "v_mfma_f32_16x16x32_bf16 v[240:243], v[64:67], v[108:111], v[240:243]\n\t"
      "ds_write_b64 v12, v[16:17] offset:40960\n\t"
      "v_mfma_f32_16x16x32_bf16 v[244:247], v[68:71], v[108:111], v[244:247]\n\t"
      "ds_read_b128 v[108:111], v9 offset:59392\n\t"
      "v_cvt_pk_bf16_f32 v19, v219, v223\n\t"
      "v_mfma_f32_16x16x32_bf16 v[244:247], v[60:63], v[28:31], v[244:247]\n\t"
      "ds_read_b128 v[28:31], v8 offset:61440\n\t"
      "v_mfma_f32_16x16x32_bf16 v[252:255], v[60:63], v[36:39], v[252:255]\n\t"
      "ds_write_b64 v12, v[18:19] offset:40968\n\t"
      "v_mfma_f32_16x16x32_bf16 v[252:255], v[68:71], v[24:27], v[252:255]\n\t"
      "v_cvt_pk_bf16_f32 v20, v224, v228\n\t"
      "v_cvt_pk_bf16_f32 v21, v225, v229\n\t"
      "v_mfma_f32_16x16x32_bf16 v[248:251], v[64:67], v[24:27], v[248:251]\n\t"
      "ds_read_b128 v[24:27], v9 offset:61440\n\t"
      "v_cvt_pk_bf16_f32 v22, v226, v230\n\t"
      "LH15x5_%=:\n\t"
      "s_cbranch_execz LHRET_%=\n\t"
      "v_mfma_f32_16x16x32_bf16 v[248:251], v[56:59], v[36:39], v[248:251]\n\t"
      "ds_write_b64 v12, v[20:21] offset:45056\n\t"
      "v_mfma_f32_16x16x32_bf16 v[232:235], v[40:43], v[100:103], v[232:235]\n\t"
      "v_cvt_pk_bf16_f32 v23, v227, v231\n\t"
      "v_mfma_f32_16x16x32_bf16 v[232:235], v[48:51], v[32:35], v[232:235]\n\t"
      "ds_write_b64 v12, v[22:23] offset:45064\n\t"
      "v_mfma_f32_16x16x32_bf16 v[236:239], v[52:55], v[32:35], v[236:239]\n\t"
      "v_mfma_f32_16x16x32_bf16 v[236:239], v[44:47], v[100:103], v[236:239]\n\t"
      "v_mfma_f32_16x16x32_bf16 v[244:247], v[44:47], v[128:131], v[244:247]\n\t"
      "s_waitcnt lgkmcnt(2)\n\t"
      "v_mfma_f32_16x16x32_bf16 v[244:247], v[52:55], v[132:135], v[244:247]\n\t"
      "v_mfma_f32_16x16x32_bf16 v[240:243], v[48:51], v[132:135], v[240:243]\n\t"
      "v_mfma_f32_16x16x32_bf16 v[240:243], v[40:43], v[128:131], v[240:243]\n\t"
      "s_waitcnt lgkmcnt(0)\n\t"
      "s_barrier\n\t"
      "v_mfma_f32_16x16x32_bf16 v[248:251], v[40:43], v[120:123], v[248:251]\n\t"
      "v_mfma_f32_16x16x32_bf16 v[248:251], v[48:51], v[124:127], v[248:251]\n\t"
      "v_mfma_f32_16x16x32_bf16 v[252:255], v[52:55], v[124:127], v[252:255]\n\t"
      "v_mfma_f32_16x16x32_bf16 v[252:255], v[44:47], v[120:123], v[252:255]\n\t"
      "v_mfma_f32_16x16x32_bf16 v[236:239], v[76:79], v[112:115], v[236:239]\n\t"
      "v_mfma_f32_16x16x32_bf16 v[236:239], v[84:87], v[116:119], v[236:239]\n\t"
      "v_mfma_f32_16x16x32_bf16 v[232:235], v[80:83], v[116:119], v[232:235]\n\t"
      "v_mfma_f32_16x16x32_bf16 v[232:235], v[72:75], v[112:115], v[232:235]\n\t"
      "v_mfma_f32_16x16x32_bf16 v[240:243], v[72:75], v[104:107], v[240:243]\n\t"
      "v_mfma_f32_16x16x32_bf16 v[240:243], v[80:83], v[108:111], v[240:243]\n\t"
      "v_mfma_f32_16x16x32_bf16 v[244:247], v[84:87], v[108:111], v[244:247]\n\t"
      "v_mfma_f32_16x16x32_bf16 v[244:247], v[76:79], v[104:107], v[244:247]\n\t"
      "v_mfma_f32_16x16x32_bf16 v[252:255], v[76:79], v[28:31], v[252:255]\n\t"
      "v_mfma_f32_16x16x32_bf16 v[252:255], v[84:87], v[24:27], v[252:255]\n\t"
      "v_mfma_f32_16x16x32_bf16 v[248:251], v[80:83], v[24:27], v[248:251]\n\t"
      "s_nop 0\n\t"
      "v_cvt_pk_bf16_f32 v16, v232, v236\n\t"
      "v_cvt_pk_bf16_f32 v17, v233, v237\n\t"
      "v_mfma_f32_16x16x32_bf16 v[248:251], v[72:75], v[28:31], v[248:251]\n\t"
      "v_cvt_pk_bf16_f32 v18, v234, v238\n\t"
      "ds_write_b64 v12, v[16:17] offset:49152\n\t"
      "v_cvt_pk_bf16_f32 v19, v235, v239\n\t"
      "ds_write_b64 v12, v[18:19] offset:49160\n\t"
      "v_cvt_pk_bf16_f32 v20, v240, v244\n\t"
      "v_cvt_pk_bf16_f32 v21, v241, v245\n\t"
      "v_cvt_pk_bf16_f32 v22, v242, v246\n\t"
      "ds_write_b64 v12, v[20:21] offset:53248\n\t"
      "v_cvt_pk_bf16_f32 v23, v243, v247\n\t"
      "ds_write_b64 v12, v[22:23] offset:53256\n\t"
      "v_cvt_pk_bf16_f32 v40, v248, v252\n\t"
      "v_cvt_pk_bf16_f32 v41, v249, v253\n\t"
      "v_cvt_pk_bf16_f32 v42, v250, v254\n\t"
      "v_cvt_pk_bf16_f32 v43, v251, v255\n\t"
      "ds_write_b128 v12, v[40:43] offset:57344\n\t"
      "s_waitcnt lgkmcnt(0)\n\t"
      "s_barrier\n\t"
      "s_endpgm\n\t"
      "LEND_%=:\n\t"
      : "+{v0}"(tid)
      : "{s[0:1]}"(__builtin_amdgcn_kernarg_segment_ptr()), "{s2}"(blockIdx.x),
        "s"((unsigned)(size_t)(__attribute__((address_space(3))) char*)smem)
      : "v1", "v2", "v3", "v4", "v5", "v6", "v7", "v8", "v9", "v10", "v11", "v12", "v13", "v14", "v15", "v16", "v17", "v18", "v19", "v20", "v21", "v22", "v23", "v24", "v25", "v26", "v27", "v28", "v29", "v30", "v31", "v32", "v33", "v34", "v35", "v36", "v37", "v38", "v39", "v40", "v41", "v42", "v43", "v44", "v45", "v46", "v47", "v48", "v49", "v50", "v51", "v52", "v53", "v54", "v55", "v56", "v57", "v58", "v59", "v60", "v61", "v62", "v63", "v64", "v65", "v66", "v67", "v68", "v69", "v70", "v71", "v72", "v73", "v74", "v75", "v76", "v77", "v78", "v79", "v80", "v81", "v82", "v83", "v84", "v85", "v86", "v87", "v88", "v89", "v90", "v91", "v92", "v93", "v94", "v95", "v96", "v97", "v98", "v99", "v100", "v101", "v102", "v103", "v104", "v105", "v106", "v107", "v108", "v109", "v110", "v111", "v112", "v113", "v114", "v115", "v116", "v117", "v118", "v119", "v120", "v121", "v122", "v123", "v124", "v125", "v126", "v127", "v128", "v129", "v130", "v131", "v132", "v133", "v134", "v135", "v136", "v137", "v138", "v139", "v140", "v141", "v142", "v143", "v144", "v145", "v146", "v147", "v148", "v149", "v150", "v151", "v152", "v153", "v154", "v155", "v156", "v157", "v158", "v159", "v160", "v161", "v162", "v163", "v164", "v165", "v166", "v167", "v168", "v169", "v170", "v171", "v172", "v173", "v174", "v175", "v176", "v177", "v178", "v179", "v180", "v181", "v182", "v183", "v184", "v185", "v186", "v187", "v188", "v189", "v190", "v191", "v192", "v193", "v194", "v195", "v196", "v197", "v198", "v199", "v200", "v201", "v202", "v203", "v204", "v205", "v206", "v207", "v208", "v209", "v210", "v211", "v212", "v213", "v214", "v215", "v216", "v217", "v218", "v219", "v220", "v221", "v222", "v223", "v224", "v225", "v226", "v227", "v228", "v229", "v230", "v231", "v232", "v233", "v234", "v235", "v236", "v237", "v238", "v239", "v240", "v241", "v242", "v243", "v244", "v245", "v246", "v247", "v248", "v249", "v250", "v251", "v252", "v253", "v254", "v255", "s16", "s17", "s18", "s19", "s20", "s21", "s22", "s23", "s24", "s25", "s26", "s27", "s28", "s29", "s30", "s31", "s36", "s37", "s38", "s39", "s40", "s41", "s42", "s43", "s44", "s45", "s46", "s47", "s48", "s49", "s50", "s51", "s52", "s53", "s54", "s55", "s56", "s57", "s58", "s59", "s60", "s61", "s62", "s63", "s64", "s65", "s66", "s67", "scc", "vcc", "memory");
}

extern "C" void kernel_launch(void* const* d_in, const int* in_sizes, int n_in, void* d_out, int out_size,
                              void* d_ws, size_t ws_size, hipStream_t stream) {
  gptq_w4a16_kernel<<<256, 512, 0, stream>>>(d_in[0], d_in[1], d_in[2], d_in[3], d_out);
}
```
